```python
import torch

def _softmax(x, axis=-1):
    return torch.softmax(x, dim=axis)
def _gelu(x):
    return torch.nn.functional.gelu(x, approximate="tanh")
def _silu(x):
    return torch.nn.functional.silu(x)
def _sigmoid(x):
    return torch.sigmoid(x)
def _layernorm(x, axis=-1, eps=1e-5):
    m = x.mean(dim=axis, keepdim=True)
    v = x.var(dim=axis, keepdim=True, unbiased=False)
    return (x - m) / torch.sqrt(v + eps)
def _scatter_add(tgt, idx, src, axis=0):
    return tgt.clone().index_add_(axis, idx, src)
def _segment_sum(src, seg, n):
    out = torch.zeros((n,) + src.shape[1:], dtype=src.dtype)
    return out.index_add_(0, seg, src)
def _pad(x, pw):
    flat = tuple(v for pair in reversed(pw) for v in pair)
    return torch.nn.functional.pad(x, flat)

kernel_name = 'sequential_preLN__sliding_block_cached__glu_gated_glu__decode'

def setup_inputs():

    _w = (lambda a: (lambda t: t.to(torch.bfloat16) if t.is_floating_point() else t)(torch.as_tensor(a)))
    def _A_setup():
        B, Sq, D = 1, 1, 640
        Hq, Hkv, Sk, Dh = 32, 32, 8192, 192
        assert Hq % Hkv == 0, (Hq, Hkv)
        return {
            "x": _w((lambda *s: torch.randn(*s, dtype=torch.bfloat16) * 0.05)(B, Sq, D)),
            "k_cache": _w((lambda *s: torch.randn(*s, dtype=torch.bfloat16) * 3.2)(B, Hkv, Sk, Dh)),
            "v_cache": _w((lambda *s: torch.randn(*s, dtype=torch.bfloat16) * 0.4)(B, Hkv, Sk, Dh)),
            "wq": _w((lambda *s: torch.randn(*s, dtype=torch.bfloat16) * 3.2)(D, Hq * Dh) * (D ** -0.5)),
            "wo": _w((lambda *s: torch.randn(*s, dtype=torch.bfloat16) * 0.4)(Hq * Dh, D) * ((Hq * Dh) ** -0.5)),
        }
    _A_in = _A_setup()

    def _B_setup():
        B, S, D, F = 1, 1, 640, 1536
        return {
            "x": _w((lambda *s: torch.randn(*s, dtype=torch.bfloat16) * 0.05)(B, S, D)),
            "w_gate": _w((lambda *s: torch.randn(*s, dtype=torch.bfloat16) * 0.05)(D, F)),
            "w_up": _w((lambda *s: torch.randn(*s, dtype=torch.bfloat16) * 0.05)(D, F)),
            "w_down": _w((lambda *s: torch.randn(*s, dtype=torch.bfloat16) * 0.05)(F, D)),
        }
    _B_in = _B_setup()

    return {
        "A_x": _A_in["x"],
        "A_k_cache": _A_in["k_cache"],
        "A_v_cache": _A_in["v_cache"],
        "A_wq": _A_in["wq"],
        "A_wo": _A_in["wo"],
        "B_x": _B_in["x"],
        "B_w_gate": _B_in["w_gate"],
        "B_w_up": _B_in["w_up"],
        "B_w_down": _B_in["w_down"],
    }

def reference(A_x, A_k_cache, A_v_cache, A_wq, A_wo, B_x, B_w_gate, B_w_up, B_w_down):
    def _A_ref(x, k_cache, v_cache, wq, wo):
        B, Sq, D = x.shape
        Hq, Dh = 32, 192
        Hkv, Sk = k_cache.shape[1], k_cache.shape[2]
        W = min(4096, max(256, Sk // 4))
        rep = Hq // Hkv
        q = (lambda x, *d: x.permute(*d))((x @ wq).reshape(B, Sq, Hq, Dh), 0, 2, 1, 3)
        k = (lambda x, n, axis: torch.repeat_interleave(x, n, dim=axis))(k_cache[:, :, Sk - W:, :], rep, axis=1)
        v = (lambda x, n, axis: torch.repeat_interleave(x, n, dim=axis))(v_cache[:, :, Sk - W:, :], rep, axis=1)
        scores = torch.einsum("bhqd,bhkd->bhqk", q, k) * (Dh ** -0.5)
        p = _softmax(scores, axis=-1)
        o = torch.einsum("bhqk,bhkd->bhqd", p, v)
        return (lambda x, *d: x.permute(*d))(o, 0, 2, 1, 3).reshape(B, Sq, Hq * Dh) @ wo

    def _B_ref(x, w_gate, w_up, w_down):
        g = torch.einsum("bsd,df->bsf", x, w_gate)
        u = torch.einsum("bsd,df->bsf", x, w_up)
        h = _sigmoid(g) * u
        return torch.einsum("bsf,fd->bsd", h, w_down)

    x = A_x
    h = x + _A_ref(_layernorm(x, axis=-1), A_k_cache, A_v_cache, A_wq, A_wo)
    h2 = h + _B_ref(_layernorm(h, axis=-1), B_w_gate, B_w_up, B_w_down)
    return h2
```

```cpp
#include <hip/hip_runtime.h>
#include <hip/hip_bf16.h>
#include <stdint.h>

#define MIMIC 0

typedef unsigned short u16;
typedef unsigned v4u __attribute__((ext_vector_type(4)));

constexpr int D = 640, H = 32, SK = 8192, DH = 192, W = 2048, F = 1536;
constexpr int NS = 8;
constexpr int KPS = W / NS;

__device__ __forceinline__ float bf2f(u16 b) { return __uint_as_float(((unsigned)b) << 16); }
__device__ __forceinline__ float rbf(float f) {
    return f;
}
__device__ __forceinline__ u16 f2bf(float f) {
    unsigned u = __float_as_uint(f);
    u += 0x7FFFu + ((u >> 16) & 1u);
    return (u16)(u >> 16);
}
__device__ __forceinline__ float wave_sum(float v) {
    for (int o = 32; o > 0; o >>= 1) v += __shfl_xor(v, o);
    return v;
}
__device__ __forceinline__ float wave_max(float v) {
    for (int o = 32; o > 0; o >>= 1) v = fmaxf(v, __shfl_xor(v, o));
    return v;
}

__device__ void block_ln(float* xs, float* red) {
    int t = threadIdx.x;
    float s = 0.f;
    for (int i = t; i < D; i += 256) s += xs[i];
    s = wave_sum(s);
    if ((t & 63) == 0) red[t >> 6] = s;
    __syncthreads();
    float mean = (red[0] + red[1] + red[2] + red[3]) * (1.0f / D);
    __syncthreads();
    float v = 0.f;
    for (int i = t; i < D; i += 256) { float d = xs[i] - mean; v += d * d; }
    v = wave_sum(v);
    if ((t & 63) == 0) red[t >> 6] = v;
    __syncthreads();
    float var = (red[0] + red[1] + red[2] + red[3]) * (1.0f / D);
    float mb = rbf(mean), vb = rbf(var);
    float den = rbf(sqrtf(rbf(vb + 1e-5f)));
    __syncthreads();
    for (int i = t; i < D; i += 256) xs[i] = rbf(rbf(xs[i] - mb) / den);
    __syncthreads();
}

__global__ void __launch_bounds__(256) k_q(const u16* x, const u16* wq, float* q) {
    __shared__ float xs[D];
    __shared__ float red[4];
    __shared__ float part[32][64];
    int t = threadIdx.x;
    for (int i = t; i < D; i += 256) xs[i] = bf2f(x[i]);
    __syncthreads();
    block_ln(xs, red);
    int c0 = blockIdx.x * 64 + (t & 7) * 8;
    int rg = t >> 3;
    float acc[8] = {0};
    for (int r = rg; r < D; r += 32) {
        v4u w = *(const v4u*)(wq + (size_t)r * (H * DH) + c0);
        float xv = xs[r];
        for (int k = 0; k < 4; k++) {
            acc[2 * k] += xv * __uint_as_float(w[k] << 16);
            acc[2 * k + 1] += xv * __uint_as_float(w[k] & 0xFFFF0000u);
        }
    }
    for (int k = 0; k < 8; k++) part[rg][(t & 7) * 8 + k] = acc[k];
    __syncthreads();
    if (t < 64) {
        float s = 0.f;
        for (int g = 0; g < 32; g++) s += part[g][t];
        q[blockIdx.x * 64 + t] = rbf(s);
    }
}

__global__ void __launch_bounds__(256) k_scores(const float* q, const u16* kc, float* sc) {
    __shared__ float qs[DH];
    int t = threadIdx.x, s = blockIdx.x, h = blockIdx.y;
    if (t < DH) qs[t] = q[h * DH + t];
    __syncthreads();
    int wave = t >> 6, lane = t & 63;
    for (int kk = 0; kk < 64; kk += 2) {
        int key = s * KPS + wave * 64 + kk + (lane >> 5);
        int l = lane & 31;
        float a = 0.f;
        if (l < 24) {
            v4u w = *(const v4u*)(kc + ((size_t)h * SK + (SK - W) + key) * DH + l * 8);
            for (int k = 0; k < 4; k++) {
                a += qs[l * 8 + 2 * k] * __uint_as_float(w[k] << 16);
                a += qs[l * 8 + 2 * k + 1] * __uint_as_float(w[k] & 0xFFFF0000u);
            }
        }
        for (int o = 16; o > 0; o >>= 1) a += __shfl_xor(a, o);
        if (l == 0) sc[h * W + key] = rbf(rbf(a) * 0.07216878364870322f);
    }
}

__global__ void __launch_bounds__(256) k_pv(const float* sc, const u16* vc, float* opart) {
    __shared__ float red[4];
    __shared__ float ps[KPS];
    __shared__ float part[8][DH];
    int t = threadIdx.x, s = blockIdx.x, h = blockIdx.y;
    float m = -INFINITY;
    for (int i = t; i < W; i += 256) m = fmaxf(m, sc[h * W + i]);
    m = wave_max(m);
    if ((t & 63) == 0) red[t >> 6] = m;
    __syncthreads();
    m = fmaxf(fmaxf(red[0], red[1]), fmaxf(red[2], red[3]));
    __syncthreads();
    float l = 0.f;
    for (int i = t; i < W; i += 256) l += __expf(sc[h * W + i] - m);
    l = wave_sum(l);
    if ((t & 63) == 0) red[t >> 6] = l;
    __syncthreads();
    l = red[0] + red[1] + red[2] + red[3];
    ps[t] = rbf(__expf(sc[h * W + s * KPS + t] - m) / l);
    __syncthreads();
    int g = t >> 5, ln = t & 31;
    float acc[8] = {0};
    if (ln < 24) {
        for (int kk = g; kk < KPS; kk += 8) {
            v4u w = *(const v4u*)(vc + ((size_t)h * SK + (SK - W) + s * KPS + kk) * DH + ln * 8);
            float p = ps[kk];
            for (int k = 0; k < 4; k++) {
                acc[2 * k] += p * __uint_as_float(w[k] << 16);
                acc[2 * k + 1] += p * __uint_as_float(w[k] & 0xFFFF0000u);
            }
        }
        for (int k = 0; k < 8; k++) part[g][ln * 8 + k] = acc[k];
    }
    __syncthreads();
    if (t < DH) {
        float a = 0.f;
        for (int i = 0; i < 8; i++) a += part[i][t];
        opart[(h * NS + s) * DH + t] = a;
    }
}

__global__ void __launch_bounds__(256) k_wo(const float* opart, const u16* wo, float* apart) {
    __shared__ float os[DH];
    __shared__ float part[16][128];
    int t = threadIdx.x, h = blockIdx.y, j = blockIdx.x;
    if (t < DH) {
        float a = 0.f;
        for (int s = 0; s < NS; s++) a += opart[(h * NS + s) * DH + t];
        os[t] = rbf(a);
    }
    __syncthreads();
    int c = (t & 15) * 8, rg = t >> 4;
    float acc[8] = {0};
    for (int r = rg; r < DH; r += 16) {
        v4u w = *(const v4u*)(wo + (size_t)(h * DH + r) * D + j * 128 + c);
        float xv = os[r];
        for (int k = 0; k < 4; k++) {
            acc[2 * k] += xv * __uint_as_float(w[k] << 16);
            acc[2 * k + 1] += xv * __uint_as_float(w[k] & 0xFFFF0000u);
        }
    }
    for (int k = 0; k < 8; k++) part[rg][c + k] = acc[k];
    __syncthreads();
    if (t < 128) {
        float a = 0.f;
        for (int g = 0; g < 16; g++) a += part[g][t];
        apart[h * D + j * 128 + t] = a;
    }
}

__global__ void __launch_bounds__(256) k_ffn1(const u16* x, const float* apart, const u16* wg, const u16* wu, float* hbuf, float* hh) {
    __shared__ float xs[D];
    __shared__ float red[4];
    __shared__ float pg[32][64], pu[32][64];
    int t = threadIdx.x;
    for (int i = t; i < D; i += 256) {
        float a = 0.f;
        for (int h = 0; h < H; h++) a += apart[h * D + i];
        float hv = rbf(bf2f(x[i]) + rbf(a));
        xs[i] = hv;
        if (blockIdx.x == 0) hbuf[i] = hv;
    }
    __syncthreads();
    block_ln(xs, red);
    int c0 = blockIdx.x * 64 + (t & 7) * 8;
    int rg = t >> 3;
    float ag[8] = {0}, au[8] = {0};
    for (int r = rg; r < D; r += 32) {
        v4u w = *(const v4u*)(wg + (size_t)r * F + c0);
        v4u w2 = *(const v4u*)(wu + (size_t)r * F + c0);
        float xv = xs[r];
        for (int k = 0; k < 4; k++) {
            ag[2 * k] += xv * __uint_as_float(w[k] << 16);
            ag[2 * k + 1] += xv * __uint_as_float(w[k] & 0xFFFF0000u);
            au[2 * k] += xv * __uint_as_float(w2[k] << 16);
            au[2 * k + 1] += xv * __uint_as_float(w2[k] & 0xFFFF0000u);
        }
    }
    for (int k = 0; k < 8; k++) { pg[rg][(t & 7) * 8 + k] = ag[k]; pu[rg][(t & 7) * 8 + k] = au[k]; }
    __syncthreads();
    if (t < 64) {
        float g = 0.f, u = 0.f;
        for (int i = 0; i < 32; i++) { g += pg[i][t]; u += pu[i][t]; }
        g = rbf(g); u = rbf(u);
        float sg = rbf(1.0f / (1.0f + __expf(-g)));
        hh[blockIdx.x * 64 + t] = rbf(sg * u);
    }
}

__global__ void __launch_bounds__(256) k_down(const float* hbuf, const float* hh, const u16* wd, u16* out) {
    __shared__ float xs[F];
    __shared__ float part[32][64];
    int t = threadIdx.x;
    for (int i = t; i < F; i += 256) xs[i] = hh[i];
    __syncthreads();
    int c0 = blockIdx.x * 64 + (t & 7) * 8;
    int rg = t >> 3;
    float acc[8] = {0};
    for (int r = rg; r < F; r += 32) {
        v4u w = *(const v4u*)(wd + (size_t)r * D + c0);
        float xv = xs[r];
        for (int k = 0; k < 4; k++) {
            acc[2 * k] += xv * __uint_as_float(w[k] << 16);
            acc[2 * k + 1] += xv * __uint_as_float(w[k] & 0xFFFF0000u);
        }
    }
    for (int k = 0; k < 8; k++) part[rg][(t & 7) * 8 + k] = acc[k];
    __syncthreads();
    if (t < 64) {
        float s = 0.f;
        for (int g = 0; g < 32; g++) s += part[g][t];
        int c = blockIdx.x * 64 + t;
        out[c] = f2bf(hbuf[c] + rbf(s));
    }
}

extern "C" void kernel_launch(void* const* d_in, const int* in_sizes, int n_in,
                              void* d_out, int out_size, void* d_ws, size_t ws_size,
                              hipStream_t stream) {
    const u16* x = (const u16*)d_in[0];
    const u16* kc = (const u16*)d_in[1];
    const u16* vc = (const u16*)d_in[2];
    const u16* wq = (const u16*)d_in[3];
    const u16* wo = (const u16*)d_in[4];
    const u16* wg = (const u16*)d_in[6];
    const u16* wu = (const u16*)d_in[7];
    const u16* wd = (const u16*)d_in[8];
    float* ws = (float*)d_ws;
    float* q = ws;
    float* sc = q + H * DH;
    float* opart = sc + H * W;
    float* apart = opart + H * NS * DH;
    float* hbuf = apart + H * D;
    float* hh = hbuf + D;
    k_q<<<96, 256, 0, stream>>>(x, wq, q);
    k_scores<<<dim3(NS, H), 256, 0, stream>>>(q, kc, sc);
    k_pv<<<dim3(NS, H), 256, 0, stream>>>(sc, vc, opart);
    k_wo<<<dim3(5, H), 256, 0, stream>>>(opart, wo, apart);
    k_ffn1<<<24, 256, 0, stream>>>(x, apart, wg, wu, hbuf, hh);
    k_down<<<10, 256, 0, stream>>>(hbuf, hh, wd, (u16*)d_out);
}
```

```cpp
#include <hip/hip_runtime.h>
#include <stdint.h>

typedef unsigned short u16;
typedef unsigned long long u64;
typedef unsigned v4u __attribute__((ext_vector_type(4)));
typedef unsigned v2u __attribute__((ext_vector_type(2)));
typedef float f32x4 __attribute__((ext_vector_type(4)));
typedef short s16x4 __attribute__((ext_vector_type(4)));
typedef __bf16 bf16x8 __attribute__((ext_vector_type(8)));
typedef __bf16 bf16x2 __attribute__((ext_vector_type(2)));
#define GAS __attribute__((address_space(1)))
#define LAS __attribute__((address_space(3)))
typedef GAS u64 gu64;
typedef GAS unsigned gu32;
typedef __amdgpu_buffer_rsrc_t rsrc_t;
#define RLX_AGENT __ATOMIC_RELAXED, __HIP_MEMORY_SCOPE_AGENT

constexpr int D = 640, H = 32, SK = 8192, DH = 192, W = 2048, F = 1536;
#define KTHR 6

constexpr unsigned WS_EPOCH = 0;
constexpr unsigned WS_QP = 4096;
constexpr unsigned WS_OP = WS_QP + 32 * 8 * 192 * 8;
constexpr unsigned WS_ST = WS_OP + 32 * 8 * 192 * 8;
constexpr unsigned WS_AP = WS_ST + 32 * 16 * 8;
constexpr unsigned WS_HB = WS_AP + 40 * 512 * 8;
constexpr unsigned WS_HH = WS_HB + 4096;
constexpr unsigned WS_END = WS_HH + 768 * 8;
#define OP_NATURAL 1

constexpr unsigned L_W = 0;
constexpr unsigned L_V = 53248;
constexpr unsigned L_Q = L_V + 98304;
constexpr unsigned L_P = L_Q + 384;
constexpr unsigned L_O = L_P + 512;
constexpr unsigned L_LN = L_O + 384;
constexpr unsigned L_F1P = L_LN + 1280;
constexpr unsigned L_CS = L_F1P + 1024;
constexpr unsigned L_MX = L_CS + 1024;
constexpr unsigned L_WGT = L_MX + 32;
constexpr unsigned L_CNT = L_WGT + 32;
constexpr unsigned L_TAG = L_CNT + 64;
constexpr unsigned L_SC = L_TAG + 16;
constexpr unsigned L_STAMP = L_SC + 3072;
constexpr unsigned L_END = L_STAMP + 256;
static_assert(L_END <= 163840, "LDS");
enum { C_X = 0, C_KI, C_VI, C_Q, C_MAX, C_P, C_W, C_V, C_PVA, C_O, C_WO, C_F1, C_CS, C_H, C_PUB };

constexpr unsigned SPIN_LDS = 1u << 20;
constexpr unsigned SPIN_GLB = 1u << 15;

__device__ __forceinline__ unsigned pk2(float lo, float hi) {
    const bf16x2 r = {(__bf16)lo, (__bf16)hi};
    return __builtin_bit_cast(unsigned, r);
}
__device__ __forceinline__ unsigned bfbits(float f) { return (unsigned)__builtin_bit_cast(u16, (__bf16)f); }
__device__ __forceinline__ float rbf(float f) { return __uint_as_float(bfbits(f) << 16); }
__device__ __forceinline__ float lo16(unsigned w) { return __uint_as_float(w << 16); }
__device__ __forceinline__ float hi16(unsigned w) { return __uint_as_float(w & 0xFFFF0000u); }
__device__ __forceinline__ float dot2(unsigned a, unsigned b, float c) {
    return __builtin_amdgcn_fdot2_f32_bf16(__builtin_bit_cast(bf16x2, a), __builtin_bit_cast(bf16x2, b), c, false);
}
template <int CTRL> __device__ __forceinline__ float dpp(float x) {
    return __builtin_bit_cast(float, __builtin_amdgcn_mov_dpp(__builtin_bit_cast(int, x), CTRL, 0xf, 0xf, true));
}
constexpr int XOR1 = 0xB1, XOR2 = 0x4E, XOR7 = 0x141, XOR8 = 0x128;
__device__ __forceinline__ float sw16_sum(float x) {
    auto s = __builtin_amdgcn_permlane16_swap(__float_as_uint(x), __float_as_uint(x), false, false);
    return __uint_as_float(s[0]) + __uint_as_float(s[1]);
}
__device__ __forceinline__ float sw32_sum(float x) {
    auto s = __builtin_amdgcn_permlane32_swap(__float_as_uint(x), __float_as_uint(x), false, false);
    return __uint_as_float(s[0]) + __uint_as_float(s[1]);
}
__device__ __forceinline__ float sw16_max(float x) {
    auto s = __builtin_amdgcn_permlane16_swap(__float_as_uint(x), __float_as_uint(x), false, false);
    return fmaxf(__uint_as_float(s[0]), __uint_as_float(s[1]));
}
__device__ __forceinline__ float sw32_max(float x) {
    auto s = __builtin_amdgcn_permlane32_swap(__float_as_uint(x), __float_as_uint(x), false, false);
    return fmaxf(__uint_as_float(s[0]), __uint_as_float(s[1]));
}
__device__ __forceinline__ float wave_sum(float x) {
    x += dpp<XOR1>(x); x += dpp<XOR2>(x); x += dpp<XOR7>(x); x += dpp<XOR8>(x);
    return sw32_sum(sw16_sum(x));
}
__device__ __forceinline__ float wave_max(float x) {
    x = fmaxf(x, dpp<XOR1>(x)); x = fmaxf(x, dpp<XOR2>(x)); x = fmaxf(x, dpp<XOR7>(x)); x = fmaxf(x, dpp<XOR8>(x));
    return sw32_max(sw16_max(x));
}
__device__ __forceinline__ rsrc_t mkrs(const void* p, unsigned bytes) { return __builtin_amdgcn_make_buffer_rsrc((void*)p, 0, bytes, 0x00020000); }
__device__ __forceinline__ void dma16(rsrc_t rs, unsigned voff, unsigned soff, unsigned lds_dst) {
    unsigned keep;
    asm volatile("s_mov_b32 %0, m0\n\ts_mov_b32 m0, %1\n\ts_nop 0\n\tbuffer_load_dwordx4 %2, %3, %4 offen nt lds\n\ts_mov_b32 m0, %0"
                 : "=&s"(keep) : "s"(lds_dst), "v"(voff), "s"(rs), "s"(soff) : "memory");
}
template <int N> __device__ __forceinline__ void waitvm() { asm volatile("s_waitcnt vmcnt(%0)" :: "n"(N) : "memory"); }
#define LGKM_DRAIN() asm volatile("s_waitcnt lgkmcnt(0)" ::: "memory")
#define MEMBAR() asm volatile("" ::: "memory")
#define BAR() do { __builtin_amdgcn_sched_barrier(0); asm volatile("s_barrier" ::: "memory"); __builtin_amdgcn_sched_barrier(0); } while (0)

__device__ __forceinline__ void lds_signal(int c) {
    u64 keep;
    const unsigned addr = L_CNT + 4 * c, one = 1u;
    asm volatile("s_mov_b64 %0, exec\n\ts_mov_b64 exec, 1\n\tds_add_u32 %1, %2\n\ts_mov_b64 exec, %0" : "=&s"(keep) : "v"(addr), "v"(one) : "memory");
}
template <bool SLEEP> __device__ __forceinline__ void lds_wait(LAS unsigned char* L, int c, unsigned n) {
    volatile LAS unsigned* p = (volatile LAS unsigned*)(L + L_CNT) + c;
#pragma unroll 1
    for (unsigned s = 0; s < SPIN_LDS; ++s) {
        if ((unsigned)__builtin_amdgcn_readfirstlane(*p) >= n) break;
        if (SLEEP) __builtin_amdgcn_s_sleep(1);
    }
    MEMBAR();
}
__device__ __forceinline__ void put_granule(unsigned char* ws, unsigned off, unsigned tag, unsigned val) {
    __hip_atomic_store((gu64*)(ws + off), ((u64)tag << 32) | val, RLX_AGENT);
}
template <int N> __device__ __forceinline__ void get_granules(unsigned char* ws, unsigned off, unsigned stride, unsigned tag, unsigned (&v)[N]) {
#pragma unroll 1
    for (unsigned s = 0; s < SPIN_GLB; ++s) {
        bool ok = true;
#pragma unroll
        for (int k = 0; k < N; ++k) {
            const u64 x = __hip_atomic_load((gu64*)(ws + off + k * stride), RLX_AGENT);
            v[k] = (unsigned)x; ok &= (unsigned)(x >> 32) == tag;
        }
        if (__all(ok)) break;
    }
    MEMBAR();
}
template <int N> __device__ __forceinline__ void get_granule_pairs(rsrc_t rs, unsigned off, unsigned stride, unsigned tag, unsigned (&a)[N], unsigned (&b)[N]) {
#pragma unroll 1
    for (unsigned s = 0; s < SPIN_GLB; ++s) {
        MEMBAR();
        bool ok = true;
#pragma unroll
        for (int k = 0; k < N; ++k) {
            const v4u x = __builtin_amdgcn_raw_buffer_load_b128(rs, off + k * stride, 0, 16);
            a[k] = x[0]; b[k] = x[2]; ok &= (x[1] == tag) & (x[3] == tag);
        }
        if (__all(ok)) break;
    }
    MEMBAR();
}
template <int NS> __device__ __forceinline__ void get_afrags(rsrc_t rs, unsigned off, unsigned tag, v4u (&af)[NS]) {
#pragma unroll 1
    for (unsigned sp = 0; sp < SPIN_GLB; ++sp) {
        MEMBAR();
        bool ok = true;
#pragma unroll
        for (int s = 0; s < NS; ++s) {
            const v4u x0 = __builtin_amdgcn_raw_buffer_load_b128(rs, off + s * 128, 0, 16);
            const v4u x1 = __builtin_amdgcn_raw_buffer_load_b128(rs, off + s * 128 + 16, 0, 16);
            af[s] = v4u{x0[0], x0[2], x1[0], x1[2]};
            ok &= (x0[1] == tag) & (x0[3] == tag) & (x1[1] == tag) & (x1[3] == tag);
        }
        if (__all(ok)) break;
    }
    MEMBAR();
}
__device__ __forceinline__ s16x4 trd(LAS unsigned char* L, unsigned off) { return __builtin_amdgcn_ds_read_tr16_b64_v4i16((LAS s16x4*)(L + off)); }
__device__ __forceinline__ bf16x8 cat8(s16x4 a, s16x4 b) { return __builtin_bit_cast(bf16x8, __builtin_shufflevector(a, b, 0, 1, 2, 3, 4, 5, 6, 7)); }
__device__ __forceinline__ f32x4 mfma32(bf16x8 a, bf16x8 b, f32x4 c) { return __builtin_amdgcn_mfma_f32_16x16x32_bf16(a, b, c, 0, 0, 0); }
__device__ __forceinline__ bf16x8 asb(v4u a) { return __builtin_bit_cast(bf16x8, a); }

__device__ __forceinline__ void ln_stats(const unsigned (&xp)[5], float& mb, float& rden) {
    const float c = lo16(__builtin_amdgcn_readfirstlane(xp[0]));
    float s = 0.f, s2 = 0.f;
#pragma unroll
    for (int i = 0; i < 5; ++i) {
        const float a = lo16(xp[i]) - c, b = hi16(xp[i]) - c;
        s += a + b; s2 = fmaf(a, a, s2); s2 = fmaf(b, b, s2);
    }
    s = wave_sum(s); s2 = wave_sum(s2);
    const float dm = s * (1.0f / D);
    const float var = fmaxf(s2 * (1.0f / D) - dm * dm, 0.f);
    mb = rbf(c + dm);
    const float den = rbf(__builtin_amdgcn_sqrtf(rbf(rbf(var) + 1e-5f)));
    rden = __builtin_amdgcn_rcpf(den);
}
__device__ __forceinline__ unsigned ln_pair(unsigned w, float mb, float rden) {
    const unsigned d = pk2(lo16(w) - mb, hi16(w) - mb);
    return pk2(lo16(d) * rden, hi16(d) * rden);
}

#define STAMP(i) do { } while (0)

__global__ void __launch_bounds__(576) k_block(const u16* __restrict__ x, const u16* __restrict__ kc, const u16* __restrict__ vc,
                                               const u16* __restrict__ wq, const u16* __restrict__ wo, const u16* __restrict__ wg,
                                               const u16* __restrict__ wu, const u16* __restrict__ wd, u16* __restrict__ out,
                                               unsigned char* ws) {
    extern __shared__ __attribute__((aligned(16))) unsigned char lds_raw[];
    LAS unsigned char* L = (LAS unsigned char*)lds_raw;
    const int b = blockIdx.x, xcd = b & 7, idx = b >> 3;
    const int h = xcd * 4 + (idx >> 3), j = idx & 7;
    const int tid = threadIdx.x, lane = tid & 63, wave = __builtin_amdgcn_readfirstlane(tid >> 6);
    const int g = lane >> 4, tq = (lane & 15) >> 2, tp = lane & 3;
    const bool wocu = j < 5;
    const int oc = (idx >> 3) * 3 + (j - 5);
    const int red = (idx < 5) ? xcd * 5 + idx : -1;
    const int f1 = (!wocu && oc < 6) ? xcd * 6 + oc : -1;
    const int f2 = wocu ? -1 : (oc == 6 || oc == 7) ? xcd * 2 + (oc - 6) : ((oc == 8 || oc == 9) && xcd < 2) ? 16 + xcd * 2 + (oc - 8) : -1;
    const rsrc_t rws = mkrs(ws, WS_END);
    unsigned tag = 0;

    if (wave == 0) {
        __builtin_amdgcn_s_setprio(3);
        unsigned xp[5];
        tag = __hip_atomic_load((gu32*)(ws + WS_EPOCH), RLX_AGENT);
#pragma unroll
        for (int i = 0; i < 5; ++i) xp[i] = ((const unsigned*)x)[lane + 64 * i];
        unsigned xres = ((const unsigned*)x)[8 * (red < 0 ? 0 : red) + (lane & 7)];
        if (lane < 16) ((LAS unsigned*)(L + L_CNT))[lane] = 0u;
        LGKM_DRAIN();
        BAR();
        {
            float mb, rden;
            ln_stats(xp, mb, rden);
#pragma unroll
            for (int i = 0; i < 5; ++i) *(LAS unsigned*)(L + L_LN + (lane + 64 * i) * 4) = ln_pair(xp[i], mb, rden);
        }
        tag = __builtin_amdgcn_readfirstlane(tag) + 1u;
        if (lane == 0) *(LAS unsigned*)(L + L_TAG) = tag;
        lds_signal(C_X);
        STAMP(23);
        asm volatile("" : "+v"(xres));
        __builtin_amdgcn_s_setprio(0);
        const rsrc_t rv = mkrs(vc + ((size_t)h * SK + (SK - W) + j * 256) * DH, 98304);
        unsigned vo[6];
#pragma unroll
        for (int c = 0; c < 6; ++c) {
            const int P = 64 * c + lane, row = P / 24, ch = P - row * 24;
            const int f = ((row >> 1) & 1) | (((row >> 3) & 1) << 1);
            vo[c] = row * 384 + (ch ^ (f << 1)) * 16;
        }
        lds_wait<true>(L, C_KI, 4);
#pragma unroll 1
        for (int s = 0; s < 8; ++s) {
#pragma unroll
            for (int c = 0; c < 12; ++c) dma16(rv, vo[c % 6], (s * 2 + c / 6) * 6144, L_V + (s * 12 + c) * 1024);
            waitvm<48>();
        }
        lds_signal(C_VI);
        STAMP(6);
        __builtin_amdgcn_s_setprio(3);
        waitvm<24>();
        lds_signal(C_V);
        waitvm<12>();
        lds_signal(C_V);
        waitvm<0>();
        lds_signal(C_V);
        STAMP(20);
        if (red >= 0) {
            lds_wait<true>(L, C_O, 4);
            unsigned pa[4], pb[4];
            get_granule_pairs<4>(rws, WS_AP + (red * 512) * 8 + lane * 16, 1024, tag, pa, pb);
            float s0 = (__uint_as_float(pa[0]) + __uint_as_float(pa[1])) + (__uint_as_float(pa[2]) + __uint_as_float(pa[3]));
            float s1 = (__uint_as_float(pb[0]) + __uint_as_float(pb[1])) + (__uint_as_float(pb[2]) + __uint_as_float(pb[3]));
            s0 += dpp<XOR8>(s0); s1 += dpp<XOR8>(s1);
            s0 = sw32_sum(sw16_sum(s0)); s1 = sw32_sum(sw16_sum(s1));
            const unsigned sp = pk2(s0, s1);
            const unsigned hp = pk2(lo16(xres) + lo16(sp), hi16(xres) + hi16(sp));
            if (lane < 8) put_granule(ws, WS_HB + (8 * red + lane) * 8, tag, hp);
            STAMP(11);
        } else if (f1 >= 0) {
            lds_wait<true>(L, C_CS, 8);
            float cs = 0.f;
#pragma unroll
            for (int k = 0; k < 4; ++k) cs += *(LAS float*)(L + L_CS + (k * 64 + lane) * 4);
            asm volatile("" : "+v"(cs));
            STAMP(17);
            unsigned hv[5];
            get_granules<5>(ws, WS_HB + lane * 8, 512, tag, hv);
            STAMP(12);
            float mb, rden;
            ln_stats(hv, mb, rden);
            float mc = mb * cs;
            asm volatile("" : "+v"(mc), "+v"(rden));
            lds_wait<false>(L, C_F1, 8);
            float v = 0.f;
#pragma unroll
            for (int k = 0; k < 4; ++k) v += *(LAS float*)(L + L_F1P + (k * 64 + lane) * 4);
            v = rbf((v - mc) * rden);
            auto s = __builtin_amdgcn_permlane32_swap(__float_as_uint(v), __float_as_uint(v), false, false);
            const float gt = __uint_as_float(s[0]), up = __uint_as_float(s[1]);
            const float sg = rbf(__builtin_amdgcn_rcpf(1.0f + __expf(-gt)));
            const unsigned hb = bfbits(sg * up);
            const unsigned nb = __builtin_amdgcn_mov_dpp(hb, XOR1, 0xf, 0xf, true);
            if (lane < 32 && !(lane & 1)) put_granule(ws, WS_HH + (16 * f1 + (lane >> 1)) * 8, tag, hb | (nb << 16));
            STAMP(13);
        } else if (f2 >= 0) {
            lds_wait<true>(L, C_WO, 1);
            STAMP(18);
            unsigned hres[1];
            get_granules<1>(ws, WS_HB + (16 * f2 + ((lane & 31) >> 1)) * 8, 0, tag, hres);
            lds_signal(C_H);
            STAMP(14);
            lds_wait<false>(L, C_F1, 8);
            float v = 0.f;
#pragma unroll
            for (int k = 0; k < 4; ++k) v += *(LAS float*)(L + L_F1P + ((2 * k + (lane >> 5)) * 32 + (lane & 31)) * 4);
            v = sw32_sum(v);
            const float hv = (lane & 1) ? hi16(hres[0]) : lo16(hres[0]);
            const unsigned ob = bfbits(hv + rbf(v));
            const unsigned nb = __builtin_amdgcn_mov_dpp(ob, XOR1, 0xf, 0xf, true);
            if (lane < 32 && !(lane & 1)) ((unsigned*)out)[16 * f2 + (lane >> 1)] = ob | (nb << 16);
            if (f2 == 0 && lane == 0) __hip_atomic_store((gu32*)(ws + WS_EPOCH), tag, RLX_AGENT);
            STAMP(16);
        }
    } else {
        if (wave < 5) {
            __builtin_amdgcn_s_setprio(3);
            const int a = wave - 1;
            if (a < 3) {
                const rsrc_t rq = mkrs(wq + (size_t)(80 * j) * (H * DH) + h * DH + 64 * a, 79 * 12288 + 128);
                const unsigned f0 = (lane >> 4) & 1;
                const unsigned vo0 = (lane >> 3) * 12288 + ((lane & 7) ^ (f0 << 1)) * 16, vo1 = (lane >> 3) * 12288 + ((lane & 7) ^ ((f0 | 2) << 1)) * 16;
#pragma unroll
                for (int i = 0; i < 10; ++i) dma16(rq, (i & 1) ? vo1 : vo0, i * 98304, L_W + a * 10240 + i * 1024);
                BAR();
                if (a == 0) STAMP(22);
                lds_wait<true>(L, C_X, 1);
                tag = __builtin_amdgcn_readfirstlane(*(LAS unsigned*)(L + L_TAG));
                const bf16x8 xa0 = *(LAS bf16x8*)(L + L_LN + (80 * j + 8 * g) * 2), xa1 = *(LAS bf16x8*)(L + L_LN + (80 * j + 32 + 8 * g) * 2);
                const s16x4 xa2 = *(LAS s16x4*)(L + L_LN + (80 * j + 64 + 4 * g) * 2);
                waitvm<0>();
                if (a == 0) STAMP(1);
                float qv = 0.f;
                const int fa = (tq >> 1) | ((g & 1) << 1), fb = (tq >> 1) | ((g >> 1) << 1);
#pragma unroll
                for (int t = 0; t < 4; ++t) {
                    const unsigned ca = L_W + a * 10240 + (8 * g + tq) * 128 + ((t ^ fa) * 2 + (tp >> 1)) * 16 + (tp & 1) * 8;
                    const unsigned cb = L_W + a * 10240 + (64 + 4 * g + tq) * 128 + ((t ^ fb) * 2 + (tp >> 1)) * 16 + (tp & 1) * 8;
                    const f32x4 z4 = {0.f, 0.f, 0.f, 0.f};
                    f32x4 acc = mfma32(xa0, cat8(trd(L, ca), trd(L, ca + 512)), z4);
                    acc = mfma32(xa1, cat8(trd(L, ca + 4096), trd(L, ca + 4608)), acc);
                    const f32x4 acc2 = __builtin_amdgcn_mfma_f32_16x16x16bf16_1k(xa2, trd(L, cb), z4, 0, 0, 0);
                    if (g == t) qv = acc[0] + acc2[0];
                }
                put_granule(ws, WS_QP + ((h * 8 + j) * 192 + 64 * a + lane) * 8, tag, __float_as_uint(qv));
                if (a == 0) STAMP(2);
                unsigned v[8];
                get_granules<8>(ws, WS_QP + (h * 8 * 192 + 64 * a + lane) * 8, 192 * 8, tag, v);
                float s = 0.f;
#pragma unroll
                for (int k = 0; k < 8; ++k) s += __uint_as_float(v[k]);
                *(LAS u16*)(L + L_Q + (64 * a + lane) * 2) = (u16)bfbits(s);
                lds_signal(C_Q);
                if (a == 0) {
                    STAMP(3);
                    __builtin_amdgcn_s_setprio(0);
                    lds_wait<true>(L, C_Q, 3);
                    lds_wait<true>(L, C_VI, 1);
                    if (wocu) {
                        const rsrc_t ro = mkrs(wo + (size_t)(h * DH) * D + j * 128, 191 * 1280 + 256);
                        const unsigned w0 = g * 1280 + ((lane & 15) ^ (g << 1)) * 16, w1 = g * 1280 + ((lane & 15) ^ ((g | 4) << 1)) * 16;
#pragma unroll 1
                        for (int i = 0; i < 48; i += 4) {
                            dma16(ro, w0, i * 5120, L_W + i * 1024); dma16(ro, w0, (i + 1) * 5120, L_W + (i + 1) * 1024);
                            dma16(ro, w1, (i + 2) * 5120, L_W + (i + 2) * 1024); dma16(ro, w1, (i + 3) * 5120, L_W + (i + 3) * 1024);
                        }
                    } else if (f1 >= 0) {
                        const rsrc_t rg = mkrs(wg + f1 * 32, 639 * 3072 + 64), ru = mkrs(wu + f1 * 32, 639 * 3072 + 64);
                        const unsigned w0 = (lane >> 2) * 3072 + ((lane & 3) ^ ((lane >> 5) << 1)) * 16;
#pragma unroll 1
                        for (int i = 0; i < 40; ++i) dma16(rg, w0, i * 49152, L_W + i * 1024);
#pragma unroll 1
                        for (int i = 0; i < 12; ++i) dma16(ru, w0, i * 49152, L_W + 40960 + i * 1024);
                        lds_wait<true>(L, C_PVA, 4);
#pragma unroll 1
                        for (int i = 12; i < 40; ++i) dma16(ru, w0, i * 49152, L_W + 40960 + i * 1024);
                    } else if (f2 >= 0) {
                        const rsrc_t rd = mkrs(wd + f2 * 32, 1535 * 1280 + 64);
                        const unsigned w0 = (lane >> 2) * 1280 + ((lane & 3) ^ ((lane >> 5) << 1)) * 16;
#pragma unroll 1
                        for (int i = 0; i < 52; ++i) dma16(rd, w0, i * 20480, L_W + i * 1024);
                        lds_wait<true>(L, C_PVA, 4);
#pragma unroll 1
                        for (int i = 52; i < 96; ++i) dma16(rd, w0, i * 20480, L_W + i * 1024);
                    }
                    waitvm<0>();
                    lds_signal(C_WO);
                    __builtin_amdgcn_s_setprio(3);
                    STAMP(9);
                }
            } else {
                BAR();
                lds_wait<true>(L, C_P, 4);
                tag = __builtin_amdgcn_readfirstlane(*(LAS unsigned*)(L + L_TAG));
                const f32x4 mx = *(LAS f32x4*)(L + L_MX), ls = *(LAS f32x4*)(L + L_MX + 16);
                const float m = fmaxf(fmaxf(mx[0], mx[1]), fmaxf(mx[2], mx[3]));
                const float l = (ls[0] + ls[1]) + (ls[2] + ls[3]);
                if (lane < 2) put_granule(ws, WS_ST + ((h * 8 + j) * 2 + lane) * 8, tag, __float_as_uint(lane == 0 ? m : l));
                if (wocu) {
                    unsigned sm[1], sl[1];
                    get_granule_pairs<1>(rws, WS_ST + (h * 8 + (lane & 7)) * 16, 0, tag, sm, sl);
                    const float ms = __uint_as_float(sm[0]);
                    float M = fmaxf(ms, dpp<XOR1>(ms)); M = fmaxf(M, dpp<XOR2>(M)); M = fmaxf(M, dpp<XOR7>(M));
                    const float e = __expf(ms - M);
                    float Ls = e * __uint_as_float(sl[0]);
                    Ls += dpp<XOR1>(Ls); Ls += dpp<XOR2>(Ls); Ls += dpp<XOR7>(Ls);
                    if (lane < 8) *(LAS float*)(L + L_WGT + lane * 4) = e / Ls;
                    lds_signal(C_W);
                    STAMP(19);
                }
            }
            if (wocu) {
                lds_wait<true>(L, C_WO, 1);
                bf16x8 bw[2][6];
                const int f3 = tq | ((g & 1) << 2);
#pragma unroll
                for (int t = 0; t < 2; ++t) {
                    const unsigned cb = L_W + (8 * g + tq) * 256 + (((2 * a + t) ^ f3) * 2 + (tp >> 1)) * 16 + (tp & 1) * 8;
#pragma unroll
                    for (int s = 0; s < 6; ++s) bw[t][s] = cat8(trd(L, cb + s * 8192), trd(L, cb + s * 8192 + 1024));
                }
#pragma unroll
                for (int s = 0; s < 6; ++s) asm volatile("" : "+v"(bw[0][s]), "+v"(bw[1][s]));
                lds_wait<false>(L, C_O, 4);
                f32x4 a0 = {0.f, 0.f, 0.f, 0.f}, a1 = {0.f, 0.f, 0.f, 0.f};
#pragma unroll
                for (int s = 0; s < 6; ++s) {
                    const bf16x8 af = *(LAS bf16x8*)(L + L_O + (32 * s + 8 * g) * 2);
                    a0 = mfma32(af, bw[0][s], a0); a1 = mfma32(af, bw[1][s], a1);
                }
                if (lane < 32) put_granule(ws, WS_AP + (((8 * j + 2 * a + g) * 32 + h) * 16 + (lane & 15)) * 8, tag, __float_as_uint(g ? a1[0] : a0[0]));
                if (a == 0) STAMP(10);
            }
        } else {
            const int kw = wave - 5;
            const rsrc_t rk = mkrs(kc + ((size_t)h * SK + (SK - W) + j * 256) * DH, 98304);
            v4u kr[24];
#pragma unroll
            for (int i = 0; i < 24; ++i) {
                if (i == KTHR) BAR();
                if (i >= KTHR) waitvm<KTHR - 1>();
                kr[i] = __builtin_amdgcn_raw_buffer_load_b128(rk, lane * 16, ((i / 3) * 12 + 3 * kw + (i % 3)) * 1024, 2);
                __builtin_amdgcn_sched_barrier(0);
            }
            lds_signal(C_KI);
            if (kw == 0) STAMP(4);
            __builtin_amdgcn_s_setprio(2);
            lds_wait<true>(L, C_Q, 3);
            tag = __builtin_amdgcn_readfirstlane(*(LAS unsigned*)(L + L_TAG));
            v4u qf[3];
#pragma unroll
            for (int c = 0; c < 3; ++c) qf[c] = *(LAS v4u*)(L + L_Q + ((64 * c + lane) % 24) * 16);
            float sc[24];
#pragma unroll
            for (int i = 0; i < 24; ++i) {
                const v4u q4 = qf[i % 3];
                sc[i] = dot2(kr[i][3], q4[3], dot2(kr[i][2], q4[2], dot2(kr[i][1], q4[1], dot2(kr[i][0], q4[0], 0.f))));
            }
#define TR_STEP(HALF, CTRL, BIT) _Pragma("unroll") for (int i = 0; i < 3 * (HALF); ++i) { \
            const float t1 = sc[i] + dpp<CTRL>(sc[i]), t2 = sc[i + 3 * (HALF)] + dpp<CTRL>(sc[i + 3 * (HALF)]); sc[i] = (lane & (BIT)) ? t2 : t1; }
            TR_STEP(4, XOR7, 4) TR_STEP(2, XOR2, 2) TR_STEP(1, XOR1, 1)
#undef TR_STEP
#pragma unroll
            for (int c = 0; c < 3; ++c) *(LAS float*)(L + L_SC + (kw * 192 + (lane & 7) * 24 + 8 * c + (lane >> 3)) * 4) = sc[c];
            MEMBAR();
            const LAS float* scp = (const LAS float*)(L + L_SC + (kw * 192 + 3 * lane) * 4);
            const float s = rbf(rbf((scp[0] + scp[1]) + scp[2]) * 0.07216878364870322f);
            const float mw = wave_max(s);
            if (lane == 0) *(LAS float*)(L + L_MX + kw * 4) = mw;
            lds_signal(C_MAX);
            lds_wait<false>(L, C_MAX, 4);
            const f32x4 mx = *(LAS f32x4*)(L + L_MX);
            const float m = fmaxf(fmaxf(mx[0], mx[1]), fmaxf(mx[2], mx[3]));
            const float e = __expf(s - m);
            *(LAS u16*)(L + L_P + (32 * (lane >> 3) + 8 * kw + (lane & 7)) * 2) = (u16)bfbits(e);
            const float lw = wave_sum(e);
            if (lane == 0) *(LAS float*)(L + L_MX + 16 + kw * 4) = lw;
            lds_signal(C_P);
            lds_wait<false>(L, C_P, 4);
            if (kw == 0) STAMP(5);
            bf16x8 pf[8];
#pragma unroll
            for (int s8 = 0; s8 < 8; ++s8) pf[s8] = *(LAS bf16x8*)(L + L_P + (32 * s8 + 8 * g) * 2);
            const int fv = (tq >> 1) | ((g & 1) << 1);
            unsigned vb[3];
#pragma unroll
            for (int t = 0; t < 3; ++t) vb[t] = L_V + (8 * g + tq) * 384 + (((3 * kw + t) ^ fv) * 2 + (tp >> 1)) * 16 + (tp & 1) * 8;
            f32x4 oa[3] = {{0.f, 0.f, 0.f, 0.f}, {0.f, 0.f, 0.f, 0.f}, {0.f, 0.f, 0.f, 0.f}};
            lds_wait<true>(L, C_V, 1);
#pragma unroll
            for (int s8 = 0; s8 < 6; ++s8)
#pragma unroll
                for (int t = 0; t < 3; ++t) oa[t] = mfma32(pf[s8], cat8(trd(L, vb[t] + s8 * 12288), trd(L, vb[t] + s8 * 12288 + 1536)), oa[t]);
            lds_signal(C_PVA);
            lds_wait<false>(L, C_V, 2);
#pragma unroll
            for (int t = 0; t < 3; ++t) oa[t] = mfma32(pf[6], cat8(trd(L, vb[t] + 6 * 12288), trd(L, vb[t] + 6 * 12288 + 1536)), oa[t]);
            lds_wait<false>(L, C_V, 3);
#pragma unroll
            for (int t = 0; t < 3; ++t) oa[t] = mfma32(pf[7], cat8(trd(L, vb[t] + 7 * 12288), trd(L, vb[t] + 7 * 12288 + 1536)), oa[t]);
            __builtin_amdgcn_s_setprio(3);
            const float op = g == 0 ? oa[0][0] : g == 1 ? oa[1][0] : oa[2][0];
            const int l48 = lane < 48 ? lane : 47;
            if (lane < 48) put_granule(ws, WS_OP + ((h * 8 + j) * 192 + 48 * kw + lane) * 8, tag, __float_as_uint(op));
            if (kw == 0) STAMP(7);
            if (wocu) {
                lds_wait<false>(L, C_W, 1);
                const f32x4 w0 = *(LAS f32x4*)(L + L_WGT), w1 = *(LAS f32x4*)(L + L_WGT + 16);
                unsigned v[8];
                get_granules<8>(ws, WS_OP + (h * 8 * 192 + 48 * kw + l48) * 8, 192 * 8, tag, v);
                float o = 0.f;
#pragma unroll
                for (int k = 0; k < 4; ++k) o = fmaf(w0[k], __uint_as_float(v[k]), o);
#pragma unroll
                for (int k = 0; k < 4; ++k) o = fmaf(w1[k], __uint_as_float(v[4 + k]), o);
                *(LAS u16*)(L + L_O + (48 * kw + l48) * 2) = (u16)bfbits(o);
                lds_signal(C_O);
                if (kw == 0) STAMP(8);
            }
        }
        const int w8 = wave - 1;
        if (f1 >= 0) {
            const int mat = w8 & 1, kq = w8 >> 1;
            lds_wait<true>(L, C_WO, 1);
            bf16x8 bw[2][5];
#pragma unroll
            for (int t = 0; t < 2; ++t) {
                const unsigned cb = L_W + mat * 40960 + (160 * kq + 8 * g + tq) * 64 + ((t ^ (g & 1)) * 2 + (tp >> 1)) * 16 + (tp & 1) * 8;
#pragma unroll
                for (int s = 0; s < 5; ++s) bw[t][s] = cat8(trd(L, cb + s * 2048), trd(L, cb + s * 2048 + 256));
            }
            {
                const v4u ones = {0x3F803F80u, 0x3F803F80u, 0x3F803F80u, 0x3F803F80u};
                f32x4 c0 = {0.f, 0.f, 0.f, 0.f}, c1 = {0.f, 0.f, 0.f, 0.f};
#pragma unroll
                for (int s = 0; s < 5; ++s) { c0 = mfma32(asb(ones), bw[0][s], c0); c1 = mfma32(asb(ones), bw[1][s], c1); }
                if (lane < 32) *(LAS float*)(L + L_CS + (kq * 64 + mat * 32 + lane) * 4) = g ? c1[0] : c0[0];
                lds_signal(C_CS);
            }
            v4u af[5];
            get_afrags<5>(rws, WS_HB + (80 * kq + 4 * g) * 8, tag, af);
            f32x4 a0 = {0.f, 0.f, 0.f, 0.f}, a1 = {0.f, 0.f, 0.f, 0.f};
#pragma unroll
            for (int s = 0; s < 5; ++s) { a0 = mfma32(asb(af[s]), bw[0][s], a0); a1 = mfma32(asb(af[s]), bw[1][s], a1); }
            if (lane < 32) *(LAS float*)(L + L_F1P + (kq * 64 + mat * 32 + lane) * 4) = g ? a1[0] : a0[0];
            lds_signal(C_F1);
        } else if (f2 >= 0) {
            lds_wait<true>(L, C_WO, 1);
            bf16x8 bw[2][6];
#pragma unroll
            for (int t = 0; t < 2; ++t) {
                const unsigned cb = L_W + (192 * w8 + 8 * g + tq) * 64 + ((t ^ (g & 1)) * 2 + (tp >> 1)) * 16 + (tp & 1) * 8;
#pragma unroll
                for (int s = 0; s < 6; ++s) bw[t][s] = cat8(trd(L, cb + s * 2048), trd(L, cb + s * 2048 + 256));
            }
#pragma unroll
            for (int s = 0; s < 6; ++s) asm volatile("" : "+v"(bw[0][s]), "+v"(bw[1][s]));
            lds_wait<true>(L, C_H, 1);
            v4u af[6];
            get_afrags<6>(rws, WS_HH + (96 * w8 + 4 * g) * 8, tag, af);
            if (w8 == 0) STAMP(15);
            f32x4 a0 = {0.f, 0.f, 0.f, 0.f}, a1 = {0.f, 0.f, 0.f, 0.f};
#pragma unroll
            for (int s = 0; s < 6; ++s) { a0 = mfma32(asb(af[s]), bw[0][s], a0); a1 = mfma32(asb(af[s]), bw[1][s], a1); }
            if (lane < 32) *(LAS float*)(L + L_F1P + (w8 * 32 + lane) * 4) = g ? a1[0] : a0[0];
            lds_signal(C_F1);
        }
    }
}

extern "C" void kernel_launch(void* const* d_in, const int* in_sizes, int n_in, void* d_out, int out_size, void* d_ws, size_t ws_size, hipStream_t stream) {
    (void)hipFuncSetAttribute((const void*)k_block, hipFuncAttributeMaxDynamicSharedMemorySize, L_END);
    k_block<<<256, 576, L_END, stream>>>((const u16*)d_in[0], (const u16*)d_in[1], (const u16*)d_in[2], (const u16*)d_in[3], (const u16*)d_in[4],
                                         (const u16*)d_in[6], (const u16*)d_in[7], (const u16*)d_in[8], (u16*)d_out, (unsigned char*)d_ws);
}
```

```cpp
#include <hip/hip_runtime.h>
#include <stdint.h>

typedef unsigned short u16;
typedef unsigned long long u64;
typedef unsigned v4u __attribute__((ext_vector_type(4)));
typedef unsigned v2u __attribute__((ext_vector_type(2)));
typedef float f32x4 __attribute__((ext_vector_type(4)));
typedef short s16x4 __attribute__((ext_vector_type(4)));
typedef __bf16 bf16x8 __attribute__((ext_vector_type(8)));
typedef __bf16 bf16x2 __attribute__((ext_vector_type(2)));
#define GAS __attribute__((address_space(1)))
#define LAS __attribute__((address_space(3)))
typedef GAS u64 gu64;
typedef GAS unsigned gu32;
typedef __amdgpu_buffer_rsrc_t rsrc_t;
#define RLX_AGENT __ATOMIC_RELAXED, __HIP_MEMORY_SCOPE_AGENT

constexpr int D = 640, H = 32, SK = 8192, DH = 192, W = 2048, F = 1536;

constexpr unsigned WS_EPOCH = 0;
constexpr unsigned WS_QP = 4096;
constexpr unsigned WS_OP = WS_QP + 32 * 3072;
constexpr unsigned WS_ST = WS_OP + 32 * 8 * 192 * 8;
constexpr unsigned WS_AP = WS_ST + 32 * 8 * 128;
constexpr unsigned WS_HB = WS_AP + 40 * 512 * 8;
constexpr unsigned WS_CP = WS_HB + 8 * 4096;
constexpr unsigned WS_GP = WS_CP + 48 * 5120;
constexpr unsigned WS_DP = WS_GP + 48 * 5120;
constexpr unsigned WS_END = WS_DP + 40 * 6144;
constexpr unsigned OOB = 0x7FFFFF00u;

constexpr unsigned L_W = 0;
constexpr unsigned L_V = 49152;
constexpr unsigned L_Q = L_V + 98304;
constexpr unsigned L_P = L_Q + 384;
constexpr unsigned L_O = L_P + 512;
constexpr unsigned L_LN = L_O + 512;
constexpr unsigned L_OWN = L_LN + 1280;
constexpr unsigned L_HH = L_OWN + 1024;
constexpr unsigned L_MX = L_HH + 64;
constexpr unsigned L_WGT = L_MX + 32;
constexpr unsigned L_CNT = L_WGT + 32;
constexpr unsigned L_ST = L_CNT + 64;
constexpr unsigned L_TAG = L_ST + 16;
constexpr unsigned L_STAMP = L_TAG + 16;
constexpr unsigned L_JUNK = L_STAMP + 256;
constexpr unsigned L_END = L_JUNK + 1024;
static_assert(L_END <= 163840, "LDS");
enum { C_X = 0, C_PROG, C_KA, C_KB, C_Q, C_MAX, C_P, C_W, C_OWN, C_O, C_HH, C_WQ, C_QP, C_PVA, C_CS };
constexpr int P_K = 96, P_WO = 240, P_WD = 282;

#define KNOB_DEFAULTS {2, 100, 160, 900, 60, 420, 820, 0, 0, 1220, 1340, 1450, 0, 0, 0, 0}
constexpr int kn_c[16] = KNOB_DEFAULTS;
#define KN(i) kn_c[i]
#define KNARG
#define N1G KN(0)
#define TA0 KN(1)
#define TAQ KN(2)
#define TE KN(3)
#define FDELAY KN(4)
#define TQ KN(5)
#define TS KN(6)
#define KN7 KN(7)
#define TG1 KN(8)
#define TG2 KN(9)
#define TG3 KN(10)
#define TG4 KN(11)
#define DDELAY KN(12)
#define PACE2 KN(13)
#define WMAX 36
#define DEPTH 2

constexpr unsigned SPIN_LDS = 1u << 20;
constexpr unsigned SPIN_GLB = 1u << 15;

__device__ __forceinline__ unsigned pk2(float lo, float hi) {
    const bf16x2 r = {(__bf16)lo, (__bf16)hi};
    return __builtin_bit_cast(unsigned, r);
}
__device__ __forceinline__ unsigned bfbits(float f) { return (unsigned)__builtin_bit_cast(u16, (__bf16)f); }
__device__ __forceinline__ float rbf(float f) { return __uint_as_float(bfbits(f) << 16); }
__device__ __forceinline__ float lo16(unsigned w) { return __uint_as_float(w << 16); }
__device__ __forceinline__ float hi16(unsigned w) { return __uint_as_float(w & 0xFFFF0000u); }
__device__ __forceinline__ float dot2(unsigned a, unsigned b, float c) {
    return __builtin_amdgcn_fdot2_f32_bf16(__builtin_bit_cast(bf16x2, a), __builtin_bit_cast(bf16x2, b), c, false);
}
template <int CTRL> __device__ __forceinline__ float dpp(float x) {
    return __builtin_bit_cast(float, __builtin_amdgcn_mov_dpp(__builtin_bit_cast(int, x), CTRL, 0xf, 0xf, true));
}
constexpr int XOR1 = 0xB1, XOR2 = 0x4E, XOR7 = 0x141, XOR8 = 0x128;
__device__ __forceinline__ float sw16_sum(float x) {
    auto s = __builtin_amdgcn_permlane16_swap(__float_as_uint(x), __float_as_uint(x), false, false);
    return __uint_as_float(s[0]) + __uint_as_float(s[1]);
}
__device__ __forceinline__ float sw32_sum(float x) {
    auto s = __builtin_amdgcn_permlane32_swap(__float_as_uint(x), __float_as_uint(x), false, false);
    return __uint_as_float(s[0]) + __uint_as_float(s[1]);
}
__device__ __forceinline__ float sw16_max(float x) {
    auto s = __builtin_amdgcn_permlane16_swap(__float_as_uint(x), __float_as_uint(x), false, false);
    return fmaxf(__uint_as_float(s[0]), __uint_as_float(s[1]));
}
__device__ __forceinline__ float sw32_max(float x) {
    auto s = __builtin_amdgcn_permlane32_swap(__float_as_uint(x), __float_as_uint(x), false, false);
    return fmaxf(__uint_as_float(s[0]), __uint_as_float(s[1]));
}
__device__ __forceinline__ float wave_sum(float x) {
    x += dpp<XOR1>(x); x += dpp<XOR2>(x); x += dpp<XOR7>(x); x += dpp<XOR8>(x);
    return sw32_sum(sw16_sum(x));
}
__device__ __forceinline__ float wave_max(float x) {
    x = fmaxf(x, dpp<XOR1>(x)); x = fmaxf(x, dpp<XOR2>(x)); x = fmaxf(x, dpp<XOR7>(x)); x = fmaxf(x, dpp<XOR8>(x));
    return sw32_max(sw16_max(x));
}
__device__ __forceinline__ rsrc_t mkrs(const void* p, unsigned bytes) { return __builtin_amdgcn_make_buffer_rsrc((void*)p, 0, bytes, 0x00020000); }

__device__ __forceinline__ void dma16(rsrc_t rs, unsigned voff, unsigned soff, unsigned lds_dst) {
    asm volatile("s_mov_b32 m0, %0\n\ts_nop 0\n\tbuffer_load_dwordx4 %1, %2, %3 offen nt lds"
                 :: "s"(lds_dst), "v"(voff), "s"(rs), "s"(soff) : "memory");
}
template <int N> __device__ __forceinline__ void waitvm() { asm volatile("s_waitcnt vmcnt(%0)" :: "n"(N) : "memory"); }
#define LGKM_DRAIN() asm volatile("s_waitcnt lgkmcnt(0)" ::: "memory")
#define MEMBAR() asm volatile("" ::: "memory")
#define BAR() do { __builtin_amdgcn_sched_barrier(0); asm volatile("s_barrier" ::: "memory"); __builtin_amdgcn_sched_barrier(0); } while (0)

__device__ __forceinline__ void lds_signal(int c) {
    const unsigned addr = L_CNT + 4 * c, one = 1u;
    asm volatile("s_mov_b64 exec, 1\n\tds_add_u32 %0, %1\n\ts_mov_b64 exec, -1" :: "v"(addr), "v"(one) : "memory");
}
__device__ __forceinline__ void lds_set(int c, unsigned val) {
    const unsigned addr = L_CNT + 4 * c;
    asm volatile("s_mov_b64 exec, 1\n\tds_write_b32 %0, %1\n\ts_mov_b64 exec, -1" :: "v"(addr), "v"(val) : "memory");
}
template <bool SLEEP, class F> __device__ __forceinline__ void lds_wait_rd(LAS unsigned char* L, int c, unsigned n, F rd) {
    volatile LAS unsigned* p = (volatile LAS unsigned*)(L + L_CNT) + c;
    unsigned cap = SPIN_LDS;
#pragma unroll 1
    while (true) {
        const unsigned f = *p;
        MEMBAR();
        rd();
        MEMBAR();
        if ((unsigned)__builtin_amdgcn_readfirstlane(f) >= n) break;
        if (SLEEP) __builtin_amdgcn_s_sleep(1);
        MEMBAR();
        if (--cap == 0) break;
    }
    MEMBAR();
}
template <bool SLEEP> __device__ __forceinline__ void lds_wait(LAS unsigned char* L, int c, unsigned n) { lds_wait_rd<SLEEP>(L, c, n, [] {}); }
__device__ __forceinline__ void until(u64 t) { while (__builtin_amdgcn_s_memrealtime() < t) __builtin_amdgcn_s_sleep(2); }
#define PIN(x) asm volatile("" : "+v"(x))
#define SB() __builtin_amdgcn_sched_barrier(0)
__device__ __forceinline__ float fv(unsigned u) { return __uint_as_float(u); }
__device__ __forceinline__ void put1(rsrc_t rs, unsigned off, unsigned tag, unsigned val) { __builtin_amdgcn_raw_buffer_store_b64(v2u{val, tag}, rs, off, 0, 16); }
__device__ __forceinline__ void put2(rsrc_t rs, unsigned off, unsigned tag, float a, float b) {
    __builtin_amdgcn_raw_buffer_store_b128(v4u{__float_as_uint(a), tag, __float_as_uint(b), tag}, rs, off, 0, 16);
}
__device__ __forceinline__ u64 badmask(v2u x, unsigned tag) {
    u64 m;
    asm volatile("v_cmp_ne_u32_e64 %0, %1, %2" : "=s"(m) : "s"(tag), "v"(x[1]));
    return m;
}
__device__ __forceinline__ u64 badmask(v4u x, unsigned tag) {
    u64 m;
    const unsigned t = __builtin_amdgcn_bitop3_b32(x[1], x[3], tag, 0x7E);
    asm volatile("v_cmp_ne_u32_e64 %0, 0, %1" : "=s"(m) : "v"(t));
    return m;
}
__device__ __forceinline__ void gload(v2u& x, u64 m, rsrc_t rs, unsigned voff, unsigned soff) {
    asm volatile("s_mov_b64 exec, %2\n\tbuffer_load_dwordx2 %0, %1, %3, %4 offen sc1\n\ts_mov_b64 exec, -1" : "+v"(x) : "v"(voff), "s"(m), "s"(rs), "s"(soff) : "memory");
}
__device__ __forceinline__ void gload(v4u& x, u64 m, rsrc_t rs, unsigned voff, unsigned soff) {
    asm volatile("s_mov_b64 exec, %2\n\tbuffer_load_dwordx4 %0, %1, %3, %4 offen sc1\n\ts_mov_b64 exec, -1" : "+v"(x) : "v"(voff), "s"(m), "s"(rs), "s"(soff) : "memory");
}
template <int DP, int SLP, int N, class T> __device__ __forceinline__ void gather(rsrc_t rs, unsigned voff, unsigned base, unsigned stride, unsigned tag, T (&x)[N]) {
    u64 m[N];
#pragma unroll
    for (int k = 0; k < N; ++k) { PIN(x[k]); m[k] = badmask(x[k], tag); }
    unsigned cap = SPIN_GLB;
    bool first = true;
#pragma unroll 1
    while (true) {
#pragma unroll
        for (int k = 0; k < N; ++k) gload(x[k], m[k], rs, voff, base + k * stride);
        if (DP > 1 && first) waitvm<0>(); else waitvm<(DP - 1) * N>();
#pragma unroll
        for (int k = 0; k < N; ++k) PIN(x[k]);
        u64 any = 0;
#pragma unroll
        for (int k = 0; k < N; ++k) { m[k] = badmask(x[k], tag); any |= m[k]; }
        if (!any) break;
        if (SLP) __builtin_amdgcn_s_sleep(SLP);
        if (DP > 1 && first) {
            first = false;
#pragma unroll
            for (int k = 0; k < N; ++k) gload(x[k], m[k], rs, voff, base + k * stride);
            __builtin_amdgcn_s_sleep(2);
        }
        MEMBAR();
        if (--cap == 0) break;
    }
    if (DP > 1) {
        waitvm<0>();
#pragma unroll
        for (int k = 0; k < N; ++k) PIN(x[k]);
    }
}
__device__ __forceinline__ s16x4 trd(LAS unsigned char* L, unsigned off) { return __builtin_amdgcn_ds_read_tr16_b64_v4i16((LAS s16x4*)(L + off)); }
__device__ __forceinline__ bf16x8 cat8(s16x4 a, s16x4 b) { return __builtin_bit_cast(bf16x8, __builtin_shufflevector(a, b, 0, 1, 2, 3, 4, 5, 6, 7)); }
__device__ __forceinline__ f32x4 mfma32(bf16x8 a, bf16x8 b, f32x4 c) { return __builtin_amdgcn_mfma_f32_16x16x32_bf16(a, b, c, 0, 0, 0); }
__device__ __forceinline__ bf16x8 asb(v4u a) { return __builtin_bit_cast(bf16x8, a); }

__device__ __forceinline__ void ln_stats(const unsigned (&xp)[5], float& mb, float& rden) {
    const float c = lo16(__builtin_amdgcn_readfirstlane(xp[0]));
    float s = 0.f, s2 = 0.f;
#pragma unroll
    for (int i = 0; i < 5; ++i) {
        const float a = lo16(xp[i]) - c, b = hi16(xp[i]) - c;
        s += a + b; s2 = fmaf(a, a, s2); s2 = fmaf(b, b, s2);
    }
    s = wave_sum(s); s2 = wave_sum(s2);
    const float dm = s * (1.0f / D);
    const float var = fmaxf(s2 * (1.0f / D) - dm * dm, 0.f);
    mb = rbf(c + dm);
    const float den = rbf(__builtin_amdgcn_sqrtf(rbf(rbf(var) + 1e-5f)));
    rden = __builtin_amdgcn_rcpf(den);
}
__device__ __forceinline__ unsigned ln_pair(unsigned w, float mb, float rden) {
    const unsigned d = pk2(lo16(w) - mb, hi16(w) - mb);
    return pk2(lo16(d) * rden, hi16(d) * rden);
}

template <int N> __device__ __forceinline__ unsigned reduce_cols(rsrc_t rs, unsigned voff, unsigned base, unsigned tag, unsigned res) {
    v4u p[N];
#pragma unroll
    for (int k = 0; k < N; ++k) p[k] = v4u{0u, ~tag, 0u, ~tag};
    float r0 = lo16(res), r1 = hi16(res);
    PIN(r0); PIN(r1);
    gather<DEPTH, 0>(rs, voff, base, 1024, tag, p);
    float s0 = fv(p[0][0]) + fv(p[1][0]), s1 = fv(p[0][2]) + fv(p[1][2]);
#pragma unroll
    for (int k = 2; k < N; k += 2) { s0 += fv(p[k][0]) + fv(p[k + 1][0]); s1 += fv(p[k][2]) + fv(p[k + 1][2]); }
    auto sw = __builtin_amdgcn_permlane32_swap(__float_as_uint(s0), __float_as_uint(s1), false, false);
    float t = fv(sw[0]) + fv(sw[1]);
    t += dpp<XOR8>(t);
    t = sw16_sum(t);
    auto s2 = __builtin_amdgcn_permlane32_swap(__float_as_uint(t), __float_as_uint(t), false, false);
    const unsigned sp = pk2(fv(s2[0]), fv(s2[1]));
    return pk2(r0 + lo16(sp), r1 + hi16(sp));
}
#define STAMP(i) do { } while (0)

#define LD_REPORT \
    "s_getreg_b32 s92, hwreg(HW_REG_IB_STS)\n\t" \
    "s_and_b32 s93, s92, 15\n\t" \
    "s_bfe_u32 s92, s92, 0x20016\n\t" \
    "s_lshl4_add_u32 s93, s92, s93\n\t" \
    "s_sub_u32 s92, %[iss], s93\n\t" \
    "v_mov_b32 %[vt], s92\n\t"
#define LD_WRITE \
    "s_mov_b64 exec, 1\n\t" \
    "ds_write_b32 %[va], %[vt]\n\t" \
    "s_mov_b64 exec, -1\n\t"
struct Ld { unsigned issued, tnext, pace, va; };
template <bool FLIP> __device__ __forceinline__ void ld_stream(Ld& s, rsrc_t rs, unsigned (&o)[6], unsigned soff, unsigned sstep, unsigned dst, int ng) {
    unsigned vt;
    const unsigned end = __builtin_amdgcn_readfirstlane(s.issued + 6 * ng);
    soff = __builtin_amdgcn_readfirstlane(soff); dst = __builtin_amdgcn_readfirstlane(dst);
    s.issued = __builtin_amdgcn_readfirstlane(s.issued); s.tnext = __builtin_amdgcn_readfirstlane(s.tnext); s.pace = __builtin_amdgcn_readfirstlane(s.pace);
    asm volatile(
        "s_memrealtime s[90:91]\n\t"
        "1:\n\t"
        LD_REPORT
        "s_waitcnt lgkmcnt(0)\n\t"
        "s_lshl_b32 s92, s90, 2\n\t"
        "s_memrealtime s[90:91]\n\t"
        LD_WRITE
        "s_sub_u32 s92, s92, %[tn]\n\t"
        "s_cmp_lt_i32 s92, 0\n\t"
        "s_cbranch_scc1 1b\n\t"
        "s_cmp_gt_u32 s93, %[wm]\n\t"
        "s_cbranch_scc1 1b\n\t"
        "s_mov_b32 m0, %[dst]\n\t"
        "s_add_u32 %[tn], %[tn], %[pace]\n\t"
        "buffer_load_dwordx4 %[o0], %[rs], %[so] offen nt lds\n\t"
        "buffer_load_dwordx4 %[o1], %[rs], %[so] offen offset:1024 nt lds\n\t"
        "buffer_load_dwordx4 %[o2], %[rs], %[so] offen offset:2048 nt lds\n\t"
        "s_add_u32 m0, %[dst], 3072\n\t"
        "s_add_u32 %[iss], %[iss], 6\n\t"
        "buffer_load_dwordx4 %[o3], %[rs], %[so] offen nt lds\n\t"
        "buffer_load_dwordx4 %[o4], %[rs], %[so] offen offset:1024 nt lds\n\t"
        "buffer_load_dwordx4 %[o5], %[rs], %[so] offen offset:2048 nt lds\n\t"
        "s_add_u32 %[dst], %[dst], 6144\n\t"
        "s_add_u32 %[so], %[so], %[ss]\n\t"
        ".if %c[flip]\n\t"
        "v_xor_b32 %[o0], 0x80, %[o0]\n\tv_xor_b32 %[o1], 0x80, %[o1]\n\tv_xor_b32 %[o2], 0x80, %[o2]\n\t"
        "v_xor_b32 %[o3], 0x80, %[o3]\n\tv_xor_b32 %[o4], 0x80, %[o4]\n\tv_xor_b32 %[o5], 0x80, %[o5]\n\t"
        ".endif\n\t"
        "s_cmp_lg_u32 %[iss], %[end]\n\t"
        "s_cbranch_scc1 1b\n\t"
        "s_waitcnt lgkmcnt(0)"
        : [iss] "+&s"(s.issued), [tn] "+&s"(s.tnext), [dst] "+&s"(dst), [so] "+&s"(soff), [vt] "=&v"(vt),
          [o0] "+&v"(o[0]), [o1] "+&v"(o[1]), [o2] "+&v"(o[2]), [o3] "+&v"(o[3]), [o4] "+&v"(o[4]), [o5] "+&v"(o[5])
        : [rs] "s"(rs), [ss] "s"(sstep), [end] "s"(end), [pace] "s"(s.pace), [va] "v"(s.va), [wm] "n"(WMAX - 6), [flip] "n"(FLIP ? 1 : 0)
        : "s90", "s91", "s92", "s93", "scc", "memory");
    s.issued = end;
    s.tnext = __builtin_amdgcn_readfirstlane(s.tnext);
}
__device__ __forceinline__ void ld_gate(Ld& s, int c, unsigned n) {
    unsigned vt, vg, cap = SPIN_LDS;
    const unsigned ga = L_CNT + 4 * c;
    asm volatile(
        "1:\n\t"
        "ds_read_b32 %[vg], %[ga]\n\t"
        LD_REPORT
        LD_WRITE
        "s_sub_u32 %[cap], %[cap], 1\n\t"
        "s_cbranch_scc1 2f\n\t"
        "s_waitcnt lgkmcnt(1)\n\t"
        "v_readfirstlane_b32 s92, %[vg]\n\t"
        "s_cmp_lt_u32 s92, %[n]\n\t"
        "s_cbranch_scc1 1b\n\t"
        "2:\n\t"
        "s_waitcnt lgkmcnt(0)"
        : [vt] "=&v"(vt), [vg] "=&v"(vg), [cap] "+&s"(cap)
        : [iss] "s"(s.issued), [va] "v"(s.va), [ga] "v"(ga), [n] "s"(n)
        : "s92", "s93", "scc", "memory");
}
__device__ __forceinline__ void ld_drain(Ld& s) {
    unsigned vt;
    asm volatile(
        "1:\n\t"
        LD_REPORT
        LD_WRITE
        "s_cmp_lg_u32 s93, 0\n\t"
        "s_cbranch_scc1 1b\n\t"
        : [vt] "=&v"(vt) : [iss] "s"(s.issued), [va] "v"(s.va) : "s92", "s93", "scc", "memory");
}

__global__ void __launch_bounds__(640) k_block(const u16* __restrict__ x, const u16* __restrict__ kc, const u16* __restrict__ vc,
                                               const u16* __restrict__ wq, const u16* __restrict__ wo, const u16* __restrict__ wg,
                                               const u16* __restrict__ wu, const u16* __restrict__ wd, u16* __restrict__ out,
                                               unsigned char* ws KNARG) {
    extern __shared__ __attribute__((aligned(16))) unsigned char lds_raw[];
    LAS unsigned char* L = (LAS unsigned char*)lds_raw;
    const u64 t_start = __builtin_amdgcn_s_memrealtime();
    if (threadIdx.x < 20) ((LAS unsigned*)(L + L_CNT))[threadIdx.x] = 0u;
    asm volatile("s_waitcnt lgkmcnt(0)\n\ts_barrier" :: "s"(x), "s"(kc), "s"(vc), "s"(wq), "s"(wo), "s"(wg), "s"(wu), "s"(wd), "s"(out), "s"(ws) : "memory");
    const int b = blockIdx.x, xcd = b & 7, idx = b >> 3;
    const int h = xcd * 4 + (idx >> 3), j = idx & 7;
    const int tid = threadIdx.x, lane = tid & 63, wave = __builtin_amdgcn_readfirstlane(tid >> 6);
    const int g = lane >> 4, tq = (lane & 15) >> 2, tp = lane & 3, i16 = lane & 15;
    const bool qcu = j >= 2;
    const int qr = (j + 3) >> 3, qc = j - 2 - 3 * qr;
    unsigned tag, xp[5];
    asm volatile("" : "=v"(xp[0]), "=v"(xp[1]), "=v"(xp[2]), "=v"(xp[3]), "=v"(xp[4]), "=v"(tag));
    if (__builtin_expect(wave == 9, 1)) {
        const unsigned xo = lane * 4, zo = 0;
        asm volatile("global_load_dword %0, %6, %7\n\tglobal_load_dword %1, %6, %7 offset:256\n\tglobal_load_dword %2, %6, %7 offset:512\n\t"
                     "global_load_dword %3, %6, %7 offset:768\n\tglobal_load_dword %4, %6, %7 offset:1024\n\tglobal_load_dword %5, %8, %9 sc1"
                     : "+v"(xp[0]), "+v"(xp[1]), "+v"(xp[2]), "+v"(xp[3]), "+v"(xp[4]), "+v"(tag) : "v"(xo), "s"(x), "v"(zo), "s"(ws) : "memory");
    }
#define CODEPF 24000
    if (__builtin_expect(wave == 8, 1)) {
        const rsrc_t rc = mkrs((const void*)(__builtin_amdgcn_s_getpc() & ~255ull), CODEPF);
        asm volatile("s_mov_b32 m0, %0\n\ts_nop 0\n\tbuffer_load_dwordx4 %1, %2, %3 offen lds" :: "s"(L_JUNK), "v"(lane * 16), "s"(rc), "s"(idx * 1024) : "memory");
    }
    if (__builtin_expect(wave == 4 && qcu, 1)) {
        const rsrc_t rq = mkrs(wq + (size_t)(320 * qr) * (H * DH) + h * DH + 64 * qc, 319 * 12288 + 128);
        const unsigned q0 = (lane >> 3) * 12288 + ((lane & 7) ^ (g & 1) * 2) * 16, q1 = (q0 ^ 64) + 98304 - 1024, q2 = q0 + 2 * (98304 - 1024), q3 = q1 + 2 * (98304 - 1024);
#pragma unroll 1
        for (int n = 0; n < 10; ++n)
            asm volatile("s_mov_b32 m0, %0\n\ts_nop 0\n\tbuffer_load_dwordx4 %1, %5, %6 offen nt lds\n\tbuffer_load_dwordx4 %2, %5, %6 offen offset:1024 nt lds\n\t"
                         "buffer_load_dwordx4 %3, %5, %6 offen offset:2048 nt lds\n\tbuffer_load_dwordx4 %4, %5, %6 offen offset:3072 nt lds"
                         :: "s"(L_W + n * 4096), "v"(q0), "v"(q1), "v"(q2), "v"(q3), "s"(rq), "s"(n * 393216) : "memory");
        waitvm<0>();
        lds_signal(C_WQ);
    }
    const bool wocu = j < 5;
    const int gi = (idx >> 3) * 2 + j, oi = (idx >> 3) * 3 + (j - 2), oc = (idx >> 3) * 3 + (j - 5);
    const int rg = (!wocu && oc < 10) ? oc : -1;
    const int gcu = (!qcu && gi < 6) ? xcd * 6 + gi : -1;
    const int ocu = (wocu && qcu && oi < 5) ? xcd * 5 + oi : -1;
    const int pend = gcu >= 0 ? P_WD : P_WO;
    const rsrc_t rws = mkrs(ws, WS_END);
    const int n1 = __builtin_amdgcn_readfirstlane(!qcu ? 8 : wocu ? N1G : 0);
    const int pv0 = P_K + 6 * n1;
    const f32x4 z4 = {0.f, 0.f, 0.f, 0.f};

    if (__builtin_expect(wave == 0, 1)) {
        __builtin_amdgcn_s_setprio(3);
        STAMP(14);
        unsigned ko[6], vo[6], po[6];
#pragma unroll
        for (int c = 0; c < 3; ++c) {
            const unsigned P = 64 * c + lane, r = P / 24, ch = P - r * 24;
            ko[c] = r * 384 + (ch ^ (r >> 1)) * 16 - c * 1024; ko[c + 3] = (ko[c] ^ 64) + 3072;
        }
        const size_t kvoff = ((size_t)h * SK + (SK - W) + j * 256) * DH;
        Ld s;
        s.issued = 0; s.va = L_CNT + 4 * C_PROG;
        s.pace = !qcu ? ((TE - TA0) * 4) / 39 : wocu ? ((TE - TAQ) * 4) / (31 + N1G) : ((TE - TAQ) * 4) / 31;
        s.tnext = ((unsigned)t_start + (qcu ? TAQ : TA0)) * 4;
        ld_stream<false>(s, mkrs(kc + kvoff, 98304), ko, 0, 6144, L_V, 16);
        STAMP(4);
#pragma unroll
        for (int c = 0; c < 3; ++c) {
            const unsigned P = 64 * c + lane, r = P / 24, ch = P - r * 24;
            vo[c] = r * 384 + (ch ^ (r & 2u)) * 16 - c * 1024; vo[c + 3] = (vo[c] ^ 64) + 3072;
            const unsigned wb = (i16 ^ (g << 1)) * 16;
            po[c] = wocu ? (4 * c + g) * 1280 + (wb ^ ((c >> 1) << 7)) - c * 1024 : vo[c] + r * (3072 - 384);
            po[c + 3] = wocu ? (4 * c + 12 + g) * 1280 + (wb ^ ((((c + 3) >> 1) & 1) << 7)) - c * 1024 : vo[c + 3] + (r + 8) * (3072 - 384);
        }
        const rsrc_t rv = mkrs(vc + kvoff, 98304);
        const rsrc_t ro = mkrs(wo + (size_t)(h * DH) * D + j * 128, 191 * 1280 + 256);
        ld_gate(s, C_QP, __builtin_amdgcn_readfirstlane(qcu ? 4 : 0));
        if (n1) ld_stream<true>(s, ro, po, 0, 30720, L_W, n1);
        ld_gate(s, C_KA, 4);
        ld_stream<false>(s, rv, vo, 0, 6144, L_V, 8);
        ld_gate(s, C_KB, 4);
        ld_stream<false>(s, rv, vo, 49152, 6144, L_V + 49152, 8);
        STAMP(6);
        s.pace = 0;
        if (wocu) {
            if (n1 < 8) ld_stream<true>(s, ro, po, n1 * 30720, 30720, L_W + n1 * 6144, 8 - n1);
            if (gcu >= 0) {
#pragma unroll
                for (int c = 0; c < 6; ++c) po[c] = (c / 3) * 3072 + g * 256 + ((i16 ^ (g << 1)) * 16 ^ (((c >> 1) & 1) << 7));
                ld_gate(s, C_O, 4);
                ld_stream<true>(s, mkrs(wd + (size_t)(32 * gcu) * D, 40960), po, 0, 6144, L_V, 7);
            }
        } else if (rg >= 0) {
            const rsrc_t r0 = mkrs(wg + (size_t)(64 * rg) * F + 192 * xcd, 63 * 3072 + 384), r1 = mkrs(wu + (size_t)(64 * rg) * F + 192 * xcd, 63 * 3072 + 384);
            s.tnext += FDELAY * 4; s.pace = PACE2;
            ld_stream<false>(s, r0, po, 0, 49152, L_W, 4);
            ld_stream<false>(s, r1, po, 0, 49152, L_W + 24576, 4);
        }
        ld_drain(s);
        STAMP(9);
        tag = __builtin_amdgcn_readfirstlane(*(LAS unsigned*)(L + L_TAG));
        if (gcu >= 0) {
            STAMP(17);
            v4u p[5];
#pragma unroll
            for (int k = 0; k < 5; ++k) p[k] = v4u{0u, ~tag, 0u, ~tag};
            gather<1, 8>(rws, lane * 16, WS_CP + gcu * 5120, 1024, tag, p);
            auto fold = [&]() {
                const float a0 = ((fv(p[0][0]) + fv(p[1][0])) + (fv(p[2][0]) + fv(p[3][0]))) + fv(p[4][0]);
                const float a1 = ((fv(p[0][2]) + fv(p[1][2])) + (fv(p[2][2]) + fv(p[3][2]))) + fv(p[4][2]);
                auto sw = __builtin_amdgcn_permlane32_swap(__float_as_uint(a0), __float_as_uint(a1), false, false);
                return fv(sw[0]) + fv(sw[1]);
            };
            float cs = fold();
            PIN(cs);
            unsigned ha = L_HH + (lane & 31) * 2;
            PIN(ha);
#pragma unroll
            for (int k = 0; k < 5; ++k) p[k] = v4u{0u, ~tag, 0u, ~tag};
            STAMP(18);
            until(t_start + TG3);
            gather<DEPTH, 0>(rws, lane * 16, WS_GP + gcu * 5120, 1024, tag, p);
            f32x4 st = *(volatile LAS f32x4*)(L + L_ST);
            STAMP(12);
            float sv = fold();
            PIN(sv);
#pragma unroll 1
            for (unsigned sp = 0; sp < SPIN_LDS && __builtin_amdgcn_readfirstlane(__float_as_uint(st[2])) == 0u; ++sp) st = *(volatile LAS f32x4*)(L + L_ST);
            const float v = rbf((sv - st[0] * cs) * st[1]);
            auto sw = __builtin_amdgcn_permlane32_swap(__float_as_uint(v), __float_as_uint(v), false, false);
            const float sg = rbf(__builtin_amdgcn_rcpf(1.0f + __expf(-fv(sw[0]))));
            *(LAS u16*)(L + ha) = (u16)bfbits(sg * fv(sw[1]));
            lds_signal(C_HH);
        } else if (ocu >= 0) {
            const rsrc_t rout = mkrs(out, 1280);
            unsigned oo = lane < 8 ? (8 * ocu + lane) * 4 : OOB;
            PIN(oo);
            lds_wait<true>(L, C_O, 4);
            v2u hres[1] = {v2u{0u, ~tag}};
            until(t_start + TG3);
            gather<1, 4>(rws, (lane & 7) * 8, WS_HB + xcd * 4096 + 64 * ocu, 0, tag, hres);
            until(t_start + TG4);
            const unsigned ov = reduce_cols<6>(rws, lane * 16, WS_DP + ocu * 6144, tag, hres[0][0]);
            __builtin_amdgcn_raw_buffer_store_b32(ov, rout, oo, 0, 0);
            if (ocu == 0 && lane == 0) __hip_atomic_store((gu32*)(ws + WS_EPOCH), tag, RLX_AGENT);
            STAMP(16);
        }
    } else if (wave == 9) {
        asm volatile("s_waitcnt vmcnt(0)" : "+v"(xp[0]), "+v"(xp[1]), "+v"(xp[2]), "+v"(xp[3]), "+v"(xp[4]), "+v"(tag) :: "memory");
        float mb, rden;
        ln_stats(xp, mb, rden);
#pragma unroll
        for (int i = 0; i < 5; ++i) *(LAS unsigned*)(L + L_LN + (lane + 64 * i) * 4) = ln_pair(xp[i], mb, rden);
        tag = __builtin_amdgcn_readfirstlane(tag) + 1u;
        if (lane == 0) *(LAS unsigned*)(L + L_TAG) = tag;
        lds_signal(C_X);
        STAMP(23);
        {
            until(t_start + TQ);
            v4u qp[3];
#pragma unroll
            for (int k = 0; k < 3; ++k) qp[k] = v4u{0u, ~tag, 0u, ~tag};
            if (!(KN7 & 1)) gather<1, 8>(rws, lane * 16, WS_QP + h * 3072, 1024, tag, qp);
#pragma unroll
            for (int k = 0; k < 3; ++k) *(LAS unsigned*)(L + L_Q + k * 128 + (lane & 31) * 4) = pk2(sw32_sum(fv(qp[k][0])), sw32_sum(fv(qp[k][2])));
            lds_signal(C_Q);
            STAMP(3);
        }
        lds_wait<true>(L, C_P, 4);
        const f32x4 mx = *(LAS f32x4*)(L + L_MX), ls = *(LAS f32x4*)(L + L_MX + 16);
        const float m = fmaxf(fmaxf(mx[0], mx[1]), fmaxf(mx[2], mx[3]));
        const float l = (ls[0] + ls[1]) + (ls[2] + ls[3]);
        if (!(KN7 & 8)) put2(rws, lane == 0 ? WS_ST + (h * 8 + j) * 128 : OOB, tag, m, l);
        if (wocu) {
            until(t_start + TS);
            const unsigned tt = lane < 8 ? ~tag : tag;
            v4u st[1] = {v4u{0u, tt, 0u, tt}};
            if (!(KN7 & 4)) gather<1, 8>(rws, lane * 128, WS_ST + h * 1024, 0, tag, st);
            const float ms = fv(st[0][0]);
            float M = fmaxf(ms, dpp<XOR1>(ms)); M = fmaxf(M, dpp<XOR2>(M)); M = fmaxf(M, dpp<XOR7>(M));
            const float e = __expf(ms - M);
            float Ls = e * fv(st[0][2]);
            Ls += dpp<XOR1>(Ls); Ls += dpp<XOR2>(Ls); Ls += dpp<XOR7>(Ls);
            if (lane < 8) *(LAS float*)(L + L_WGT + lane * 4) = e * __builtin_amdgcn_rcpf(Ls);
            lds_signal(C_W);
            STAMP(19);
            if (gcu >= 0) {
                lds_wait<true>(L, C_O, 4);
                until(t_start + TG3 - 25);
                v2u hv[5];
#pragma unroll
                for (int i = 0; i < 5; ++i) hv[i] = v2u{0u, ~tag};
                gather<DEPTH, 0>(rws, lane * 8, WS_HB + xcd * 4096, 512, tag, hv);
#pragma unroll
                for (int i = 0; i < 5; ++i) xp[i] = hv[i][0];
                ln_stats(xp, mb, rden);
                if (lane == 0) *(LAS f32x4*)(L + L_ST) = f32x4{mb, rden, 1.0f, 0.f};
                STAMP(15);
            }
        }
    } else {
        if (wave < 5) {
            const int a = wave - 1;
            unsigned xres = 0;
            if (rg >= 0) xres = ((const unsigned*)x)[32 * rg + 8 * a + (lane & 7)];
            lds_wait<true>(L, C_X, 1);
            tag = __builtin_amdgcn_readfirstlane(*(LAS unsigned*)(L + L_TAG));
            if (qcu) {
                bf16x8 xa[10];
#pragma unroll
                for (int s = 0; s < 10; ++s) xa[s] = *(LAS bf16x8*)(L + L_LN + (320 * qr + 32 * s + 8 * g) * 2);
                lds_wait<true>(L, C_WQ, 1);
                if (a == 0) STAMP(1);
                const unsigned ca = L_W + (8 * g + tq) * 128 + ((a ^ ((tq >> 1) | ((g & 1) << 1))) * 2 + (tp >> 1)) * 16 + (tp & 1) * 8;
                f32x4 q0 = z4, q1 = z4;
#pragma unroll
                for (int s = 0; s < 10; s += 2) {
                    q0 = mfma32(xa[s], cat8(trd(L, ca + s * 4096), trd(L, ca + s * 4096 + 512)), q0);
                    q1 = mfma32(xa[s + 1], cat8(trd(L, ca + s * 4096 + 4096), trd(L, ca + s * 4096 + 4608)), q1);
                }
                lds_signal(C_QP);
                if (!(KN7 & 2)) put1(rws, lane < 16 ? WS_QP + h * 3072 + qc * 1024 + qr * 512 + (16 * a + lane) * 8 : OOB, tag, __float_as_uint(q0[0] + q1[0]));
                if (a == 0) STAMP(2);
            }
            if (wocu) {
                unsigned oa = L_O + (lane < 48 ? 48 * a + lane : 192 + lane) * 2;
                PIN(oa);
                lds_wait<true>(L, C_W, 1);
                f32x4 w0 = *(LAS f32x4*)(L + L_WGT), w1 = *(LAS f32x4*)(L + L_WGT + 16);
                PIN(w0); PIN(w1);
                unsigned own;
                lds_wait_rd<true>(L, C_OWN, 4, [&] { own = *(volatile LAS unsigned*)(L + L_OWN + (64 * a + lane) * 4); });
                v2u v[8];
#pragma unroll
                for (int k = 0; k < 8; ++k) v[k] = v2u{own, (k == j || lane >= 48) ? tag : ~tag};
                until(t_start + TG1);
                lds_wait<false>(L, C_PROG, P_WO);
                gather<DEPTH, 0>(rws, lane * 8, WS_OP + (h * 8 * 192 + 48 * a) * 8, 192 * 8, tag, v);
                float o0 = w0[0] * fv(v[0][0]), o1 = w1[0] * fv(v[4][0]);
#pragma unroll
                for (int k = 1; k < 4; ++k) { o0 = fmaf(w0[k], fv(v[k][0]), o0); o1 = fmaf(w1[k], fv(v[4 + k][0]), o1); }
                *(LAS u16*)(L + oa) = (u16)bfbits(o0 + o1);
                lds_signal(C_O);
                if (a == 0) STAMP(8);
            } else if (rg >= 0) {
                unsigned ha = L_O + (8 * a + (lane & 7)) * 4, ho = lane < 8 ? WS_HB + xcd * 4096 + (32 * rg + 8 * a + lane) * 8 : OOB;
                PIN(ha); PIN(ho);
                lds_wait<true>(L, C_PROG, P_WO);
                until(t_start + TG2);
                const unsigned hp = reduce_cols<4>(rws, lane * 16, WS_AP + (4 * rg + a) * 4096, tag, xres);
                *(LAS unsigned*)(L + ha) = hp;
                lds_signal(C_O);
                put1(rws, ho, tag, hp);
                if (a == 0) STAMP(11);
            }
        } else {
            const int kw = wave - 5;
            bf16x8 kf[4][6];
            {
                const unsigned kb = L_V + (16 * kw + i16) * 384;
#pragma unroll
                for (int n = 0; n < 4; ++n) {
                    lds_wait<true>(L, C_PROG, 12 * ((kw >> 1) + 2 * n + 1));
#pragma unroll
                    for (int s = 0; s < 6; ++s) kf[n][s] = *(LAS bf16x8*)(L + kb + n * 24576 + ((4 * s + g) ^ (i16 >> 1)) * 16);
                    if (n & 1) lds_signal(n == 1 ? C_KA : C_KB);
                }
            }
            lds_wait<true>(L, C_Q, 1);
            tag = __builtin_amdgcn_readfirstlane(*(LAS unsigned*)(L + L_TAG));
            f32x4 sa[4] = {z4, z4, z4, z4};
#pragma unroll
            for (int s = 0; s < 6; ++s) {
                const bf16x8 qf = *(LAS bf16x8*)(L + L_Q + (32 * s + 8 * g) * 2);
#pragma unroll
                for (int n = 0; n < 4; ++n) sa[n] = mfma32(qf, kf[n][s], sa[n]);
            }
            const float raw = g == 0 ? sa[0][0] : g == 1 ? sa[1][0] : g == 2 ? sa[2][0] : sa[3][0];
            const float s = rbf(rbf(raw) * 0.07216878364870322f);
            const float mw = wave_max(s);
            if (lane == 0) *(LAS float*)(L + L_MX + kw * 4) = mw;
            lds_signal(C_MAX);
            lds_wait<false>(L, C_MAX, 4);
            const f32x4 mx = *(LAS f32x4*)(L + L_MX);
            const float m = fmaxf(fmaxf(mx[0], mx[1]), fmaxf(mx[2], mx[3]));
            const float e = __expf(s - m);
            *(LAS u16*)(L + L_P + (16 * (kw + 4 * g) + i16) * 2) = (u16)bfbits(e);
            const float lw = wave_sum(e);
            if (lane == 0) *(LAS float*)(L + L_MX + 16 + kw * 4) = lw;
            lds_signal(C_P);
            lds_wait<false>(L, C_P, 4);
            if (kw == 0) STAMP(5);
            bf16x8 pf[8];
#pragma unroll
            for (int s8 = 0; s8 < 8; ++s8) pf[s8] = *(LAS bf16x8*)(L + L_P + (32 * s8 + 8 * g) * 2);
            const int fvs = (tq >> 1) | ((g & 1) << 1);
            unsigned vb[3], vl[3];
#pragma unroll
            for (int t = 0; t < 3; ++t) { vb[t] = (8 * g + tq) * 384 + (((3 * kw + t) ^ fvs) * 2 + (tp >> 1)) * 16 + (tp & 1) * 8; vl[t] = L_V + 6 * 12288 + vb[t]; PIN(vl[t]); }
            unsigned oo = lane < 48 ? WS_OP + ((h * 8 + j) * 192 + 48 * kw + lane) * 8 : OOB, ow = L_OWN + (64 * kw + lane) * 4;
            PIN(oo); PIN(ow);
            f32x4 oa[3] = {z4, z4, z4};
            lds_wait<true>(L, C_PROG, pv0 + 72);
#pragma unroll
            for (int s8 = 0; s8 < 6; ++s8)
#pragma unroll
                for (int t = 0; t < 3; ++t) oa[t] = mfma32(pf[s8], cat8(trd(L, L_V + vb[t] + s8 * 12288), trd(L, L_V + vb[t] + s8 * 12288 + 1536)), oa[t]);
            lds_signal(C_PVA);
#pragma unroll
            for (int s8 = 6; s8 < 8; ++s8) {
                bf16x8 vf[3];
                lds_wait_rd<false>(L, C_PROG, pv0 + 12 * s8 + 12, [&] {
#pragma unroll
                    for (int t = 0; t < 3; ++t) vf[t] = cat8(trd(L, vl[t] + (s8 - 6) * 12288), trd(L, vl[t] + (s8 - 6) * 12288 + 1536));
                });
                if (s8 == 7 && kw == 0) STAMP(20);
#pragma unroll
                for (int t = 0; t < 3; ++t) oa[t] = mfma32(pf[s8], vf[t], oa[t]);
            }
            const float op = g == 0 ? oa[0][0] : g == 1 ? oa[1][0] : oa[2][0];
            put1(rws, oo, tag, __float_as_uint(op));
            *(LAS float*)(L + ow) = op;
            lds_signal(C_OWN);
            if (kw == 0) STAMP(7);
            if (wocu) {
                unsigned ao = lane < 32 ? WS_AP + (((8 * j + 2 * kw + g) * 32 + h) * 16 + i16) * 8 : OOB, al = L_O + 16 * g;
                PIN(ao); PIN(al);
                lds_wait<false>(L, C_PROG, P_WO);
                bf16x8 bw[2][6], af[6];
                const int f3 = tq | ((g & 1) << 2);
#pragma unroll
                for (int t = 0; t < 2; ++t) {
                    const unsigned cb = L_W + (8 * g + tq) * 256 + (((2 * kw + t) ^ f3) * 2 + (tp >> 1)) * 16 + (tp & 1) * 8;
#pragma unroll
                    for (int s6 = 0; s6 < 6; ++s6) bw[t][s6] = cat8(trd(L, cb + s6 * 8192), trd(L, cb + s6 * 8192 + 1024));
                }
                lds_wait_rd<false>(L, C_O, 4, [&] {
#pragma unroll
                    for (int s6 = 0; s6 < 6; ++s6) af[s6] = *(volatile LAS bf16x8*)(L + al + 64 * s6);
                });
                f32x4 a0 = z4, a1 = z4;
#pragma unroll
                for (int s6 = 0; s6 < 6; ++s6) { a0 = mfma32(af[s6], bw[0][s6], a0); a1 = mfma32(af[s6], bw[1][s6], a1); }
                put1(rws, ao, tag, __float_as_uint(g ? a1[0] : a0[0]));
                if (kw == 0) STAMP(10);
            } else if (rg >= 0) {
                lds_wait<true>(L, C_PROG, P_WO);
                bf16x8 bw[2][3][2];
#pragma unroll
                for (int mt = 0; mt < 2; ++mt)
#pragma unroll
                    for (int t = 0; t < 3; ++t)
#pragma unroll
                        for (int s2 = 0; s2 < 2; ++s2) bw[mt][t][s2] = cat8(trd(L, L_W + mt * 24576 + vb[t] + s2 * 12288), trd(L, L_W + mt * 24576 + vb[t] + s2 * 12288 + 1536));
                const int f = 48 * kw + lane;
                unsigned go = lane < 48 ? WS_CP + ((6 * xcd + (f >> 5)) * 10 + rg) * 512 + (f & 31) * 16 : OOB, al = L_O + 16 * g;
                PIN(al);
                const v4u ones = {0x3F803F80u, 0x3F803F80u, 0x3F803F80u, 0x3F803F80u};
                bf16x8 af0 = asb(ones), af1 = asb(ones);
#pragma unroll 1
                for (int pass = 0; pass < 2; ++pass) {
                    float r[2];
#pragma unroll
                    for (int mt = 0; mt < 2; ++mt) {
                        f32x4 d[3];
#pragma unroll
                        for (int t = 0; t < 3; ++t) d[t] = mfma32(af1, bw[mt][t][1], mfma32(af0, bw[mt][t][0], z4));
                        r[mt] = g == 0 ? d[0][0] : g == 1 ? d[1][0] : d[2][0];
                    }
                    put2(rws, go, tag, r[0], r[1]);
                    if (pass) break;
                    go += WS_GP - WS_CP;
                    PIN(go);
                    lds_wait_rd<false>(L, C_O, 4, [&] { af0 = *(volatile LAS bf16x8*)(L + al); af1 = *(volatile LAS bf16x8*)(L + al + 64); });
                }
                if (kw == 0) STAMP(21);
            }
        }
        if (gcu >= 0) {
            const int w8 = wave - 1;
            unsigned d0 = WS_DP + (((5 * w8 + g) * 48 + gcu) * 16 + i16) * 8, d1 = lane < 16 ? WS_DP + (((5 * w8 + 4) * 48 + gcu) * 16 + i16) * 8 : OOB, al = L_HH + 16 * g;
            PIN(d0); PIN(d1); PIN(al);
            lds_wait<true>(L, C_PROG, P_WD);
            bf16x8 bw[5], af;
#pragma unroll
            for (int t = 0; t < 5; ++t) {
                const int tt = 5 * w8 + t;
                unsigned ad[2];
#pragma unroll
                for (int u = 0; u < 2; ++u) {
                    const int r = 8 * g + 4 * u + tq, B = 5 * r + (tt >> 3);
                    ad[u] = L_V + r * 1280 + (tt ^ ((B & 3) | ((B >> 1) & 4))) * 32 + tp * 8;
                }
                bw[t] = cat8(trd(L, ad[0]), trd(L, ad[1]));
            }
            lds_wait_rd<false>(L, C_HH, 1, [&] { af = *(volatile LAS bf16x8*)(L + al); });
            f32x4 d[5];
#pragma unroll
            for (int t = 0; t < 5; ++t) d[t] = mfma32(af, bw[(t + 4) % 5], z4);
            put1(rws, d1, tag, __float_as_uint(d[0][0]));
            put1(rws, d0, tag, __float_as_uint(g == 0 ? d[1][0] : g == 1 ? d[2][0] : g == 2 ? d[3][0] : d[4][0]));
            if (w8 == 0) STAMP(13);
        }
    }
}

extern "C" void kernel_launch(void* const* d_in, const int* in_sizes, int n_in, void* d_out, int out_size, void* d_ws, size_t ws_size, hipStream_t stream) {
    (void)hipFuncSetAttribute((const void*)k_block, hipFuncAttributeMaxDynamicSharedMemorySize, L_END);
    k_block<<<256, 640, L_END, stream>>>((const u16*)d_in[0], (const u16*)d_in[1], (const u16*)d_in[2], (const u16*)d_in[3], (const u16*)d_in[4],
                                         (const u16*)d_in[6], (const u16*)d_in[7], (const u16*)d_in[8], (u16*)d_out, (unsigned char*)d_ws
        );
}
```

```cpp
#include <hip/hip_runtime.h>
#include <stdint.h>

typedef unsigned short u16;
typedef unsigned long long u64;
typedef unsigned v4u __attribute__((ext_vector_type(4)));
typedef unsigned v2u __attribute__((ext_vector_type(2)));
typedef float f32x4 __attribute__((ext_vector_type(4)));
typedef short s16x4 __attribute__((ext_vector_type(4)));
typedef __bf16 bf16x8 __attribute__((ext_vector_type(8)));
typedef __bf16 bf16x2 __attribute__((ext_vector_type(2)));
#define GAS __attribute__((address_space(1)))
#define LAS __attribute__((address_space(3)))
typedef GAS u64 gu64;
typedef GAS unsigned gu32;
typedef __amdgpu_buffer_rsrc_t rsrc_t;
#define RLX_AGENT __ATOMIC_RELAXED, __HIP_MEMORY_SCOPE_AGENT

constexpr int D = 640, H = 32, SK = 8192, DH = 192, W = 2048, F = 1536;

constexpr unsigned WS_EPOCH = 0;
constexpr unsigned WS_T0 = 1024;
constexpr unsigned WS_QP = 4096;
constexpr unsigned WS_OP = WS_QP + 32 * 3072;
constexpr unsigned WS_ST = WS_OP + 32 * 8 * 192 * 8;
constexpr unsigned WS_AP = WS_ST + 32 * 8 * 128;
constexpr unsigned WS_HB = WS_AP + 40 * 512 * 8;
constexpr unsigned WS_CP = WS_HB + 8 * 4096;
constexpr unsigned WS_GP = WS_CP + 48 * 5120;
constexpr unsigned WS_DP = WS_GP + 48 * 5120;
constexpr unsigned WS_END = WS_DP + 40 * 6144;
constexpr unsigned OOB = 0x7FFFFF00u;

constexpr unsigned L_W = 0;
constexpr unsigned L_V = 49152;
constexpr unsigned L_Q = L_V + 98304;
constexpr unsigned L_P = L_Q + 384;
constexpr unsigned L_O = L_P + 512;
constexpr unsigned L_LN = L_O + 512;
constexpr unsigned L_OWN = L_LN + 1280;
constexpr unsigned L_HH = L_OWN + 1024;
constexpr unsigned L_MX = L_HH + 64;
constexpr unsigned L_WGT = L_MX + 32;
constexpr unsigned L_CNT = L_WGT + 32;
constexpr unsigned L_ST = L_CNT + 64;
constexpr unsigned L_TAG = L_ST + 16;
constexpr unsigned L_STAMP = L_TAG + 16;
constexpr unsigned L_JUNK = L_STAMP + 256;
constexpr unsigned L_END = L_JUNK + 1024;
static_assert(L_END <= 163840, "LDS");
enum { C_X = 0, C_PROG, C_KA, C_KB, C_Q, C_MAX, C_P, C_W, C_OWN, C_O, C_HH, C_WQ, C_QP, C_CS, C_GO, C_LATE };
constexpr int P_K = 96, P_WO = 240, P_WD = 282;

#define KTE 865
#define KNOB_DEFAULTS {2, 40, 130, KTE, 40, 420, -80, 0, 6, 150, 30, 130, 60, 0, 0, 0, 0, 0, 0, 0, 2, 4, 16, 4}
constexpr int kn_c[24] = KNOB_DEFAULTS;
#define KN(i) kn_c[i]
#define KNARG
#define N1G KN(0)
#define TA0 KN(1)
#define TAQ KN(2)
#define TE KN(3)
#define FDELAY KN(4)
#define TQ KN(5)
#define TS (TE + KN(6))
#define KN7 KN(7)
#define D1 KN(8)
#define D2 KN(9)
#define D3 KN(10)
#define D4 KN(11)
#define DOBS KN(12)
#define PACE2 KN(13)
#define RSL KN(14)
#define THR1 KN(16)
#define THR2 KN(17)
#define THR3 KN(18)
#define THR4 KN(19)
#define WMAX 36
#define DEPTH 2

constexpr unsigned SPIN_LDS = 1u << 20;
constexpr unsigned SPIN_GLB = 1u << 15;

__device__ __forceinline__ unsigned pk2(float lo, float hi) {
    const bf16x2 r = {(__bf16)lo, (__bf16)hi};
    return __builtin_bit_cast(unsigned, r);
}
__device__ __forceinline__ unsigned bfbits(float f) { return (unsigned)__builtin_bit_cast(u16, (__bf16)f); }
__device__ __forceinline__ float rbf(float f) { return __uint_as_float(bfbits(f) << 16); }
__device__ __forceinline__ float lo16(unsigned w) { return __uint_as_float(w << 16); }
__device__ __forceinline__ float hi16(unsigned w) { return __uint_as_float(w & 0xFFFF0000u); }
__device__ __forceinline__ float dot2(unsigned a, unsigned b, float c) {
    return __builtin_amdgcn_fdot2_f32_bf16(__builtin_bit_cast(bf16x2, a), __builtin_bit_cast(bf16x2, b), c, false);
}
template <int CTRL> __device__ __forceinline__ float dpp(float x) {
    return __builtin_bit_cast(float, __builtin_amdgcn_mov_dpp(__builtin_bit_cast(int, x), CTRL, 0xf, 0xf, true));
}
constexpr int XOR1 = 0xB1, XOR2 = 0x4E, XOR7 = 0x141, XOR8 = 0x128;
__device__ __forceinline__ float sw16_sum(float x) {
    auto s = __builtin_amdgcn_permlane16_swap(__float_as_uint(x), __float_as_uint(x), false, false);
    return __uint_as_float(s[0]) + __uint_as_float(s[1]);
}
__device__ __forceinline__ float sw32_sum(float x) {
    auto s = __builtin_amdgcn_permlane32_swap(__float_as_uint(x), __float_as_uint(x), false, false);
    return __uint_as_float(s[0]) + __uint_as_float(s[1]);
}
__device__ __forceinline__ float sw16_max(float x) {
    auto s = __builtin_amdgcn_permlane16_swap(__float_as_uint(x), __float_as_uint(x), false, false);
    return fmaxf(__uint_as_float(s[0]), __uint_as_float(s[1]));
}
__device__ __forceinline__ float sw32_max(float x) {
    auto s = __builtin_amdgcn_permlane32_swap(__float_as_uint(x), __float_as_uint(x), false, false);
    return fmaxf(__uint_as_float(s[0]), __uint_as_float(s[1]));
}
__device__ __forceinline__ float wave_sum(float x) {
    x += dpp<XOR1>(x); x += dpp<XOR2>(x); x += dpp<XOR7>(x); x += dpp<XOR8>(x);
    return sw32_sum(sw16_sum(x));
}
__device__ __forceinline__ float wave_max(float x) {
    x = fmaxf(x, dpp<XOR1>(x)); x = fmaxf(x, dpp<XOR2>(x)); x = fmaxf(x, dpp<XOR7>(x)); x = fmaxf(x, dpp<XOR8>(x));
    return sw32_max(sw16_max(x));
}
__device__ __forceinline__ rsrc_t mkrs(const void* p, unsigned bytes) { return __builtin_amdgcn_make_buffer_rsrc((void*)p, 0, bytes, 0x00020000); }

__device__ __forceinline__ void dma16(rsrc_t rs, unsigned voff, unsigned soff, unsigned lds_dst) {
    asm volatile("s_mov_b32 m0, %0\n\ts_nop 0\n\tbuffer_load_dwordx4 %1, %2, %3 offen nt lds"
                 :: "s"(lds_dst), "v"(voff), "s"(rs), "s"(soff) : "memory");
}
template <int N> __device__ __forceinline__ void waitvm() { asm volatile("s_waitcnt vmcnt(%0)" :: "n"(N) : "memory"); }
#define LGKM_DRAIN() asm volatile("s_waitcnt lgkmcnt(0)" ::: "memory")
#define MEMBAR() asm volatile("" ::: "memory")
#define BAR() do { __builtin_amdgcn_sched_barrier(0); asm volatile("s_barrier" ::: "memory"); __builtin_amdgcn_sched_barrier(0); } while (0)

__device__ __forceinline__ void lds_signal(int c) {
    const unsigned addr = L_CNT + 4 * c, one = 1u;
    asm volatile("s_mov_b64 exec, 1\n\tds_add_u32 %0, %1\n\ts_mov_b64 exec, -1" :: "v"(addr), "v"(one) : "memory");
}
__device__ __forceinline__ void lds_set(int c, unsigned val) {
    const unsigned addr = L_CNT + 4 * c;
    asm volatile("s_mov_b64 exec, 1\n\tds_write_b32 %0, %1\n\ts_mov_b64 exec, -1" :: "v"(addr), "v"(val) : "memory");
}
template <bool SLEEP, class F> __device__ __forceinline__ void lds_wait_rd(LAS unsigned char* L, int c, unsigned n, F rd) {
    volatile LAS unsigned* p = (volatile LAS unsigned*)(L + L_CNT) + c;
    unsigned cap = SPIN_LDS;
#pragma unroll 1
    do {
        const unsigned f = *p;
        MEMBAR();
        rd();
        MEMBAR();
        if ((unsigned)__builtin_amdgcn_readfirstlane(f) >= n) break;
        if (SLEEP) __builtin_amdgcn_s_sleep(1);
        MEMBAR();
    } while (--cap != 0);
    MEMBAR();
}
template <bool SLEEP> __device__ __forceinline__ void lds_wait(LAS unsigned char* L, int c, unsigned n) { lds_wait_rd<SLEEP>(L, c, n, [] {}); }
__device__ __forceinline__ void until(u64 t) { while (__builtin_amdgcn_s_memrealtime() < t) __builtin_amdgcn_s_sleep(1); }
__device__ __forceinline__ void delay(int ticks) { until(__builtin_amdgcn_s_memrealtime() + ticks); }
__device__ __forceinline__ void nap(int n) { for (int i = 0; i < n; ++i) __builtin_amdgcn_s_sleep(1); }
#define PIN(x) asm volatile("" : "+v"(x))
#define SB() __builtin_amdgcn_sched_barrier(0)
__device__ __forceinline__ float fv(unsigned u) { return __uint_as_float(u); }
__device__ __forceinline__ void put1(rsrc_t rs, unsigned off, unsigned tag, unsigned val) { __builtin_amdgcn_raw_buffer_store_b64(v2u{val, tag}, rs, off, 0, 16); }
__device__ __forceinline__ void put2(rsrc_t rs, unsigned off, unsigned tag, float a, float b) {
    __builtin_amdgcn_raw_buffer_store_b128(v4u{__float_as_uint(a), tag, __float_as_uint(b), tag}, rs, off, 0, 16);
}
__device__ __forceinline__ u64 badmask(v2u x, unsigned tag) {
    u64 m;
    asm volatile("v_cmp_ne_u32_e64 %0, %1, %2" : "=s"(m) : "s"(tag), "v"(x[1]));
    return m;
}
__device__ __forceinline__ u64 badmask(v4u x, unsigned tag) {
    u64 m;
    const unsigned t = __builtin_amdgcn_bitop3_b32(x[1], x[3], tag, 0x7E);
    asm volatile("v_cmp_ne_u32_e64 %0, 0, %1" : "=s"(m) : "v"(t));
    return m;
}
__device__ __forceinline__ void gload(v2u& x, u64 m, rsrc_t rs, unsigned voff, unsigned soff) {
    asm volatile("s_mov_b64 exec, %2\n\tbuffer_load_dwordx2 %0, %1, %3, %4 offen sc1\n\ts_mov_b64 exec, -1" : "+v"(x) : "v"(voff), "s"(m), "s"(rs), "s"(soff) : "memory");
}
__device__ __forceinline__ void gload(v4u& x, u64 m, rsrc_t rs, unsigned voff, unsigned soff) {
    asm volatile("s_mov_b64 exec, %2\n\tbuffer_load_dwordx4 %0, %1, %3, %4 offen sc1\n\ts_mov_b64 exec, -1" : "+v"(x) : "v"(voff), "s"(m), "s"(rs), "s"(soff) : "memory");
}
template <int DP, int SLP, int N, class T> __device__ __forceinline__ void gather(rsrc_t rs, unsigned voff, unsigned base, unsigned stride, unsigned tag, T (&x)[N]) {
    u64 m[N];
#pragma unroll
    for (int k = 0; k < N; ++k) { PIN(x[k]); m[k] = badmask(x[k], tag); }
    unsigned cap = SPIN_GLB;
#pragma unroll 1
    do {
#pragma unroll
        for (int k = 0; k < N; ++k) gload(x[k], m[k], rs, voff, base + k * stride);
        waitvm<0>();
#pragma unroll
        for (int k = 0; k < N; ++k) PIN(x[k]);
        u64 any = 0;
#pragma unroll
        for (int k = 0; k < N; ++k) { m[k] = badmask(x[k], tag); any |= m[k]; }
        if (!any) break;
        if (SLP) __builtin_amdgcn_s_sleep(SLP);
        MEMBAR();
    } while (--cap != 0);
}
template <int N, int PM> __device__ __forceinline__ void sum_pairs(rsrc_t rs, unsigned voff, unsigned base, unsigned tag, float& a0, float& a1);
template <int PM> __device__ __forceinline__ float combine_o(rsrc_t rs, unsigned voff, unsigned base, unsigned tag, int j, unsigned own, f32x4 w0, f32x4 w1, unsigned napn);
template <> __device__ __forceinline__ void sum_pairs<4, 0>(rsrc_t rs, unsigned voff, unsigned base, unsigned tag, float& a0, float& a1) {
    unsigned cap = SPIN_GLB;
    asm volatile(
        "buffer_load_dwordx4 v[144:147], %[vo], %[rs], %[b0] offen offset:0 sc1\n\t"
        "buffer_load_dwordx4 v[148:151], %[vo], %[rs], %[b0] offen offset:1024 sc1\n\t"
        "buffer_load_dwordx4 v[152:155], %[vo], %[rs], %[b0] offen offset:2048 sc1\n\t"
        "buffer_load_dwordx4 v[156:159], %[vo], %[rs], %[b0] offen offset:3072 sc1\n\t"
        "s_waitcnt vmcnt(0)\n\t"
        "v_bitop3_b32 v140, v145, v147, %[tag] bitop3:0x7e\n\t"
        "v_bitop3_b32 v141, v149, v151, %[tag] bitop3:0x7e\n\t"
        "v_bitop3_b32 v142, v153, v155, %[tag] bitop3:0x7e\n\t"
        "v_bitop3_b32 v143, v157, v159, %[tag] bitop3:0x7e\n\t"
        "v_cmp_ne_u32_e64 s[78:79], 0, v140\n\t"
        "v_cmp_ne_u32_e64 s[80:81], 0, v141\n\t"
        "v_cmp_ne_u32_e64 s[82:83], 0, v142\n\t"
        "v_cmp_ne_u32_e64 s[84:85], 0, v143\n\t"
        "s_or_b64 s[92:93], s[78:79], s[80:81]\n\t"
        "s_or_b64 s[92:93], s[92:93], s[82:83]\n\t"
        "s_or_b64 s[92:93], s[92:93], s[84:85]\n\t"
        "s_cbranch_scc0 3f\n\t"
        "1:\n\t"
        "s_mov_b64 exec, s[78:79]\n\t"
        "buffer_load_dwordx4 v[144:147], %[vo], %[rs], %[b0] offen offset:0 sc1\n\t"
        "s_mov_b64 exec, s[80:81]\n\t"
        "buffer_load_dwordx4 v[148:151], %[vo], %[rs], %[b0] offen offset:1024 sc1\n\t"
        "s_mov_b64 exec, s[82:83]\n\t"
        "buffer_load_dwordx4 v[152:155], %[vo], %[rs], %[b0] offen offset:2048 sc1\n\t"
        "s_mov_b64 exec, s[84:85]\n\t"
        "buffer_load_dwordx4 v[156:159], %[vo], %[rs], %[b0] offen offset:3072 sc1\n\t"
        "s_mov_b64 exec, -1\n\t"
        "s_sub_u32 %[cap], %[cap], 1\n\t"
        "s_cbranch_scc1 3f\n\t"
        "s_waitcnt vmcnt(0)\n\t"
        "v_bitop3_b32 v140, v145, v147, %[tag] bitop3:0x7e\n\t"
        "v_bitop3_b32 v141, v149, v151, %[tag] bitop3:0x7e\n\t"
        "v_bitop3_b32 v142, v153, v155, %[tag] bitop3:0x7e\n\t"
        "v_bitop3_b32 v143, v157, v159, %[tag] bitop3:0x7e\n\t"
        "v_cmp_ne_u32_e64 s[78:79], 0, v140\n\t"
        "v_cmp_ne_u32_e64 s[80:81], 0, v141\n\t"
        "v_cmp_ne_u32_e64 s[82:83], 0, v142\n\t"
        "v_cmp_ne_u32_e64 s[84:85], 0, v143\n\t"
        "s_or_b64 s[92:93], s[78:79], s[80:81]\n\t"
        "s_or_b64 s[92:93], s[92:93], s[82:83]\n\t"
        "s_or_b64 s[92:93], s[92:93], s[84:85]\n\t"
        "s_cbranch_scc1 1b\n\t"
        "3:\n\t"
        "s_waitcnt vmcnt(0)\n\t"
        "v_add_f32 %[a0], v144, v148\n\t"
        "v_add_f32 %[a1], v146, v150\n\t"
        "v_add_f32 v140, v152, v156\n\t"
        "v_add_f32 v141, v154, v158\n\t"
        "v_add_f32 %[a0], %[a0], v140\n\t"
        "v_add_f32 %[a1], %[a1], v141\n\t"
        "s_nop 1"
        : [a0] "=&v"(a0), [a1] "=&v"(a1), [cap] "+&s"(cap) : [vo] "v"(voff), [rs] "s"(rs), [b0] "s"(base), [b1] "s"(base + 4096), [tag] "s"(tag)
        : "v140", "v141", "v142", "v143", "v144", "v145", "v146", "v147", "v148", "v149", "v150", "v151", "v152", "v153", "v154", "v155", "v156", "v157", "v158", "v159", "s78", "s79", "s80", "s81", "s82", "s83", "s84", "s85", "s92", "s93", "scc", "memory");
}
template <> __device__ __forceinline__ void sum_pairs<4, 1>(rsrc_t rs, unsigned voff, unsigned base, unsigned tag, float& a0, float& a1) {
    unsigned cap = SPIN_GLB;
    asm volatile(
        "buffer_load_dwordx4 v[144:147], %[vo], %[rs], %[b0] offen offset:0 sc1\n\t"
        "buffer_load_dwordx4 v[148:151], %[vo], %[rs], %[b0] offen offset:1024 sc1\n\t"
        "buffer_load_dwordx4 v[152:155], %[vo], %[rs], %[b0] offen offset:2048 sc1\n\t"
        "buffer_load_dwordx4 v[156:159], %[vo], %[rs], %[b0] offen offset:3072 sc1\n\t"
        "s_waitcnt vmcnt(0)\n\t"
        "v_bitop3_b32 v140, v145, v147, %[tag] bitop3:0x7e\n\t"
        "v_bitop3_b32 v141, v149, v151, %[tag] bitop3:0x7e\n\t"
        "v_bitop3_b32 v142, v153, v155, %[tag] bitop3:0x7e\n\t"
        "v_bitop3_b32 v143, v157, v159, %[tag] bitop3:0x7e\n\t"
        "v_cmp_ne_u32_e64 s[78:79], 0, v140\n\t"
        "v_cmp_ne_u32_e64 s[80:81], 0, v141\n\t"
        "v_cmp_ne_u32_e64 s[82:83], 0, v142\n\t"
        "v_cmp_ne_u32_e64 s[84:85], 0, v143\n\t"
        "s_or_b64 s[92:93], s[78:79], s[80:81]\n\t"
        "s_or_b64 s[92:93], s[92:93], s[82:83]\n\t"
        "s_or_b64 s[92:93], s[92:93], s[84:85]\n\t"
        "s_cbranch_scc0 3f\n\t"
        "s_mov_b64 exec, s[78:79]\n\t"
        "buffer_load_dwordx4 v[144:147], %[vo], %[rs], %[b0] offen offset:0 sc1\n\t"
        "s_mov_b64 exec, s[80:81]\n\t"
        "buffer_load_dwordx4 v[148:151], %[vo], %[rs], %[b0] offen offset:1024 sc1\n\t"
        "s_mov_b64 exec, s[82:83]\n\t"
        "buffer_load_dwordx4 v[152:155], %[vo], %[rs], %[b0] offen offset:2048 sc1\n\t"
        "s_mov_b64 exec, s[84:85]\n\t"
        "buffer_load_dwordx4 v[156:159], %[vo], %[rs], %[b0] offen offset:3072 sc1\n\t"
        "s_mov_b64 exec, -1\n\t"
        "s_sleep 2\n\t"
        "1:\n\t"
        "s_mov_b64 exec, s[78:79]\n\t"
        "buffer_load_dwordx4 v[144:147], %[vo], %[rs], %[b0] offen offset:0 sc1\n\t"
        "s_mov_b64 exec, s[80:81]\n\t"
        "buffer_load_dwordx4 v[148:151], %[vo], %[rs], %[b0] offen offset:1024 sc1\n\t"
        "s_mov_b64 exec, s[82:83]\n\t"
        "buffer_load_dwordx4 v[152:155], %[vo], %[rs], %[b0] offen offset:2048 sc1\n\t"
        "s_mov_b64 exec, s[84:85]\n\t"
        "buffer_load_dwordx4 v[156:159], %[vo], %[rs], %[b0] offen offset:3072 sc1\n\t"
        "s_mov_b64 exec, -1\n\t"
        "s_sub_u32 %[cap], %[cap], 1\n\t"
        "s_cbranch_scc1 3f\n\t"
        "s_waitcnt vmcnt(4)\n\t"
        "v_bitop3_b32 v140, v145, v147, %[tag] bitop3:0x7e\n\t"
        "v_bitop3_b32 v141, v149, v151, %[tag] bitop3:0x7e\n\t"
        "v_bitop3_b32 v142, v153, v155, %[tag] bitop3:0x7e\n\t"
        "v_bitop3_b32 v143, v157, v159, %[tag] bitop3:0x7e\n\t"
        "v_cmp_ne_u32_e64 s[78:79], 0, v140\n\t"
        "v_cmp_ne_u32_e64 s[80:81], 0, v141\n\t"
        "v_cmp_ne_u32_e64 s[82:83], 0, v142\n\t"
        "v_cmp_ne_u32_e64 s[84:85], 0, v143\n\t"
        "s_or_b64 s[92:93], s[78:79], s[80:81]\n\t"
        "s_or_b64 s[92:93], s[92:93], s[82:83]\n\t"
        "s_or_b64 s[92:93], s[92:93], s[84:85]\n\t"
        "s_cbranch_scc1 1b\n\t"
        "3:\n\t"
        "s_waitcnt vmcnt(0)\n\t"
        "v_add_f32 %[a0], v144, v148\n\t"
        "v_add_f32 %[a1], v146, v150\n\t"
        "v_add_f32 v140, v152, v156\n\t"
        "v_add_f32 v141, v154, v158\n\t"
        "v_add_f32 %[a0], %[a0], v140\n\t"
        "v_add_f32 %[a1], %[a1], v141\n\t"
        "s_nop 1"
        : [a0] "=&v"(a0), [a1] "=&v"(a1), [cap] "+&s"(cap) : [vo] "v"(voff), [rs] "s"(rs), [b0] "s"(base), [b1] "s"(base + 4096), [tag] "s"(tag)
        : "v140", "v141", "v142", "v143", "v144", "v145", "v146", "v147", "v148", "v149", "v150", "v151", "v152", "v153", "v154", "v155", "v156", "v157", "v158", "v159", "s78", "s79", "s80", "s81", "s82", "s83", "s84", "s85", "s92", "s93", "scc", "memory");
}
template <> __device__ __forceinline__ void sum_pairs<5, 0>(rsrc_t rs, unsigned voff, unsigned base, unsigned tag, float& a0, float& a1) {
    unsigned cap = SPIN_GLB;
    asm volatile(
        "buffer_load_dwordx4 v[144:147], %[vo], %[rs], %[b0] offen offset:0 sc1\n\t"
        "buffer_load_dwordx4 v[148:151], %[vo], %[rs], %[b0] offen offset:1024 sc1\n\t"
        "buffer_load_dwordx4 v[152:155], %[vo], %[rs], %[b0] offen offset:2048 sc1\n\t"
        "buffer_load_dwordx4 v[156:159], %[vo], %[rs], %[b0] offen offset:3072 sc1\n\t"
        "buffer_load_dwordx4 v[160:163], %[vo], %[rs], %[b1] offen offset:0 sc1\n\t"
        "s_waitcnt vmcnt(0)\n\t"
        "v_bitop3_b32 v140, v145, v147, %[tag] bitop3:0x7e\n\t"
        "v_bitop3_b32 v141, v149, v151, %[tag] bitop3:0x7e\n\t"
        "v_bitop3_b32 v142, v153, v155, %[tag] bitop3:0x7e\n\t"
        "v_bitop3_b32 v143, v157, v159, %[tag] bitop3:0x7e\n\t"
        "v_cmp_ne_u32_e64 s[78:79], 0, v140\n\t"
        "v_cmp_ne_u32_e64 s[80:81], 0, v141\n\t"
        "v_cmp_ne_u32_e64 s[82:83], 0, v142\n\t"
        "v_cmp_ne_u32_e64 s[84:85], 0, v143\n\t"
        "v_bitop3_b32 v140, v161, v163, %[tag] bitop3:0x7e\n\t"
        "v_cmp_ne_u32_e64 s[86:87], 0, v140\n\t"
        "s_or_b64 s[92:93], s[78:79], s[80:81]\n\t"
        "s_or_b64 s[92:93], s[92:93], s[82:83]\n\t"
        "s_or_b64 s[92:93], s[92:93], s[84:85]\n\t"
        "s_or_b64 s[92:93], s[92:93], s[86:87]\n\t"
        "s_cbranch_scc0 3f\n\t"
        "1:\n\t"
        "s_mov_b64 exec, s[78:79]\n\t"
        "buffer_load_dwordx4 v[144:147], %[vo], %[rs], %[b0] offen offset:0 sc1\n\t"
        "s_mov_b64 exec, s[80:81]\n\t"
        "buffer_load_dwordx4 v[148:151], %[vo], %[rs], %[b0] offen offset:1024 sc1\n\t"
        "s_mov_b64 exec, s[82:83]\n\t"
        "buffer_load_dwordx4 v[152:155], %[vo], %[rs], %[b0] offen offset:2048 sc1\n\t"
        "s_mov_b64 exec, s[84:85]\n\t"
        "buffer_load_dwordx4 v[156:159], %[vo], %[rs], %[b0] offen offset:3072 sc1\n\t"
        "s_mov_b64 exec, s[86:87]\n\t"
        "buffer_load_dwordx4 v[160:163], %[vo], %[rs], %[b1] offen offset:0 sc1\n\t"
        "s_mov_b64 exec, -1\n\t"
        "s_sub_u32 %[cap], %[cap], 1\n\t"
        "s_cbranch_scc1 3f\n\t"
        "s_waitcnt vmcnt(0)\n\t"
        "v_bitop3_b32 v140, v145, v147, %[tag] bitop3:0x7e\n\t"
        "v_bitop3_b32 v141, v149, v151, %[tag] bitop3:0x7e\n\t"
        "v_bitop3_b32 v142, v153, v155, %[tag] bitop3:0x7e\n\t"
        "v_bitop3_b32 v143, v157, v159, %[tag] bitop3:0x7e\n\t"
        "v_cmp_ne_u32_e64 s[78:79], 0, v140\n\t"
        "v_cmp_ne_u32_e64 s[80:81], 0, v141\n\t"
        "v_cmp_ne_u32_e64 s[82:83], 0, v142\n\t"
        "v_cmp_ne_u32_e64 s[84:85], 0, v143\n\t"
        "v_bitop3_b32 v140, v161, v163, %[tag] bitop3:0x7e\n\t"
        "v_cmp_ne_u32_e64 s[86:87], 0, v140\n\t"
        "s_or_b64 s[92:93], s[78:79], s[80:81]\n\t"
        "s_or_b64 s[92:93], s[92:93], s[82:83]\n\t"
        "s_or_b64 s[92:93], s[92:93], s[84:85]\n\t"
        "s_or_b64 s[92:93], s[92:93], s[86:87]\n\t"
        "s_cbranch_scc1 1b\n\t"
        "3:\n\t"
        "s_waitcnt vmcnt(0)\n\t"
        "v_add_f32 %[a0], v144, v148\n\t"
        "v_add_f32 %[a1], v146, v150\n\t"
        "v_add_f32 v140, v152, v156\n\t"
        "v_add_f32 v141, v154, v158\n\t"
        "v_add_f32 %[a0], %[a0], v140\n\t"
        "v_add_f32 %[a1], %[a1], v141\n\t"
        "v_add_f32 %[a0], %[a0], v160\n\t"
        "v_add_f32 %[a1], %[a1], v162\n\t"
        "s_nop 1"
        : [a0] "=&v"(a0), [a1] "=&v"(a1), [cap] "+&s"(cap) : [vo] "v"(voff), [rs] "s"(rs), [b0] "s"(base), [b1] "s"(base + 4096), [tag] "s"(tag)
        : "v140", "v141", "v142", "v143", "v144", "v145", "v146", "v147", "v148", "v149", "v150", "v151", "v152", "v153", "v154", "v155", "v156", "v157", "v158", "v159", "v160", "v161", "v162", "v163", "s78", "s79", "s80", "s81", "s82", "s83", "s84", "s85", "s86", "s87", "s92", "s93", "scc", "memory");
}
template <> __device__ __forceinline__ void sum_pairs<5, 1>(rsrc_t rs, unsigned voff, unsigned base, unsigned tag, float& a0, float& a1) {
    unsigned cap = SPIN_GLB;
    asm volatile(
        "buffer_load_dwordx4 v[144:147], %[vo], %[rs], %[b0] offen offset:0 sc1\n\t"
        "buffer_load_dwordx4 v[148:151], %[vo], %[rs], %[b0] offen offset:1024 sc1\n\t"
        "buffer_load_dwordx4 v[152:155], %[vo], %[rs], %[b0] offen offset:2048 sc1\n\t"
        "buffer_load_dwordx4 v[156:159], %[vo], %[rs], %[b0] offen offset:3072 sc1\n\t"
        "buffer_load_dwordx4 v[160:163], %[vo], %[rs], %[b1] offen offset:0 sc1\n\t"
        "s_waitcnt vmcnt(0)\n\t"
        "v_bitop3_b32 v140, v145, v147, %[tag] bitop3:0x7e\n\t"
        "v_bitop3_b32 v141, v149, v151, %[tag] bitop3:0x7e\n\t"
        "v_bitop3_b32 v142, v153, v155, %[tag] bitop3:0x7e\n\t"
        "v_bitop3_b32 v143, v157, v159, %[tag] bitop3:0x7e\n\t"
        "v_cmp_ne_u32_e64 s[78:79], 0, v140\n\t"
        "v_cmp_ne_u32_e64 s[80:81], 0, v141\n\t"
        "v_cmp_ne_u32_e64 s[82:83], 0, v142\n\t"
        "v_cmp_ne_u32_e64 s[84:85], 0, v143\n\t"
        "v_bitop3_b32 v140, v161, v163, %[tag] bitop3:0x7e\n\t"
        "v_cmp_ne_u32_e64 s[86:87], 0, v140\n\t"
        "s_or_b64 s[92:93], s[78:79], s[80:81]\n\t"
        "s_or_b64 s[92:93], s[92:93], s[82:83]\n\t"
        "s_or_b64 s[92:93], s[92:93], s[84:85]\n\t"
        "s_or_b64 s[92:93], s[92:93], s[86:87]\n\t"
        "s_cbranch_scc0 3f\n\t"
        "s_mov_b64 exec, s[78:79]\n\t"
        "buffer_load_dwordx4 v[144:147], %[vo], %[rs], %[b0] offen offset:0 sc1\n\t"
        "s_mov_b64 exec, s[80:81]\n\t"
        "buffer_load_dwordx4 v[148:151], %[vo], %[rs], %[b0] offen offset:1024 sc1\n\t"
        "s_mov_b64 exec, s[82:83]\n\t"
        "buffer_load_dwordx4 v[152:155], %[vo], %[rs], %[b0] offen offset:2048 sc1\n\t"
        "s_mov_b64 exec, s[84:85]\n\t"
        "buffer_load_dwordx4 v[156:159], %[vo], %[rs], %[b0] offen offset:3072 sc1\n\t"
        "s_mov_b64 exec, s[86:87]\n\t"
        "buffer_load_dwordx4 v[160:163], %[vo], %[rs], %[b1] offen offset:0 sc1\n\t"
        "s_mov_b64 exec, -1\n\t"
        "s_sleep 2\n\t"
        "1:\n\t"
        "s_mov_b64 exec, s[78:79]\n\t"
        "buffer_load_dwordx4 v[144:147], %[vo], %[rs], %[b0] offen offset:0 sc1\n\t"
        "s_mov_b64 exec, s[80:81]\n\t"
        "buffer_load_dwordx4 v[148:151], %[vo], %[rs], %[b0] offen offset:1024 sc1\n\t"
        "s_mov_b64 exec, s[82:83]\n\t"
        "buffer_load_dwordx4 v[152:155], %[vo], %[rs], %[b0] offen offset:2048 sc1\n\t"
        "s_mov_b64 exec, s[84:85]\n\t"
        "buffer_load_dwordx4 v[156:159], %[vo], %[rs], %[b0] offen offset:3072 sc1\n\t"
        "s_mov_b64 exec, s[86:87]\n\t"
        "buffer_load_dwordx4 v[160:163], %[vo], %[rs], %[b1] offen offset:0 sc1\n\t"
        "s_mov_b64 exec, -1\n\t"
        "s_sub_u32 %[cap], %[cap], 1\n\t"
        "s_cbranch_scc1 3f\n\t"
        "s_waitcnt vmcnt(5)\n\t"
        "v_bitop3_b32 v140, v145, v147, %[tag] bitop3:0x7e\n\t"
        "v_bitop3_b32 v141, v149, v151, %[tag] bitop3:0x7e\n\t"
        "v_bitop3_b32 v142, v153, v155, %[tag] bitop3:0x7e\n\t"
        "v_bitop3_b32 v143, v157, v159, %[tag] bitop3:0x7e\n\t"
        "v_cmp_ne_u32_e64 s[78:79], 0, v140\n\t"
        "v_cmp_ne_u32_e64 s[80:81], 0, v141\n\t"
        "v_cmp_ne_u32_e64 s[82:83], 0, v142\n\t"
        "v_cmp_ne_u32_e64 s[84:85], 0, v143\n\t"
        "v_bitop3_b32 v140, v161, v163, %[tag] bitop3:0x7e\n\t"
        "v_cmp_ne_u32_e64 s[86:87], 0, v140\n\t"
        "s_or_b64 s[92:93], s[78:79], s[80:81]\n\t"
        "s_or_b64 s[92:93], s[92:93], s[82:83]\n\t"
        "s_or_b64 s[92:93], s[92:93], s[84:85]\n\t"
        "s_or_b64 s[92:93], s[92:93], s[86:87]\n\t"
        "s_cbranch_scc1 1b\n\t"
        "3:\n\t"
        "s_waitcnt vmcnt(0)\n\t"
        "v_add_f32 %[a0], v144, v148\n\t"
        "v_add_f32 %[a1], v146, v150\n\t"
        "v_add_f32 v140, v152, v156\n\t"
        "v_add_f32 v141, v154, v158\n\t"
        "v_add_f32 %[a0], %[a0], v140\n\t"
        "v_add_f32 %[a1], %[a1], v141\n\t"
        "v_add_f32 %[a0], %[a0], v160\n\t"
        "v_add_f32 %[a1], %[a1], v162\n\t"
        "s_nop 1"
        : [a0] "=&v"(a0), [a1] "=&v"(a1), [cap] "+&s"(cap) : [vo] "v"(voff), [rs] "s"(rs), [b0] "s"(base), [b1] "s"(base + 4096), [tag] "s"(tag)
        : "v140", "v141", "v142", "v143", "v144", "v145", "v146", "v147", "v148", "v149", "v150", "v151", "v152", "v153", "v154", "v155", "v156", "v157", "v158", "v159", "v160", "v161", "v162", "v163", "s78", "s79", "s80", "s81", "s82", "s83", "s84", "s85", "s86", "s87", "s92", "s93", "scc", "memory");
}
template <> __device__ __forceinline__ void sum_pairs<6, 0>(rsrc_t rs, unsigned voff, unsigned base, unsigned tag, float& a0, float& a1) {
    unsigned cap = SPIN_GLB;
    asm volatile(
        "buffer_load_dwordx4 v[144:147], %[vo], %[rs], %[b0] offen offset:0 sc1\n\t"
        "buffer_load_dwordx4 v[148:151], %[vo], %[rs], %[b0] offen offset:1024 sc1\n\t"
        "buffer_load_dwordx4 v[152:155], %[vo], %[rs], %[b0] offen offset:2048 sc1\n\t"
        "buffer_load_dwordx4 v[156:159], %[vo], %[rs], %[b0] offen offset:3072 sc1\n\t"
        "buffer_load_dwordx4 v[160:163], %[vo], %[rs], %[b1] offen offset:0 sc1\n\t"
        "buffer_load_dwordx4 v[164:167], %[vo], %[rs], %[b1] offen offset:1024 sc1\n\t"
        "s_waitcnt vmcnt(0)\n\t"
        "v_bitop3_b32 v140, v145, v147, %[tag] bitop3:0x7e\n\t"
        "v_bitop3_b32 v141, v149, v151, %[tag] bitop3:0x7e\n\t"
        "v_bitop3_b32 v142, v153, v155, %[tag] bitop3:0x7e\n\t"
        "v_bitop3_b32 v143, v157, v159, %[tag] bitop3:0x7e\n\t"
        "v_cmp_ne_u32_e64 s[78:79], 0, v140\n\t"
        "v_cmp_ne_u32_e64 s[80:81], 0, v141\n\t"
        "v_cmp_ne_u32_e64 s[82:83], 0, v142\n\t"
        "v_cmp_ne_u32_e64 s[84:85], 0, v143\n\t"
        "v_bitop3_b32 v140, v161, v163, %[tag] bitop3:0x7e\n\t"
        "v_bitop3_b32 v141, v165, v167, %[tag] bitop3:0x7e\n\t"
        "v_cmp_ne_u32_e64 s[86:87], 0, v140\n\t"
        "v_cmp_ne_u32_e64 s[88:89], 0, v141\n\t"
        "s_or_b64 s[92:93], s[78:79], s[80:81]\n\t"
        "s_or_b64 s[92:93], s[92:93], s[82:83]\n\t"
        "s_or_b64 s[92:93], s[92:93], s[84:85]\n\t"
        "s_or_b64 s[92:93], s[92:93], s[86:87]\n\t"
        "s_or_b64 s[92:93], s[92:93], s[88:89]\n\t"
        "s_cbranch_scc0 3f\n\t"
        "1:\n\t"
        "s_mov_b64 exec, s[78:79]\n\t"
        "buffer_load_dwordx4 v[144:147], %[vo], %[rs], %[b0] offen offset:0 sc1\n\t"
        "s_mov_b64 exec, s[80:81]\n\t"
        "buffer_load_dwordx4 v[148:151], %[vo], %[rs], %[b0] offen offset:1024 sc1\n\t"
        "s_mov_b64 exec, s[82:83]\n\t"
        "buffer_load_dwordx4 v[152:155], %[vo], %[rs], %[b0] offen offset:2048 sc1\n\t"
        "s_mov_b64 exec, s[84:85]\n\t"
        "buffer_load_dwordx4 v[156:159], %[vo], %[rs], %[b0] offen offset:3072 sc1\n\t"
        "s_mov_b64 exec, s[86:87]\n\t"
        "buffer_load_dwordx4 v[160:163], %[vo], %[rs], %[b1] offen offset:0 sc1\n\t"
        "s_mov_b64 exec, s[88:89]\n\t"
        "buffer_load_dwordx4 v[164:167], %[vo], %[rs], %[b1] offen offset:1024 sc1\n\t"
        "s_mov_b64 exec, -1\n\t"
        "s_sub_u32 %[cap], %[cap], 1\n\t"
        "s_cbranch_scc1 3f\n\t"
        "s_waitcnt vmcnt(0)\n\t"
        "v_bitop3_b32 v140, v145, v147, %[tag] bitop3:0x7e\n\t"
        "v_bitop3_b32 v141, v149, v151, %[tag] bitop3:0x7e\n\t"
        "v_bitop3_b32 v142, v153, v155, %[tag] bitop3:0x7e\n\t"
        "v_bitop3_b32 v143, v157, v159, %[tag] bitop3:0x7e\n\t"
        "v_cmp_ne_u32_e64 s[78:79], 0, v140\n\t"
        "v_cmp_ne_u32_e64 s[80:81], 0, v141\n\t"
        "v_cmp_ne_u32_e64 s[82:83], 0, v142\n\t"
        "v_cmp_ne_u32_e64 s[84:85], 0, v143\n\t"
        "v_bitop3_b32 v140, v161, v163, %[tag] bitop3:0x7e\n\t"
        "v_bitop3_b32 v141, v165, v167, %[tag] bitop3:0x7e\n\t"
        "v_cmp_ne_u32_e64 s[86:87], 0, v140\n\t"
        "v_cmp_ne_u32_e64 s[88:89], 0, v141\n\t"
        "s_or_b64 s[92:93], s[78:79], s[80:81]\n\t"
        "s_or_b64 s[92:93], s[92:93], s[82:83]\n\t"
        "s_or_b64 s[92:93], s[92:93], s[84:85]\n\t"
        "s_or_b64 s[92:93], s[92:93], s[86:87]\n\t"
        "s_or_b64 s[92:93], s[92:93], s[88:89]\n\t"
        "s_cbranch_scc1 1b\n\t"
        "3:\n\t"
        "s_waitcnt vmcnt(0)\n\t"
        "v_add_f32 %[a0], v144, v148\n\t"
        "v_add_f32 %[a1], v146, v150\n\t"
        "v_add_f32 v140, v152, v156\n\t"
        "v_add_f32 v141, v154, v158\n\t"
        "v_add_f32 v142, v160, v164\n\t"
        "v_add_f32 v143, v162, v166\n\t"
        "v_add_f32 %[a0], %[a0], v140\n\t"
        "v_add_f32 %[a1], %[a1], v141\n\t"
        "v_add_f32 %[a0], %[a0], v142\n\t"
        "v_add_f32 %[a1], %[a1], v143\n\t"
        "s_nop 1"
        : [a0] "=&v"(a0), [a1] "=&v"(a1), [cap] "+&s"(cap) : [vo] "v"(voff), [rs] "s"(rs), [b0] "s"(base), [b1] "s"(base + 4096), [tag] "s"(tag)
        : "v140", "v141", "v142", "v143", "v144", "v145", "v146", "v147", "v148", "v149", "v150", "v151", "v152", "v153", "v154", "v155", "v156", "v157", "v158", "v159", "v160", "v161", "v162", "v163", "v164", "v165", "v166", "v167", "s78", "s79", "s80", "s81", "s82", "s83", "s84", "s85", "s86", "s87", "s88", "s89", "s92", "s93", "scc", "memory");
}
template <> __device__ __forceinline__ void sum_pairs<6, 1>(rsrc_t rs, unsigned voff, unsigned base, unsigned tag, float& a0, float& a1) {
    unsigned cap = SPIN_GLB;
    asm volatile(
        "buffer_load_dwordx4 v[144:147], %[vo], %[rs], %[b0] offen offset:0 sc1\n\t"
        "buffer_load_dwordx4 v[148:151], %[vo], %[rs], %[b0] offen offset:1024 sc1\n\t"
        "buffer_load_dwordx4 v[152:155], %[vo], %[rs], %[b0] offen offset:2048 sc1\n\t"
        "buffer_load_dwordx4 v[156:159], %[vo], %[rs], %[b0] offen offset:3072 sc1\n\t"
        "buffer_load_dwordx4 v[160:163], %[vo], %[rs], %[b1] offen offset:0 sc1\n\t"
        "buffer_load_dwordx4 v[164:167], %[vo], %[rs], %[b1] offen offset:1024 sc1\n\t"
        "s_waitcnt vmcnt(0)\n\t"
        "v_bitop3_b32 v140, v145, v147, %[tag] bitop3:0x7e\n\t"
        "v_bitop3_b32 v141, v149, v151, %[tag] bitop3:0x7e\n\t"
        "v_bitop3_b32 v142, v153, v155, %[tag] bitop3:0x7e\n\t"
        "v_bitop3_b32 v143, v157, v159, %[tag] bitop3:0x7e\n\t"
        "v_cmp_ne_u32_e64 s[78:79], 0, v140\n\t"
        "v_cmp_ne_u32_e64 s[80:81], 0, v141\n\t"
        "v_cmp_ne_u32_e64 s[82:83], 0, v142\n\t"
        "v_cmp_ne_u32_e64 s[84:85], 0, v143\n\t"
        "v_bitop3_b32 v140, v161, v163, %[tag] bitop3:0x7e\n\t"
        "v_bitop3_b32 v141, v165, v167, %[tag] bitop3:0x7e\n\t"
        "v_cmp_ne_u32_e64 s[86:87], 0, v140\n\t"
        "v_cmp_ne_u32_e64 s[88:89], 0, v141\n\t"
        "s_or_b64 s[92:93], s[78:79], s[80:81]\n\t"
        "s_or_b64 s[92:93], s[92:93], s[82:83]\n\t"
        "s_or_b64 s[92:93], s[92:93], s[84:85]\n\t"
        "s_or_b64 s[92:93], s[92:93], s[86:87]\n\t"
        "s_or_b64 s[92:93], s[92:93], s[88:89]\n\t"
        "s_cbranch_scc0 3f\n\t"
        "s_mov_b64 exec, s[78:79]\n\t"
        "buffer_load_dwordx4 v[144:147], %[vo], %[rs], %[b0] offen offset:0 sc1\n\t"
        "s_mov_b64 exec, s[80:81]\n\t"
        "buffer_load_dwordx4 v[148:151], %[vo], %[rs], %[b0] offen offset:1024 sc1\n\t"
        "s_mov_b64 exec, s[82:83]\n\t"
        "buffer_load_dwordx4 v[152:155], %[vo], %[rs], %[b0] offen offset:2048 sc1\n\t"
        "s_mov_b64 exec, s[84:85]\n\t"
        "buffer_load_dwordx4 v[156:159], %[vo], %[rs], %[b0] offen offset:3072 sc1\n\t"
        "s_mov_b64 exec, s[86:87]\n\t"
        "buffer_load_dwordx4 v[160:163], %[vo], %[rs], %[b1] offen offset:0 sc1\n\t"
        "s_mov_b64 exec, s[88:89]\n\t"
        "buffer_load_dwordx4 v[164:167], %[vo], %[rs], %[b1] offen offset:1024 sc1\n\t"
        "s_mov_b64 exec, -1\n\t"
        "s_sleep 2\n\t"
        "1:\n\t"
        "s_mov_b64 exec, s[78:79]\n\t"
        "buffer_load_dwordx4 v[144:147], %[vo], %[rs], %[b0] offen offset:0 sc1\n\t"
        "s_mov_b64 exec, s[80:81]\n\t"
        "buffer_load_dwordx4 v[148:151], %[vo], %[rs], %[b0] offen offset:1024 sc1\n\t"
        "s_mov_b64 exec, s[82:83]\n\t"
        "buffer_load_dwordx4 v[152:155], %[vo], %[rs], %[b0] offen offset:2048 sc1\n\t"
        "s_mov_b64 exec, s[84:85]\n\t"
        "buffer_load_dwordx4 v[156:159], %[vo], %[rs], %[b0] offen offset:3072 sc1\n\t"
        "s_mov_b64 exec, s[86:87]\n\t"
        "buffer_load_dwordx4 v[160:163], %[vo], %[rs], %[b1] offen offset:0 sc1\n\t"
        "s_mov_b64 exec, s[88:89]\n\t"
        "buffer_load_dwordx4 v[164:167], %[vo], %[rs], %[b1] offen offset:1024 sc1\n\t"
        "s_mov_b64 exec, -1\n\t"
        "s_sub_u32 %[cap], %[cap], 1\n\t"
        "s_cbranch_scc1 3f\n\t"
        "s_waitcnt vmcnt(6)\n\t"
        "v_bitop3_b32 v140, v145, v147, %[tag] bitop3:0x7e\n\t"
        "v_bitop3_b32 v141, v149, v151, %[tag] bitop3:0x7e\n\t"
        "v_bitop3_b32 v142, v153, v155, %[tag] bitop3:0x7e\n\t"
        "v_bitop3_b32 v143, v157, v159, %[tag] bitop3:0x7e\n\t"
        "v_cmp_ne_u32_e64 s[78:79], 0, v140\n\t"
        "v_cmp_ne_u32_e64 s[80:81], 0, v141\n\t"
        "v_cmp_ne_u32_e64 s[82:83], 0, v142\n\t"
        "v_cmp_ne_u32_e64 s[84:85], 0, v143\n\t"
        "v_bitop3_b32 v140, v161, v163, %[tag] bitop3:0x7e\n\t"
        "v_bitop3_b32 v141, v165, v167, %[tag] bitop3:0x7e\n\t"
        "v_cmp_ne_u32_e64 s[86:87], 0, v140\n\t"
        "v_cmp_ne_u32_e64 s[88:89], 0, v141\n\t"
        "s_or_b64 s[92:93], s[78:79], s[80:81]\n\t"
        "s_or_b64 s[92:93], s[92:93], s[82:83]\n\t"
        "s_or_b64 s[92:93], s[92:93], s[84:85]\n\t"
        "s_or_b64 s[92:93], s[92:93], s[86:87]\n\t"
        "s_or_b64 s[92:93], s[92:93], s[88:89]\n\t"
        "s_cbranch_scc1 1b\n\t"
        "3:\n\t"
        "s_waitcnt vmcnt(0)\n\t"
        "v_add_f32 %[a0], v144, v148\n\t"
        "v_add_f32 %[a1], v146, v150\n\t"
        "v_add_f32 v140, v152, v156\n\t"
        "v_add_f32 v141, v154, v158\n\t"
        "v_add_f32 v142, v160, v164\n\t"
        "v_add_f32 v143, v162, v166\n\t"
        "v_add_f32 %[a0], %[a0], v140\n\t"
        "v_add_f32 %[a1], %[a1], v141\n\t"
        "v_add_f32 %[a0], %[a0], v142\n\t"
        "v_add_f32 %[a1], %[a1], v143\n\t"
        "s_nop 1"
        : [a0] "=&v"(a0), [a1] "=&v"(a1), [cap] "+&s"(cap) : [vo] "v"(voff), [rs] "s"(rs), [b0] "s"(base), [b1] "s"(base + 4096), [tag] "s"(tag)
        : "v140", "v141", "v142", "v143", "v144", "v145", "v146", "v147", "v148", "v149", "v150", "v151", "v152", "v153", "v154", "v155", "v156", "v157", "v158", "v159", "v160", "v161", "v162", "v163", "v164", "v165", "v166", "v167", "s78", "s79", "s80", "s81", "s82", "s83", "s84", "s85", "s86", "s87", "s88", "s89", "s92", "s93", "scc", "memory");
}
template <> __device__ __forceinline__ float combine_o<0>(rsrc_t rs, unsigned voff, unsigned base, unsigned tag, int j, unsigned own, f32x4 w0, f32x4 w1, unsigned napn) {
    unsigned cap = SPIN_GLB;
    float o;
    asm volatile(
        "s_cmp_eq_u32 %[j], 0\n\t"
        "s_cselect_b64 s[74:75], 0, %[lm]\n\t"
        "v_mov_b32 v144, %[own]\n\t"
        "v_mov_b32 v145, %[tag]\n\t"
        "s_cmp_eq_u32 %[j], 1\n\t"
        "s_cselect_b64 s[76:77], 0, %[lm]\n\t"
        "v_mov_b32 v146, %[own]\n\t"
        "v_mov_b32 v147, %[tag]\n\t"
        "s_cmp_eq_u32 %[j], 2\n\t"
        "s_cselect_b64 s[78:79], 0, %[lm]\n\t"
        "v_mov_b32 v148, %[own]\n\t"
        "v_mov_b32 v149, %[tag]\n\t"
        "s_cmp_eq_u32 %[j], 3\n\t"
        "s_cselect_b64 s[80:81], 0, %[lm]\n\t"
        "v_mov_b32 v150, %[own]\n\t"
        "v_mov_b32 v151, %[tag]\n\t"
        "s_cmp_eq_u32 %[j], 4\n\t"
        "s_cselect_b64 s[82:83], 0, %[lm]\n\t"
        "v_mov_b32 v152, %[own]\n\t"
        "v_mov_b32 v153, %[tag]\n\t"
        "s_cmp_eq_u32 %[j], 5\n\t"
        "s_cselect_b64 s[84:85], 0, %[lm]\n\t"
        "v_mov_b32 v154, %[own]\n\t"
        "v_mov_b32 v155, %[tag]\n\t"
        "s_cmp_eq_u32 %[j], 6\n\t"
        "s_cselect_b64 s[86:87], 0, %[lm]\n\t"
        "v_mov_b32 v156, %[own]\n\t"
        "v_mov_b32 v157, %[tag]\n\t"
        "s_cmp_eq_u32 %[j], 7\n\t"
        "s_cselect_b64 s[88:89], 0, %[lm]\n\t"
        "v_mov_b32 v158, %[own]\n\t"
        "v_mov_b32 v159, %[tag]\n\t"
        "s_cmp_eq_u32 %[nap], 0\n\t"
        "s_cbranch_scc1 5f\n\t"
        "4:\n\t"
        "s_sleep 1\n\t"
        "s_sub_u32 %[nap], %[nap], 1\n\t"
        "s_cmp_lg_u32 %[nap], 0\n\t"
        "s_cbranch_scc1 4b\n\t"
        "5:\n\t"
        "s_mov_b64 exec, s[74:75]\n\t"
        "buffer_load_dwordx2 v[144:145], %[vo], %[rs], %[b0] offen offset:0 sc1\n\t"
        "s_mov_b64 exec, s[76:77]\n\t"
        "buffer_load_dwordx2 v[146:147], %[vo], %[rs], %[b0] offen offset:1536 sc1\n\t"
        "s_mov_b64 exec, s[78:79]\n\t"
        "buffer_load_dwordx2 v[148:149], %[vo], %[rs], %[b0] offen offset:3072 sc1\n\t"
        "s_mov_b64 exec, s[80:81]\n\t"
        "buffer_load_dwordx2 v[150:151], %[vo], %[rs], %[b1] offen offset:0 sc1\n\t"
        "s_mov_b64 exec, s[82:83]\n\t"
        "buffer_load_dwordx2 v[152:153], %[vo], %[rs], %[b1] offen offset:1536 sc1\n\t"
        "s_mov_b64 exec, s[84:85]\n\t"
        "buffer_load_dwordx2 v[154:155], %[vo], %[rs], %[b1] offen offset:3072 sc1\n\t"
        "s_mov_b64 exec, s[86:87]\n\t"
        "buffer_load_dwordx2 v[156:157], %[vo], %[rs], %[b2] offen offset:0 sc1\n\t"
        "s_mov_b64 exec, s[88:89]\n\t"
        "buffer_load_dwordx2 v[158:159], %[vo], %[rs], %[b2] offen offset:1536 sc1\n\t"
        "s_mov_b64 exec, -1\n\t"
        "s_waitcnt vmcnt(0)\n\t"
        "v_cmp_ne_u32_e64 s[74:75], %[tag], v145\n\t"
        "v_cmp_ne_u32_e64 s[76:77], %[tag], v147\n\t"
        "v_cmp_ne_u32_e64 s[78:79], %[tag], v149\n\t"
        "v_cmp_ne_u32_e64 s[80:81], %[tag], v151\n\t"
        "v_cmp_ne_u32_e64 s[82:83], %[tag], v153\n\t"
        "v_cmp_ne_u32_e64 s[84:85], %[tag], v155\n\t"
        "v_cmp_ne_u32_e64 s[86:87], %[tag], v157\n\t"
        "v_cmp_ne_u32_e64 s[88:89], %[tag], v159\n\t"
        "s_or_b64 s[92:93], s[74:75], s[76:77]\n\t"
        "s_or_b64 s[92:93], s[92:93], s[78:79]\n\t"
        "s_or_b64 s[92:93], s[92:93], s[80:81]\n\t"
        "s_or_b64 s[92:93], s[92:93], s[82:83]\n\t"
        "s_or_b64 s[92:93], s[92:93], s[84:85]\n\t"
        "s_or_b64 s[92:93], s[92:93], s[86:87]\n\t"
        "s_or_b64 s[92:93], s[92:93], s[88:89]\n\t"
        "s_cbranch_scc0 3f\n\t"
        "1:\n\t"
        "s_mov_b64 exec, s[74:75]\n\t"
        "buffer_load_dwordx2 v[144:145], %[vo], %[rs], %[b0] offen offset:0 sc1\n\t"
        "s_mov_b64 exec, s[76:77]\n\t"
        "buffer_load_dwordx2 v[146:147], %[vo], %[rs], %[b0] offen offset:1536 sc1\n\t"
        "s_mov_b64 exec, s[78:79]\n\t"
        "buffer_load_dwordx2 v[148:149], %[vo], %[rs], %[b0] offen offset:3072 sc1\n\t"
        "s_mov_b64 exec, s[80:81]\n\t"
        "buffer_load_dwordx2 v[150:151], %[vo], %[rs], %[b1] offen offset:0 sc1\n\t"
        "s_mov_b64 exec, s[82:83]\n\t"
        "buffer_load_dwordx2 v[152:153], %[vo], %[rs], %[b1] offen offset:1536 sc1\n\t"
        "s_mov_b64 exec, s[84:85]\n\t"
        "buffer_load_dwordx2 v[154:155], %[vo], %[rs], %[b1] offen offset:3072 sc1\n\t"
        "s_mov_b64 exec, s[86:87]\n\t"
        "buffer_load_dwordx2 v[156:157], %[vo], %[rs], %[b2] offen offset:0 sc1\n\t"
        "s_mov_b64 exec, s[88:89]\n\t"
        "buffer_load_dwordx2 v[158:159], %[vo], %[rs], %[b2] offen offset:1536 sc1\n\t"
        "s_mov_b64 exec, -1\n\t"
        "s_sub_u32 %[cap], %[cap], 1\n\t"
        "s_cbranch_scc1 3f\n\t"
        "s_waitcnt vmcnt(0)\n\t"
        "v_cmp_ne_u32_e64 s[74:75], %[tag], v145\n\t"
        "v_cmp_ne_u32_e64 s[76:77], %[tag], v147\n\t"
        "v_cmp_ne_u32_e64 s[78:79], %[tag], v149\n\t"
        "v_cmp_ne_u32_e64 s[80:81], %[tag], v151\n\t"
        "v_cmp_ne_u32_e64 s[82:83], %[tag], v153\n\t"
        "v_cmp_ne_u32_e64 s[84:85], %[tag], v155\n\t"
        "v_cmp_ne_u32_e64 s[86:87], %[tag], v157\n\t"
        "v_cmp_ne_u32_e64 s[88:89], %[tag], v159\n\t"
        "s_or_b64 s[92:93], s[74:75], s[76:77]\n\t"
        "s_or_b64 s[92:93], s[92:93], s[78:79]\n\t"
        "s_or_b64 s[92:93], s[92:93], s[80:81]\n\t"
        "s_or_b64 s[92:93], s[92:93], s[82:83]\n\t"
        "s_or_b64 s[92:93], s[92:93], s[84:85]\n\t"
        "s_or_b64 s[92:93], s[92:93], s[86:87]\n\t"
        "s_or_b64 s[92:93], s[92:93], s[88:89]\n\t"
        "s_cbranch_scc1 1b\n\t"
        "3:\n\t"
        "s_waitcnt vmcnt(0)\n\t"
        "v_mul_f32 %[o], %[w0], v144\n\t"
        "v_mul_f32 v140, %[w4], v152\n\t"
        "v_fmac_f32 %[o], %[w1], v146\n\t"
        "v_fmac_f32 v140, %[w5], v154\n\t"
        "v_fmac_f32 %[o], %[w2], v148\n\t"
        "v_fmac_f32 v140, %[w6], v156\n\t"
        "v_fmac_f32 %[o], %[w3], v150\n\t"
        "v_fmac_f32 v140, %[w7], v158\n\t"
        "v_add_f32 %[o], %[o], v140\n\t"
        : [o] "=&v"(o), [cap] "+&s"(cap), [nap] "+&s"(napn)
        : [vo] "v"(voff), [rs] "s"(rs), [b0] "s"(base), [b1] "s"(base + 4608), [b2] "s"(base + 9216), [tag] "s"(tag), [j] "s"(j), [own] "v"(own),
          [lm] "s"(0xFFFFFFFFFFFFull), [w0] "v"(w0[0]), [w1] "v"(w0[1]), [w2] "v"(w0[2]), [w3] "v"(w0[3]), [w4] "v"(w1[0]), [w5] "v"(w1[1]), [w6] "v"(w1[2]), [w7] "v"(w1[3])
        : "v140", "v144", "v145", "v146", "v147", "v148", "v149", "v150", "v151", "v152", "v153", "v154", "v155", "v156", "v157", "v158", "v159", "s74", "s75", "s76", "s77", "s78", "s79", "s80", "s81", "s82", "s83", "s84", "s85", "s86", "s87", "s88", "s89", "s92", "s93", "scc", "memory");
    return o;
}
template <> __device__ __forceinline__ float combine_o<1>(rsrc_t rs, unsigned voff, unsigned base, unsigned tag, int j, unsigned own, f32x4 w0, f32x4 w1, unsigned napn) {
    unsigned cap = SPIN_GLB;
    float o;
    asm volatile(
        "s_cmp_eq_u32 %[j], 0\n\t"
        "s_cselect_b64 s[74:75], 0, %[lm]\n\t"
        "v_mov_b32 v144, %[own]\n\t"
        "v_mov_b32 v145, %[tag]\n\t"
        "s_cmp_eq_u32 %[j], 1\n\t"
        "s_cselect_b64 s[76:77], 0, %[lm]\n\t"
        "v_mov_b32 v146, %[own]\n\t"
        "v_mov_b32 v147, %[tag]\n\t"
        "s_cmp_eq_u32 %[j], 2\n\t"
        "s_cselect_b64 s[78:79], 0, %[lm]\n\t"
        "v_mov_b32 v148, %[own]\n\t"
        "v_mov_b32 v149, %[tag]\n\t"
        "s_cmp_eq_u32 %[j], 3\n\t"
        "s_cselect_b64 s[80:81], 0, %[lm]\n\t"
        "v_mov_b32 v150, %[own]\n\t"
        "v_mov_b32 v151, %[tag]\n\t"
        "s_cmp_eq_u32 %[j], 4\n\t"
        "s_cselect_b64 s[82:83], 0, %[lm]\n\t"
        "v_mov_b32 v152, %[own]\n\t"
        "v_mov_b32 v153, %[tag]\n\t"
        "s_cmp_eq_u32 %[j], 5\n\t"
        "s_cselect_b64 s[84:85], 0, %[lm]\n\t"
        "v_mov_b32 v154, %[own]\n\t"
        "v_mov_b32 v155, %[tag]\n\t"
        "s_cmp_eq_u32 %[j], 6\n\t"
        "s_cselect_b64 s[86:87], 0, %[lm]\n\t"
        "v_mov_b32 v156, %[own]\n\t"
        "v_mov_b32 v157, %[tag]\n\t"
        "s_cmp_eq_u32 %[j], 7\n\t"
        "s_cselect_b64 s[88:89], 0, %[lm]\n\t"
        "v_mov_b32 v158, %[own]\n\t"
        "v_mov_b32 v159, %[tag]\n\t"
        "s_cmp_eq_u32 %[nap], 0\n\t"
        "s_cbranch_scc1 5f\n\t"
        "4:\n\t"
        "s_sleep 1\n\t"
        "s_sub_u32 %[nap], %[nap], 1\n\t"
        "s_cmp_lg_u32 %[nap], 0\n\t"
        "s_cbranch_scc1 4b\n\t"
        "5:\n\t"
        "s_mov_b64 exec, s[74:75]\n\t"
        "buffer_load_dwordx2 v[144:145], %[vo], %[rs], %[b0] offen offset:0 sc1\n\t"
        "s_mov_b64 exec, s[76:77]\n\t"
        "buffer_load_dwordx2 v[146:147], %[vo], %[rs], %[b0] offen offset:1536 sc1\n\t"
        "s_mov_b64 exec, s[78:79]\n\t"
        "buffer_load_dwordx2 v[148:149], %[vo], %[rs], %[b0] offen offset:3072 sc1\n\t"
        "s_mov_b64 exec, s[80:81]\n\t"
        "buffer_load_dwordx2 v[150:151], %[vo], %[rs], %[b1] offen offset:0 sc1\n\t"
        "s_mov_b64 exec, s[82:83]\n\t"
        "buffer_load_dwordx2 v[152:153], %[vo], %[rs], %[b1] offen offset:1536 sc1\n\t"
        "s_mov_b64 exec, s[84:85]\n\t"
        "buffer_load_dwordx2 v[154:155], %[vo], %[rs], %[b1] offen offset:3072 sc1\n\t"
        "s_mov_b64 exec, s[86:87]\n\t"
        "buffer_load_dwordx2 v[156:157], %[vo], %[rs], %[b2] offen offset:0 sc1\n\t"
        "s_mov_b64 exec, s[88:89]\n\t"
        "buffer_load_dwordx2 v[158:159], %[vo], %[rs], %[b2] offen offset:1536 sc1\n\t"
        "s_mov_b64 exec, -1\n\t"
        "s_waitcnt vmcnt(0)\n\t"
        "v_cmp_ne_u32_e64 s[74:75], %[tag], v145\n\t"
        "v_cmp_ne_u32_e64 s[76:77], %[tag], v147\n\t"
        "v_cmp_ne_u32_e64 s[78:79], %[tag], v149\n\t"
        "v_cmp_ne_u32_e64 s[80:81], %[tag], v151\n\t"
        "v_cmp_ne_u32_e64 s[82:83], %[tag], v153\n\t"
        "v_cmp_ne_u32_e64 s[84:85], %[tag], v155\n\t"
        "v_cmp_ne_u32_e64 s[86:87], %[tag], v157\n\t"
        "v_cmp_ne_u32_e64 s[88:89], %[tag], v159\n\t"
        "s_or_b64 s[92:93], s[74:75], s[76:77]\n\t"
        "s_or_b64 s[92:93], s[92:93], s[78:79]\n\t"
        "s_or_b64 s[92:93], s[92:93], s[80:81]\n\t"
        "s_or_b64 s[92:93], s[92:93], s[82:83]\n\t"
        "s_or_b64 s[92:93], s[92:93], s[84:85]\n\t"
        "s_or_b64 s[92:93], s[92:93], s[86:87]\n\t"
        "s_or_b64 s[92:93], s[92:93], s[88:89]\n\t"
        "s_cbranch_scc0 3f\n\t"
        "s_mov_b64 exec, s[74:75]\n\t"
        "buffer_load_dwordx2 v[144:145], %[vo], %[rs], %[b0] offen offset:0 sc1\n\t"
        "s_mov_b64 exec, s[76:77]\n\t"
        "buffer_load_dwordx2 v[146:147], %[vo], %[rs], %[b0] offen offset:1536 sc1\n\t"
        "s_mov_b64 exec, s[78:79]\n\t"
        "buffer_load_dwordx2 v[148:149], %[vo], %[rs], %[b0] offen offset:3072 sc1\n\t"
        "s_mov_b64 exec, s[80:81]\n\t"
        "buffer_load_dwordx2 v[150:151], %[vo], %[rs], %[b1] offen offset:0 sc1\n\t"
        "s_mov_b64 exec, s[82:83]\n\t"
        "buffer_load_dwordx2 v[152:153], %[vo], %[rs], %[b1] offen offset:1536 sc1\n\t"
        "s_mov_b64 exec, s[84:85]\n\t"
        "buffer_load_dwordx2 v[154:155], %[vo], %[rs], %[b1] offen offset:3072 sc1\n\t"
        "s_mov_b64 exec, s[86:87]\n\t"
        "buffer_load_dwordx2 v[156:157], %[vo], %[rs], %[b2] offen offset:0 sc1\n\t"
        "s_mov_b64 exec, s[88:89]\n\t"
        "buffer_load_dwordx2 v[158:159], %[vo], %[rs], %[b2] offen offset:1536 sc1\n\t"
        "s_mov_b64 exec, -1\n\t"
        "s_sleep 2\n\t"
        "1:\n\t"
        "s_mov_b64 exec, s[74:75]\n\t"
        "buffer_load_dwordx2 v[144:145], %[vo], %[rs], %[b0] offen offset:0 sc1\n\t"
        "s_mov_b64 exec, s[76:77]\n\t"
        "buffer_load_dwordx2 v[146:147], %[vo], %[rs], %[b0] offen offset:1536 sc1\n\t"
        "s_mov_b64 exec, s[78:79]\n\t"
        "buffer_load_dwordx2 v[148:149], %[vo], %[rs], %[b0] offen offset:3072 sc1\n\t"
        "s_mov_b64 exec, s[80:81]\n\t"
        "buffer_load_dwordx2 v[150:151], %[vo], %[rs], %[b1] offen offset:0 sc1\n\t"
        "s_mov_b64 exec, s[82:83]\n\t"
        "buffer_load_dwordx2 v[152:153], %[vo], %[rs], %[b1] offen offset:1536 sc1\n\t"
        "s_mov_b64 exec, s[84:85]\n\t"
        "buffer_load_dwordx2 v[154:155], %[vo], %[rs], %[b1] offen offset:3072 sc1\n\t"
        "s_mov_b64 exec, s[86:87]\n\t"
        "buffer_load_dwordx2 v[156:157], %[vo], %[rs], %[b2] offen offset:0 sc1\n\t"
        "s_mov_b64 exec, s[88:89]\n\t"
        "buffer_load_dwordx2 v[158:159], %[vo], %[rs], %[b2] offen offset:1536 sc1\n\t"
        "s_mov_b64 exec, -1\n\t"
        "s_sub_u32 %[cap], %[cap], 1\n\t"
        "s_cbranch_scc1 3f\n\t"
        "s_waitcnt vmcnt(8)\n\t"
        "v_cmp_ne_u32_e64 s[74:75], %[tag], v145\n\t"
        "v_cmp_ne_u32_e64 s[76:77], %[tag], v147\n\t"
        "v_cmp_ne_u32_e64 s[78:79], %[tag], v149\n\t"
        "v_cmp_ne_u32_e64 s[80:81], %[tag], v151\n\t"
        "v_cmp_ne_u32_e64 s[82:83], %[tag], v153\n\t"
        "v_cmp_ne_u32_e64 s[84:85], %[tag], v155\n\t"
        "v_cmp_ne_u32_e64 s[86:87], %[tag], v157\n\t"
        "v_cmp_ne_u32_e64 s[88:89], %[tag], v159\n\t"
        "s_or_b64 s[92:93], s[74:75], s[76:77]\n\t"
        "s_or_b64 s[92:93], s[92:93], s[78:79]\n\t"
        "s_or_b64 s[92:93], s[92:93], s[80:81]\n\t"
        "s_or_b64 s[92:93], s[92:93], s[82:83]\n\t"
        "s_or_b64 s[92:93], s[92:93], s[84:85]\n\t"
        "s_or_b64 s[92:93], s[92:93], s[86:87]\n\t"
        "s_or_b64 s[92:93], s[92:93], s[88:89]\n\t"
        "s_cbranch_scc1 1b\n\t"
        "3:\n\t"
        "s_waitcnt vmcnt(0)\n\t"
        "v_mul_f32 %[o], %[w0], v144\n\t"
        "v_mul_f32 v140, %[w4], v152\n\t"
        "v_fmac_f32 %[o], %[w1], v146\n\t"
        "v_fmac_f32 v140, %[w5], v154\n\t"
        "v_fmac_f32 %[o], %[w2], v148\n\t"
        "v_fmac_f32 v140, %[w6], v156\n\t"
        "v_fmac_f32 %[o], %[w3], v150\n\t"
        "v_fmac_f32 v140, %[w7], v158\n\t"
        "v_add_f32 %[o], %[o], v140\n\t"
        : [o] "=&v"(o), [cap] "+&s"(cap), [nap] "+&s"(napn)
        : [vo] "v"(voff), [rs] "s"(rs), [b0] "s"(base), [b1] "s"(base + 4608), [b2] "s"(base + 9216), [tag] "s"(tag), [j] "s"(j), [own] "v"(own),
          [lm] "s"(0xFFFFFFFFFFFFull), [w0] "v"(w0[0]), [w1] "v"(w0[1]), [w2] "v"(w0[2]), [w3] "v"(w0[3]), [w4] "v"(w1[0]), [w5] "v"(w1[1]), [w6] "v"(w1[2]), [w7] "v"(w1[3])
        : "v140", "v144", "v145", "v146", "v147", "v148", "v149", "v150", "v151", "v152", "v153", "v154", "v155", "v156", "v157", "v158", "v159", "s74", "s75", "s76", "s77", "s78", "s79", "s80", "s81", "s82", "s83", "s84", "s85", "s86", "s87", "s88", "s89", "s92", "s93", "scc", "memory");
    return o;
}
#define PUMP 0
__device__ __forceinline__ s16x4 trd(LAS unsigned char* L, unsigned off) { return __builtin_amdgcn_ds_read_tr16_b64_v4i16((LAS s16x4*)(L + off)); }
__device__ __forceinline__ bf16x8 cat8(s16x4 a, s16x4 b) { return __builtin_bit_cast(bf16x8, __builtin_shufflevector(a, b, 0, 1, 2, 3, 4, 5, 6, 7)); }
__device__ __forceinline__ f32x4 mfma32(bf16x8 a, bf16x8 b, f32x4 c) { return __builtin_amdgcn_mfma_f32_16x16x32_bf16(a, b, c, 0, 0, 0); }
__device__ __forceinline__ bf16x8 asb(v4u a) { return __builtin_bit_cast(bf16x8, a); }

__device__ __forceinline__ void ln_stats(const unsigned (&xp)[5], float& mb, float& rden) {
    const float c = lo16(__builtin_amdgcn_readfirstlane(xp[0]));
    float s = 0.f, s2 = 0.f;
#pragma unroll
    for (int i = 0; i < 5; ++i) {
        const float a = lo16(xp[i]) - c, b = hi16(xp[i]) - c;
        s += a + b; s2 = fmaf(a, a, s2); s2 = fmaf(b, b, s2);
    }
    s = wave_sum(s); s2 = wave_sum(s2);
    const float dm = s * (1.0f / D);
    const float var = fmaxf(s2 * (1.0f / D) - dm * dm, 0.f);
    mb = rbf(c + dm);
    const float den = rbf(__builtin_amdgcn_sqrtf(rbf(rbf(var) + 1e-5f)));
    rden = __builtin_amdgcn_rcpf(den);
}
__device__ __forceinline__ unsigned ln_pair(unsigned w, float mb, float rden) {
    const unsigned d = pk2(lo16(w) - mb, hi16(w) - mb);
    return pk2(lo16(d) * rden, hi16(d) * rden);
}

template <int N> __device__ __forceinline__ unsigned reduce_cols(rsrc_t rs, unsigned voff, unsigned base, unsigned tag, unsigned res) {
    float r0 = lo16(res), r1 = hi16(res);
    PIN(r0); PIN(r1);
    float s0, s1;
    sum_pairs<N, PUMP>(rs, voff, base, tag, s0, s1);
    auto sw = __builtin_amdgcn_permlane32_swap(__float_as_uint(s0), __float_as_uint(s1), false, false);
    float t = fv(sw[0]) + fv(sw[1]);
    t += dpp<XOR8>(t);
    t = sw16_sum(t);
    auto s2 = __builtin_amdgcn_permlane32_swap(__float_as_uint(t), __float_as_uint(t), false, false);
    const unsigned sp = pk2(fv(s2[0]), fv(s2[1]));
    return pk2(r0 + lo16(sp), r1 + hi16(sp));
}
__device__ __forceinline__ float fold5(const v4u (&p)[5]) {
    const float a0 = ((fv(p[0][0]) + fv(p[1][0])) + (fv(p[2][0]) + fv(p[3][0]))) + fv(p[4][0]);
    const float a1 = ((fv(p[0][2]) + fv(p[1][2])) + (fv(p[2][2]) + fv(p[3][2]))) + fv(p[4][2]);
    auto sw = __builtin_amdgcn_permlane32_swap(__float_as_uint(a0), __float_as_uint(a1), false, false);
    return fv(sw[0]) + fv(sw[1]);
}
__device__ __forceinline__ unsigned lateness(rsrc_t rs, int lane, u64 t_start) {
    const v2u t = __builtin_amdgcn_raw_buffer_load_b64(rs, WS_T0 + (lane & 7) * 128, 0, 16);
    const u64 d = t_start - (((u64)t[1] << 32) | t[0]);
    float l = d < 150ull ? (float)(unsigned)d : 0.f;
    l = fmaxf(l, dpp<XOR1>(l)); l = fmaxf(l, dpp<XOR2>(l)); l = fmaxf(l, dpp<XOR7>(l));
    return __builtin_amdgcn_readfirstlane((unsigned)l);
}
__device__ __forceinline__ void observe_ap(rsrc_t rs, int lane, int cg, unsigned tag) {
    v2u o[1] = {v2u{0u, lane < 32 ? ~tag : tag}};
    gather<1, 1>(rs, lane * 128, WS_AP + cg * 4096, 0, tag, o);
}
#define STAMP(i) do { } while (0)

#define LD_REPORT \
    "s_getreg_b32 s92, hwreg(HW_REG_IB_STS)\n\t" \
    "s_and_b32 s93, s92, 15\n\t" \
    "s_bfe_u32 s92, s92, 0x20016\n\t" \
    "s_lshl4_add_u32 s93, s92, s93\n\t" \
    "s_sub_u32 s92, %[iss], s93\n\t" \
    "v_mov_b32 %[vt], s92\n\t"
#define LD_WRITE \
    "s_mov_b64 exec, 1\n\t" \
    "ds_write_b32 %[va], %[vt]\n\t" \
    "s_mov_b64 exec, -1\n\t"
struct Ld { unsigned issued, tnext, pace, va, sh, w0; };
template <bool FLIP> __device__ __forceinline__ void ld_stream(Ld& s, rsrc_t rs, unsigned (&o)[6], unsigned soff, unsigned sstep, unsigned dst, int ng, unsigned wrap = ~0u) {
    unsigned vt;
    const unsigned end = __builtin_amdgcn_readfirstlane(s.issued + 6 * ng);
    soff = __builtin_amdgcn_readfirstlane(soff); dst = __builtin_amdgcn_readfirstlane(dst);
    s.issued = __builtin_amdgcn_readfirstlane(s.issued); s.tnext = __builtin_amdgcn_readfirstlane(s.tnext); s.pace = __builtin_amdgcn_readfirstlane(s.pace);
    asm volatile(
        "s_memrealtime s[90:91]\n\t"
        "1:\n\t"
        LD_REPORT
        "s_waitcnt lgkmcnt(0)\n\t"
        "s_lshl_b32 s92, s90, 2\n\t"
        "s_memrealtime s[90:91]\n\t"
        LD_WRITE
        "s_sub_u32 s92, s92, %[tn]\n\t"
        "s_cmp_lt_i32 s92, 0\n\t"
        "s_cbranch_scc1 1b\n\t"
        "s_lshr_b32 s92, s92, %[sh]\n\t"
        "s_add_u32 s92, s92, %[w0]\n\t"
        "s_min_u32 s92, s92, %[wm]\n\t"
        "s_cmp_gt_u32 s93, s92\n\t"
        "s_cbranch_scc1 1b\n\t"
        "s_mov_b32 m0, %[dst]\n\t"
        "s_add_u32 %[tn], %[tn], %[pace]\n\t"
        "buffer_load_dwordx4 %[o0], %[rs], %[so] offen nt lds\n\t"
        "buffer_load_dwordx4 %[o1], %[rs], %[so] offen offset:1024 nt lds\n\t"
        "buffer_load_dwordx4 %[o2], %[rs], %[so] offen offset:2048 nt lds\n\t"
        "s_add_u32 m0, %[dst], 3072\n\t"
        "s_add_u32 %[iss], %[iss], 6\n\t"
        "buffer_load_dwordx4 %[o3], %[rs], %[so] offen nt lds\n\t"
        "buffer_load_dwordx4 %[o4], %[rs], %[so] offen offset:1024 nt lds\n\t"
        "buffer_load_dwordx4 %[o5], %[rs], %[so] offen offset:2048 nt lds\n\t"
        "s_add_u32 %[dst], %[dst], 6144\n\t"
        "s_add_u32 %[so], %[so], %[ss]\n\t"
        "s_cmp_eq_u32 %[so], %[wrap]\n\t"
        "s_cselect_b32 %[so], 0, %[so]\n\t"
        ".if %c[flip]\n\t"
        "v_xor_b32 %[o0], 0x80, %[o0]\n\tv_xor_b32 %[o1], 0x80, %[o1]\n\tv_xor_b32 %[o2], 0x80, %[o2]\n\t"
        "v_xor_b32 %[o3], 0x80, %[o3]\n\tv_xor_b32 %[o4], 0x80, %[o4]\n\tv_xor_b32 %[o5], 0x80, %[o5]\n\t"
        ".endif\n\t"
        "s_cmp_lg_u32 %[iss], %[end]\n\t"
        "s_cbranch_scc1 1b\n\t"
        "s_waitcnt lgkmcnt(0)"
        : [iss] "+&s"(s.issued), [tn] "+&s"(s.tnext), [dst] "+&s"(dst), [so] "+&s"(soff), [vt] "=&v"(vt),
          [o0] "+&v"(o[0]), [o1] "+&v"(o[1]), [o2] "+&v"(o[2]), [o3] "+&v"(o[3]), [o4] "+&v"(o[4]), [o5] "+&v"(o[5])
        : [rs] "s"(rs), [ss] "s"(sstep), [end] "s"(end), [pace] "s"(s.pace), [va] "v"(s.va), [wrap] "s"(wrap), [wm] "n"(WMAX - 6), [sh] "s"(s.sh), [w0] "s"(s.w0), [flip] "n"(FLIP ? 1 : 0)
        : "s90", "s91", "s92", "s93", "scc", "memory");
    s.issued = end;
    s.tnext = __builtin_amdgcn_readfirstlane(s.tnext);
}
__device__ __forceinline__ void ld_gate(Ld& s, int c, unsigned n) {
    unsigned vt, vg, cap = SPIN_LDS;
    const unsigned ga = L_CNT + 4 * c;
    asm volatile(
        "1:\n\t"
        "ds_read_b32 %[vg], %[ga]\n\t"
        LD_REPORT
        LD_WRITE
        "s_sub_u32 %[cap], %[cap], 1\n\t"
        "s_cbranch_scc1 2f\n\t"
        "s_waitcnt lgkmcnt(1)\n\t"
        "v_readfirstlane_b32 s92, %[vg]\n\t"
        "s_cmp_lt_u32 s92, %[n]\n\t"
        "s_cbranch_scc1 1b\n\t"
        "2:\n\t"
        "s_waitcnt lgkmcnt(0)"
        : [vt] "=&v"(vt), [vg] "=&v"(vg), [cap] "+&s"(cap)
        : [iss] "s"(s.issued), [va] "v"(s.va), [ga] "v"(ga), [n] "s"(n)
        : "s92", "s93", "scc", "memory");
}
__device__ __forceinline__ void ld_drain(Ld& s) {
    unsigned vt;
    asm volatile(
        "1:\n\t"
        LD_REPORT
        LD_WRITE
        "s_cmp_lg_u32 s93, 0\n\t"
        "s_cbranch_scc1 1b\n\t"
        : [vt] "=&v"(vt) : [iss] "s"(s.issued), [va] "v"(s.va) : "s92", "s93", "scc", "memory");
}

__global__ void __launch_bounds__(640) k_block(const u16* __restrict__ x, const u16* __restrict__ kc, const u16* __restrict__ vc,
                                               const u16* __restrict__ wq, const u16* __restrict__ wo, const u16* __restrict__ wg,
                                               const u16* __restrict__ wu, const u16* __restrict__ wd, u16* __restrict__ out,
                                               unsigned char* ws KNARG) {
    extern __shared__ __attribute__((aligned(16))) unsigned char lds_raw[];
    LAS unsigned char* L = (LAS unsigned char*)lds_raw;
    if (threadIdx.x < 20) ((LAS unsigned*)(L + L_CNT))[threadIdx.x] = 0u;
    asm volatile("s_waitcnt lgkmcnt(0)\n\ts_barrier" :: "s"(x), "s"(kc), "s"(vc), "s"(wq), "s"(wo), "s"(wg), "s"(wu), "s"(wd), "s"(out), "s"(ws) : "memory");
    const u64 t_start = __builtin_amdgcn_s_memrealtime();
    const int b = blockIdx.x, xcd = b & 7, idx = b >> 3;
    const int h = xcd * 4 + (idx >> 3), j = idx & 7;
    const int tid = threadIdx.x, lane = tid & 63, wave = __builtin_amdgcn_readfirstlane(tid >> 6);
    const int g = lane >> 4, tq = (lane & 15) >> 2, tp = lane & 3, i16 = lane & 15;
    const bool qcu = j >= 2;
    const int qr = (j + 3) >> 3, qc = j - 2 - 3 * qr;
    const int gi = (idx >> 3) * 2 + j;
    const int gcu = (!qcu && gi < 6) ? xcd * 6 + gi : -1;
    const bool pre = qcu || gcu >= 0;
    const rsrc_t rws = mkrs(ws, WS_END);
    if (__builtin_expect(wave == 7 && idx == 0, 1)) __builtin_amdgcn_raw_buffer_store_b64(v2u{(unsigned)t_start, (unsigned)(t_start >> 32)}, rws, lane == 0 ? WS_T0 + xcd * 128 : OOB, 0, 16);
    unsigned tag, xp[5];
    asm volatile("" : "=v"(xp[0]), "=v"(xp[1]), "=v"(xp[2]), "=v"(xp[3]), "=v"(xp[4]), "=v"(tag));
    if (__builtin_expect(wave == 9, 1)) {
        const unsigned xo = lane * 4, zo = 0;
        asm volatile("global_load_dword %0, %6, %7\n\tglobal_load_dword %1, %6, %7 offset:256\n\tglobal_load_dword %2, %6, %7 offset:512\n\t"
                     "global_load_dword %3, %6, %7 offset:768\n\tglobal_load_dword %4, %6, %7 offset:1024\n\tglobal_load_dword %5, %8, %9 sc1"
                     : "+v"(xp[0]), "+v"(xp[1]), "+v"(xp[2]), "+v"(xp[3]), "+v"(xp[4]), "+v"(tag) : "v"(xo), "s"(x), "v"(zo), "s"(ws) : "memory");
    }
#define CODEPF 19400
    if (__builtin_expect(wave == 8, 1)) {
        const rsrc_t rc = mkrs((const void*)(__builtin_amdgcn_s_getpc() & ~255ull), CODEPF);
        asm volatile("s_mov_b32 m0, %0\n\ts_nop 0\n\tbuffer_load_dwordx4 %1, %2, %3 offen lds" :: "s"(L_JUNK), "v"(lane * 16), "s"(rc), "s"(idx * 1024) : "memory");
    }
    if (__builtin_expect(wave == 4 && pre, 1)) {
        const rsrc_t rq = qcu ? mkrs(wq + (size_t)(320 * qr) * (H * DH) + h * DH + 64 * qc, 319 * 12288 + 128) : mkrs(wd + (size_t)(32 * gcu) * D, 40960);
        const unsigned d0 = g * 256 + (i16 ^ (g << 1)) * 16;
        const unsigned q0 = qcu ? (lane >> 3) * 12288 + ((lane & 7) ^ (g & 1) * 2) * 16 : d0, q1 = qcu ? (q0 ^ 64) + 98304 - 1024 : d0;
        const unsigned q2 = qcu ? q0 + 2 * (98304 - 1024) : d0 ^ 128, q3 = qcu ? q1 + 2 * (98304 - 1024) : d0 ^ 128;
        const unsigned qs = qcu ? 393216 : 4096;
#pragma unroll 1
        for (int n = 0; n < 10; ++n, nap(KN(20)))
            asm volatile("s_mov_b32 m0, %0\n\ts_nop 0\n\tbuffer_load_dwordx4 %1, %5, %6 offen nt lds\n\tbuffer_load_dwordx4 %2, %5, %6 offen offset:1024 nt lds\n\t"
                         "buffer_load_dwordx4 %3, %5, %6 offen offset:2048 nt lds\n\tbuffer_load_dwordx4 %4, %5, %6 offen offset:3072 nt lds"
                         :: "s"(L_W + n * 4096), "v"(q0), "v"(q1), "v"(q2), "v"(q3), "s"(rq), "s"(n * qs) : "memory");
        waitvm<0>();
        lds_signal(C_WQ);
    }
    const bool wocu = j < 5;
    const int oi = (idx >> 3) * 3 + (j - 2), oc = (idx >> 3) * 3 + (j - 5);
    const int rg = (!wocu && oc < 10) ? oc : -1;
    const int ocu = (wocu && qcu && oi < 5) ? xcd * 5 + oi : -1;
    const int n1 = __builtin_amdgcn_readfirstlane(!pre ? N1G + 4 : wocu ? N1G : 0);
    const int pv0 = P_K + 6 * n1;
    const f32x4 z4 = {0.f, 0.f, 0.f, 0.f};

    if (__builtin_expect(wave == 0, 1)) {
        __builtin_amdgcn_s_setprio(3);
        STAMP(14);
        unsigned ko[6], vo[6], po[6];
#pragma unroll
        for (int c = 0; c < 3; ++c) {
            const unsigned P = 64 * c + lane, r = P / 24, ch = P - r * 24;
            ko[c] = r * 384 + (ch ^ (r >> 1)) * 16 - c * 1024; ko[c + 3] = (ko[c] ^ 64) + 3072;
        }
        const size_t kvoff = (KN7 & 32) ? ((size_t)idx * SK + (SK - W) + xcd * 256) * DH : ((size_t)h * SK + (SK - W) + j * 256) * DH;
        Ld s;
        s.issued = 0; s.va = L_CNT + 4 * C_PROG; s.w0 = KN(22); s.sh = KN(23);
        nap(KN(21));
        MEMBAR();
        const unsigned late = (KN7 & 64) ? 0u : lateness(rws, lane, t_start);
        lds_set(C_LATE, late);
        s.pace = ((unsigned)(TE - (pre ? TAQ : TA0) - late) * (!pre ? 262144 / (35 + N1G) : wocu ? 262144 / (31 + N1G) : 262144 / 31)) >> 16;
        s.tnext = ((unsigned)t_start + (pre ? TAQ : TA0)) * 4;
        const unsigned rot = 0;
        ld_stream<false>(s, mkrs(kc + kvoff, 98304), ko, rot, 6144, L_V, 16, 98304);
        STAMP(4);
#pragma unroll
        for (int c = 0; c < 3; ++c) {
            const unsigned P = 64 * c + lane, r = P / 24, ch = P - r * 24;
            vo[c] = r * 384 + (ch ^ (r & 2u)) * 16 - c * 1024; vo[c + 3] = (vo[c] ^ 64) + 3072;
            const unsigned wb = (i16 ^ (g << 1)) * 16;
            po[c] = wocu ? (4 * c + g) * 1280 + (wb ^ ((c >> 1) << 7)) - c * 1024 : vo[c] + r * (3072 - 384);
            po[c + 3] = wocu ? (4 * c + 12 + g) * 1280 + (wb ^ ((((c + 3) >> 1) & 1) << 7)) - c * 1024 : vo[c + 3] + (r + 8) * (3072 - 384);
        }
        const rsrc_t rv = mkrs(vc + kvoff, 98304);
        const rsrc_t ro = mkrs(wo + (size_t)(h * DH) * D + j * 128, 191 * 1280 + 256);
        ld_gate(s, C_QP, __builtin_amdgcn_readfirstlane(qcu ? 4 : pre ? 8 : 0));
        if (n1) ld_stream<true>(s, ro, po, 0, 30720, L_W, n1);
        ld_gate(s, C_KA, 4);
        ld_stream<false>(s, rv, vo, rot, 6144, L_V, 8, 98304);
        ld_gate(s, C_KB, 4);
        ld_stream<false>(s, rv, vo, rot >= 49152 ? rot - 49152 : rot + 49152, 6144, L_V + 49152, 8, 98304);
        STAMP(6);
        s.pace = KN(15);
        if (wocu) {
            if (n1 < 8) ld_stream<true>(s, ro, po, n1 * 30720, 30720, L_W + n1 * 6144, 8 - n1);
        } else if (rg >= 0) {
            const rsrc_t r0 = mkrs(wg + (size_t)(64 * rg) * F + 192 * xcd, 63 * 3072 + 384), r1 = mkrs(wu + (size_t)(64 * rg) * F + 192 * xcd, 63 * 3072 + 384);
            s.tnext += FDELAY * 4; s.pace = PACE2;
            ld_stream<false>(s, r0, po, 0, 49152, L_W, 4);
            ld_stream<false>(s, r1, po, 0, 49152, L_W + 24576, 4);
        }
        ld_drain(s);
        STAMP(9);
        tag = __builtin_amdgcn_readfirstlane(*(LAS unsigned*)(L + L_TAG));
        if (gcu >= 0) {
            STAMP(17);
            unsigned ha = L_HH + (lane & 31) * 2;
            PIN(ha);
            float cs;
            lds_wait_rd<true>(L, C_CS, 1, [&] { cs = *(volatile LAS float*)(L + L_LN + lane * 4); });
            PIN(cs);
            lds_wait<false>(L, C_GO, 1);
            STAMP(18);
            delay(D3);
            float ga0, ga1;
            sum_pairs<5, PUMP>(rws, lane * 16, WS_GP + gcu * 5120, tag, ga0, ga1);
            f32x4 st = *(volatile LAS f32x4*)(L + L_ST);
            STAMP(12);
            auto sw0 = __builtin_amdgcn_permlane32_swap(__float_as_uint(ga0), __float_as_uint(ga1), false, false);
            float sv = fv(sw0[0]) + fv(sw0[1]);
            PIN(sv);
#pragma unroll 1
            for (unsigned sp = 0; sp < SPIN_LDS && __builtin_amdgcn_readfirstlane(__float_as_uint(st[2])) == 0u; ++sp) st = *(volatile LAS f32x4*)(L + L_ST);
            const float v = rbf((sv - st[0] * cs) * st[1]);
            auto sw = __builtin_amdgcn_permlane32_swap(__float_as_uint(v), __float_as_uint(v), false, false);
            const float sg = rbf(__builtin_amdgcn_rcpf(1.0f + __expf(-fv(sw[0]))));
            *(LAS u16*)(L + ha) = (u16)bfbits(sg * fv(sw[1]));
            lds_signal(C_HH);
        } else if (ocu >= 0) {
            const rsrc_t rout = mkrs(out, 1280);
            unsigned oo = lane < 8 ? (8 * ocu + lane) * 4 : OOB;
            PIN(oo);
            lds_wait<true>(L, C_O, 4);
            v2u hres[1] = {v2u{0u, ~tag}};
            delay(DOBS);
            observe_ap(rws, lane, ocu, tag);
            const u64 t4 = __builtin_amdgcn_s_memrealtime() + D3 + D4;
            delay(D3);
            gather<1, 4>(rws, (lane & 7) * 8, WS_HB + xcd * 4096 + 64 * ocu, 0, tag, hres);
            until(t4);
            STAMP(22);
            const unsigned ov = reduce_cols<6>(rws, lane * 16, WS_DP + ocu * 6144, tag, hres[0][0]);
            __builtin_amdgcn_raw_buffer_store_b32(ov, rout, oo, 0, 0);
            if (ocu == 0 && lane == 0) __hip_atomic_store((gu32*)(ws + WS_EPOCH), tag, RLX_AGENT);
            STAMP(16);
        }
    } else if (wave == 9) {
        asm volatile("s_waitcnt vmcnt(0)" : "+v"(xp[0]), "+v"(xp[1]), "+v"(xp[2]), "+v"(xp[3]), "+v"(xp[4]), "+v"(tag) :: "memory");
        float mb, rden;
        ln_stats(xp, mb, rden);
#pragma unroll
        for (int i = 0; i < 5; ++i) *(LAS unsigned*)(L + L_LN + (lane + 64 * i) * 4) = ln_pair(xp[i], mb, rden);
        tag = __builtin_amdgcn_readfirstlane(tag) + 1u;
        if (lane == 0) *(LAS unsigned*)(L + L_TAG) = tag;
        lds_signal(C_X);
        STAMP(23);
        {
            until(t_start + TQ);
            v4u qp[3];
#pragma unroll
            for (int k = 0; k < 3; ++k) qp[k] = v4u{0u, ~tag, 0u, ~tag};
            if (!(KN7 & 1)) gather<1, 8>(rws, lane * 16, WS_QP + h * 3072, 1024, tag, qp);
#pragma unroll
            for (int k = 0; k < 3; ++k) *(LAS unsigned*)(L + L_Q + k * 128 + (lane & 31) * 4) = pk2(sw32_sum(fv(qp[k][0])), sw32_sum(fv(qp[k][2])));
            lds_signal(C_Q);
            STAMP(3);
        }
        lds_wait<true>(L, C_P, 4);
        const f32x4 mx = *(LAS f32x4*)(L + L_MX), ls = *(LAS f32x4*)(L + L_MX + 16);
        const float m = fmaxf(fmaxf(mx[0], mx[1]), fmaxf(mx[2], mx[3]));
        const float l = (ls[0] + ls[1]) + (ls[2] + ls[3]);
        if (!(KN7 & 8)) put2(rws, lane == 0 ? WS_ST + (h * 8 + j) * 128 : OOB, tag, m, l);
        if (wocu) {
            until(t_start - __builtin_amdgcn_readfirstlane(*(volatile LAS unsigned*)(L + L_CNT + 4 * C_LATE)) + TS);
            const unsigned tt = lane < 8 ? ~tag : tag;
            v4u st[1] = {v4u{0u, tt, 0u, tt}};
            if (!(KN7 & 4)) gather<1, 8>(rws, lane * 128, WS_ST + h * 1024, 0, tag, st);
            const float ms = fv(st[0][0]);
            float M = fmaxf(ms, dpp<XOR1>(ms)); M = fmaxf(M, dpp<XOR2>(M)); M = fmaxf(M, dpp<XOR7>(M));
            const float e = __expf(ms - M);
            float Ls = e * fv(st[0][2]);
            Ls += dpp<XOR1>(Ls); Ls += dpp<XOR2>(Ls); Ls += dpp<XOR7>(Ls);
            if (lane < 8) *(LAS float*)(L + L_WGT + lane * 4) = e * __builtin_amdgcn_rcpf(Ls);
            lds_signal(C_W);
            STAMP(19);
            if (gcu >= 0) {
                lds_wait<true>(L, C_O, 4);
                delay(DOBS);
                observe_ap(rws, lane, gi * 6, tag);
                lds_signal(C_GO);
                delay(D3 - 22);
                v2u hv[5];
#pragma unroll
                for (int i = 0; i < 5; ++i) hv[i] = v2u{0u, ~tag};
                gather<1, 0>(rws, lane * 8, WS_HB + xcd * 4096, 512, tag, hv);
#pragma unroll
                for (int i = 0; i < 5; ++i) xp[i] = hv[i][0];
                ln_stats(xp, mb, rden);
                if (lane == 0) *(LAS f32x4*)(L + L_ST) = f32x4{mb, rden, 1.0f, 0.f};
                STAMP(15);
            }
        }
    } else {
        bf16x8 dw[5];
        if (gcu >= 0) {
            lds_wait<true>(L, C_WQ, 1);
#pragma unroll
            for (int t = 0; t < 5; ++t) {
                const int tt = 5 * (wave - 1) + t;
                unsigned ad[2];
#pragma unroll
                for (int u = 0; u < 2; ++u) {
                    const int r = 8 * g + 4 * u + tq, B = 5 * r + (tt >> 3);
                    ad[u] = L_W + r * 1280 + (tt ^ ((B & 3) | ((B >> 1) & 4))) * 32 + tp * 8;
                }
                dw[t] = cat8(trd(L, ad[0]), trd(L, ad[1]));
            }
            lds_signal(C_QP);
        }
        if (wave < 5) {
            const int a = wave - 1;
            unsigned xres = 0;
            if (rg >= 0) xres = ((const unsigned*)x)[32 * rg + 8 * a + (lane & 7)];
            lds_wait<true>(L, C_X, 1);
            tag = __builtin_amdgcn_readfirstlane(*(LAS unsigned*)(L + L_TAG));
            if (qcu) {
                bf16x8 xa[10];
#pragma unroll
                for (int s = 0; s < 10; ++s) xa[s] = *(LAS bf16x8*)(L + L_LN + (320 * qr + 32 * s + 8 * g) * 2);
                lds_wait<true>(L, C_WQ, 1);
                if (a == 0) STAMP(1);
                const unsigned ca = L_W + (8 * g + tq) * 128 + ((a ^ ((tq >> 1) | ((g & 1) << 1))) * 2 + (tp >> 1)) * 16 + (tp & 1) * 8;
                f32x4 q0 = z4, q1 = z4;
#pragma unroll
                for (int s = 0; s < 10; s += 2) {
                    q0 = mfma32(xa[s], cat8(trd(L, ca + s * 4096), trd(L, ca + s * 4096 + 512)), q0);
                    q1 = mfma32(xa[s + 1], cat8(trd(L, ca + s * 4096 + 4096), trd(L, ca + s * 4096 + 4608)), q1);
                }
                lds_signal(C_QP);
                if (!(KN7 & 2)) put1(rws, lane < 16 ? WS_QP + h * 3072 + qc * 1024 + qr * 512 + (16 * a + lane) * 8 : OOB, tag, __float_as_uint(q0[0] + q1[0]));
                if (a == 0) STAMP(2);
            }
            if (wocu) {
                unsigned oa = L_O + (lane < 48 ? 48 * a + lane : 192 + lane) * 2;
                PIN(oa);
                lds_wait<true>(L, C_W, 1);
                f32x4 w0 = *(LAS f32x4*)(L + L_WGT), w1 = *(LAS f32x4*)(L + L_WGT + 16);
                PIN(w0); PIN(w1);
                lds_wait<true>(L, C_PROG, pv0 + 84);
                unsigned own;
                lds_wait_rd<false>(L, C_OWN, 4, [&] { own = *(volatile LAS unsigned*)(L + L_OWN + (64 * a + lane) * 4); });
                const float o = combine_o<PUMP>(rws, lane * 8, WS_OP + (h * 8 * 192 + 48 * a) * 8, tag, j, own, w0, w1, D1);
                *(LAS u16*)(L + oa) = (u16)bfbits(o);
                lds_signal(C_O);
                if (a == 0) STAMP(8);
                if (gcu >= 0 && a == 0) {
                    v4u p[5];
#pragma unroll
                    for (int k = 0; k < 5; ++k) p[k] = v4u{0u, ~tag, 0u, ~tag};
                    delay(DOBS);
                    gather<1, 8>(rws, lane * 16, WS_CP + gcu * 5120, 1024, tag, p);
                    *(LAS float*)(L + L_LN + lane * 4) = fold5(p);
                    lds_signal(C_CS);
                }
            } else if (rg >= 0) {
                unsigned ha = L_O + (8 * a + (lane & 7)) * 4, ho = lane < 8 ? WS_HB + xcd * 4096 + (32 * rg + 8 * a + lane) * 8 : OOB;
                PIN(ha); PIN(ho);
                lds_wait<true>(L, C_OWN, 4);
                delay(D2);
                lds_wait<true>(L, C_PROG, P_WO);
                const unsigned hp = reduce_cols<4>(rws, lane * 16, WS_AP + (4 * rg + a) * 4096, tag, xres);
                put1(rws, ho, tag, hp);
                *(LAS unsigned*)(L + ha) = hp;
                lds_signal(C_O);
                if (a == 0) STAMP(11);
            }
        } else {
            const int kw = wave - 5;
            bf16x8 kf[4][6];
            {
                const unsigned kb = L_V + (16 * kw + i16) * 384;
#pragma unroll
                for (int n = 0; n < 4; ++n) {
                    lds_wait<true>(L, C_PROG, 12 * ((kw >> 1) + 2 * n + 1));
#pragma unroll
                    for (int s = 0; s < 6; ++s) kf[n][s] = *(LAS bf16x8*)(L + kb + n * 24576 + ((4 * s + g) ^ (i16 >> 1)) * 16);
                    if (n & 1) lds_signal(n == 1 ? C_KA : C_KB);
                }
            }
            lds_wait<true>(L, C_Q, 1);
            tag = __builtin_amdgcn_readfirstlane(*(LAS unsigned*)(L + L_TAG));
            f32x4 sa[4] = {z4, z4, z4, z4};
#pragma unroll
            for (int s = 0; s < 6; ++s) {
                const bf16x8 qf = *(LAS bf16x8*)(L + L_Q + (32 * s + 8 * g) * 2);
#pragma unroll
                for (int n = 0; n < 4; ++n) sa[n] = mfma32(qf, kf[n][s], sa[n]);
            }
            const float raw = g == 0 ? sa[0][0] : g == 1 ? sa[1][0] : g == 2 ? sa[2][0] : sa[3][0];
            const float s = rbf(rbf(raw) * 0.07216878364870322f);
            const float mw = wave_max(s);
            if (lane == 0) *(LAS float*)(L + L_MX + kw * 4) = mw;
            lds_signal(C_MAX);
            lds_wait<false>(L, C_MAX, 4);
            const f32x4 mx = *(LAS f32x4*)(L + L_MX);
            const float m = fmaxf(fmaxf(mx[0], mx[1]), fmaxf(mx[2], mx[3]));
            const float e = __expf(s - m);
            *(LAS u16*)(L + L_P + (16 * (kw + 4 * g) + i16) * 2) = (u16)bfbits(e);
            const float lw = wave_sum(e);
            if (lane == 0) *(LAS float*)(L + L_MX + 16 + kw * 4) = lw;
            lds_signal(C_P);
            lds_wait<false>(L, C_P, 4);
            if (kw == 0) STAMP(5);
            bf16x8 pf[8];
#pragma unroll
            for (int s8 = 0; s8 < 8; ++s8) pf[s8] = *(LAS bf16x8*)(L + L_P + (32 * s8 + 8 * g) * 2);
            const int fvs = (tq >> 1) | ((g & 1) << 1);
            unsigned vb[3], vl[3];
#pragma unroll
            for (int t = 0; t < 3; ++t) { vb[t] = (8 * g + tq) * 384 + (((3 * kw + t) ^ fvs) * 2 + (tp >> 1)) * 16 + (tp & 1) * 8; vl[t] = L_V + 6 * 12288 + vb[t]; PIN(vl[t]); }
            unsigned oo = lane < 48 ? WS_OP + ((h * 8 + j) * 192 + 48 * kw + lane) * 8 : OOB, ow = L_OWN + (64 * kw + lane) * 4;
            PIN(oo); PIN(ow);
            f32x4 oa[3] = {z4, z4, z4};
            lds_wait<true>(L, C_PROG, pv0 + 72);
#pragma unroll
            for (int s8 = 0; s8 < 6; ++s8)
#pragma unroll
                for (int t = 0; t < 3; ++t) oa[t] = mfma32(pf[s8], cat8(trd(L, L_V + vb[t] + s8 * 12288), trd(L, L_V + vb[t] + s8 * 12288 + 1536)), oa[t]);
#pragma unroll
            for (int s8 = 6; s8 < 8; ++s8) {
                bf16x8 vf[3];
                lds_wait_rd<false>(L, C_PROG, pv0 + 12 * s8 + 12, [&] {
#pragma unroll
                    for (int t = 0; t < 3; ++t) vf[t] = cat8(trd(L, vl[t] + (s8 - 6) * 12288), trd(L, vl[t] + (s8 - 6) * 12288 + 1536));
                });
                if (s8 == 7 && kw == 0) STAMP(20);
#pragma unroll
                for (int t = 0; t < 3; ++t) oa[t] = mfma32(pf[s8], vf[t], oa[t]);
            }
            const float op = g == 0 ? oa[0][0] : g == 1 ? oa[1][0] : oa[2][0];
            put1(rws, oo, tag, __float_as_uint(op));
            *(LAS float*)(L + ow) = op;
            lds_signal(C_OWN);
            if (kw == 0) STAMP(7);
            if (wocu) {
                unsigned ao = lane < 32 ? WS_AP + (((8 * j + 2 * kw + g) * 32 + h) * 16 + i16) * 8 : OOB, al = L_O + 16 * g;
                PIN(ao); PIN(al);
                lds_wait<false>(L, C_PROG, P_WO);
                bf16x8 bw[2][6], af[6];
                const int f3 = tq | ((g & 1) << 2);
#pragma unroll
                for (int t = 0; t < 2; ++t) {
                    const unsigned cb = L_W + (8 * g + tq) * 256 + (((2 * kw + t) ^ f3) * 2 + (tp >> 1)) * 16 + (tp & 1) * 8;
#pragma unroll
                    for (int s6 = 0; s6 < 6; ++s6) bw[t][s6] = cat8(trd(L, cb + s6 * 8192), trd(L, cb + s6 * 8192 + 1024));
                }
                lds_wait_rd<false>(L, C_O, 4, [&] {
#pragma unroll
                    for (int s6 = 0; s6 < 6; ++s6) af[s6] = *(volatile LAS bf16x8*)(L + al + 64 * s6);
                });
                f32x4 a0 = z4, a1 = z4;
#pragma unroll
                for (int s6 = 0; s6 < 6; ++s6) { a0 = mfma32(af[s6], bw[0][s6], a0); a1 = mfma32(af[s6], bw[1][s6], a1); }
                put1(rws, ao, tag, __float_as_uint(g ? a1[0] : a0[0]));
                if (kw == 0) STAMP(10);
            } else if (rg >= 0) {
                lds_wait<true>(L, C_PROG, P_WO);
                bf16x8 bw[2][3][2];
#pragma unroll
                for (int mt = 0; mt < 2; ++mt)
#pragma unroll
                    for (int t = 0; t < 3; ++t)
#pragma unroll
                        for (int s2 = 0; s2 < 2; ++s2) bw[mt][t][s2] = cat8(trd(L, L_W + mt * 24576 + vb[t] + s2 * 12288), trd(L, L_W + mt * 24576 + vb[t] + s2 * 12288 + 1536));
                const int f = 48 * kw + lane;
                unsigned go = lane < 48 ? WS_CP + ((6 * xcd + (f >> 5)) * 10 + rg) * 512 + (f & 31) * 16 : OOB, al = L_O + 16 * g;
                PIN(al);
                const v4u ones = {0x3F803F80u, 0x3F803F80u, 0x3F803F80u, 0x3F803F80u};
                bf16x8 af0 = asb(ones), af1 = asb(ones);
#pragma unroll 1
                for (int pass = 0; pass < 2; ++pass) {
                    float r[2];
#pragma unroll
                    for (int mt = 0; mt < 2; ++mt) {
                        f32x4 d[3];
#pragma unroll
                        for (int t = 0; t < 3; ++t) d[t] = mfma32(af1, bw[mt][t][1], mfma32(af0, bw[mt][t][0], z4));
                        r[mt] = g == 0 ? d[0][0] : g == 1 ? d[1][0] : d[2][0];
                    }
                    put2(rws, go, tag, r[0], r[1]);
                    if (pass) break;
                    go += WS_GP - WS_CP;
                    PIN(go);
                    lds_wait_rd<false>(L, C_O, 4, [&] { af0 = *(volatile LAS bf16x8*)(L + al); af1 = *(volatile LAS bf16x8*)(L + al + 64); });
                }
                if (kw == 0) STAMP(21);
            }
        }
        if (gcu >= 0) {
            const int w8 = wave - 1;
            unsigned d0 = WS_DP + (((5 * w8 + g) * 48 + gcu) * 16 + i16) * 8, d1 = lane < 16 ? WS_DP + (((5 * w8 + 4) * 48 + gcu) * 16 + i16) * 8 : OOB, al = L_HH + 16 * g;
            PIN(d0); PIN(d1); PIN(al);
            bf16x8 af;
            lds_wait<true>(L, C_GO, 1);
            lds_wait_rd<false>(L, C_HH, 1, [&] { af = *(volatile LAS bf16x8*)(L + al); });
            f32x4 d[5];
#pragma unroll
            for (int t = 0; t < 5; ++t) d[t] = mfma32(af, dw[(t + 4) % 5], z4);
            put1(rws, d1, tag, __float_as_uint(d[0][0]));
            put1(rws, d0, tag, __float_as_uint(g == 0 ? d[1][0] : g == 1 ? d[2][0] : g == 2 ? d[3][0] : d[4][0]));
            if (w8 == 0) STAMP(13);
        }
    }
}

extern "C" void kernel_launch(void* const* d_in, const int* in_sizes, int n_in, void* d_out, int out_size, void* d_ws, size_t ws_size, hipStream_t stream) {
    (void)hipFuncSetAttribute((const void*)k_block, hipFuncAttributeMaxDynamicSharedMemorySize, L_END);
    k_block<<<256, 640, L_END, stream>>>((const u16*)d_in[0], (const u16*)d_in[1], (const u16*)d_in[2], (const u16*)d_in[3], (const u16*)d_in[4],
                                         (const u16*)d_in[6], (const u16*)d_in[7], (const u16*)d_in[8], (u16*)d_out, (unsigned char*)d_ws
        );
}
```

```cpp
#include <hip/hip_runtime.h>
#include <stdint.h>

typedef unsigned short u16;
typedef unsigned long long u64;
typedef unsigned v4u __attribute__((ext_vector_type(4)));
typedef unsigned v2u __attribute__((ext_vector_type(2)));
typedef float f32x4 __attribute__((ext_vector_type(4)));
typedef short s16x4 __attribute__((ext_vector_type(4)));
typedef __bf16 bf16x8 __attribute__((ext_vector_type(8)));
typedef __bf16 bf16x2 __attribute__((ext_vector_type(2)));
#define GAS __attribute__((address_space(1)))
#define LAS __attribute__((address_space(3)))
typedef GAS u64 gu64;
typedef GAS unsigned gu32;
typedef __amdgpu_buffer_rsrc_t rsrc_t;
#define RLX_AGENT __ATOMIC_RELAXED, __HIP_MEMORY_SCOPE_AGENT

constexpr int D = 640, H = 32, SK = 8192, DH = 192, W = 2048, F = 1536;

constexpr unsigned WS_EPOCH = 0;
constexpr unsigned WS_T0 = 1024;
constexpr unsigned WS_QP = 4096;
constexpr unsigned WS_OP = WS_QP + 32 * 3072;
constexpr unsigned WS_ST = WS_OP + 32 * 8 * 192 * 8;
constexpr unsigned WS_AP = WS_ST + 32 * 8 * 128;
constexpr unsigned WS_HB = WS_AP + 40 * 512 * 8;
constexpr unsigned WS_CP = WS_HB + 8 * 4096;
constexpr unsigned WS_GP = WS_CP + 48 * 5120;
constexpr unsigned WS_DP = WS_GP + 48 * 5120;
constexpr unsigned WS_END = WS_DP + 40 * 6144;
constexpr unsigned OOB = 0x7FFFFF00u;

constexpr unsigned L_W = 0;
constexpr unsigned L_V = 49152;
constexpr unsigned L_Q = L_V + 98304;
constexpr unsigned L_P = L_Q + 384;
constexpr unsigned L_O = L_P + 512;
constexpr unsigned L_LN = L_O + 512;
constexpr unsigned L_OWN = L_LN + 1280;
constexpr unsigned L_HH = L_OWN + 1024;
constexpr unsigned L_MX = L_HH + 64;
constexpr unsigned L_WGT = L_MX + 32;
constexpr unsigned L_CNT = L_WGT + 32;
constexpr unsigned L_ST = L_CNT + 64;
constexpr unsigned L_TAG = L_ST + 16;
constexpr unsigned L_STAMP = L_TAG + 16;
constexpr unsigned L_JUNK = L_STAMP + 256;
constexpr unsigned L_END = L_JUNK + 1024;
static_assert(L_END <= 163840, "LDS");
enum { C_X = 0, C_PROG, C_KA, C_KB, C_Q, C_MAX, C_P, C_W, C_OWN, C_O, C_HH, C_WQ, C_QP, C_CS, C_GO, C_LATE };
constexpr int P_K = 96, P_WO = 240, P_WD = 282;

#define KTE 875
#define KNOB_DEFAULTS {2, 40, 130, KTE, 40, 420, -80, 0, 6, 150, 30, 130, 60, 0, 0, 0, 0, 0, 0, 0, 2, 4, 16, 4}
constexpr int kn_c[24] = KNOB_DEFAULTS;
#define KN(i) kn_c[i]
#define KNARG
#define N1G KN(0)
#define TA0 KN(1)
#define TAQ KN(2)
#define TE KN(3)
#define FDELAY KN(4)
#define TQ KN(5)
#define TS (TE + KN(6))
#define KN7 KN(7)
#define D1 KN(8)
#define D2 KN(9)
#define D3 KN(10)
#define D4 KN(11)
#define DOBS KN(12)
#define PACE2 KN(13)
#define RSL KN(14)
#define THR1 KN(16)
#define THR2 KN(17)
#define THR3 KN(18)
#define THR4 KN(19)
#define POLK 0
#define POLV 0
#define POLW 1
#define WMAX 36
#define DEPTH 2

constexpr unsigned SPIN_LDS = 1u << 20;
constexpr unsigned SPIN_GLB = 1u << 15;

__device__ __forceinline__ unsigned pk2(float lo, float hi) {
    const bf16x2 r = {(__bf16)lo, (__bf16)hi};
    return __builtin_bit_cast(unsigned, r);
}
__device__ __forceinline__ unsigned bfbits(float f) { return (unsigned)__builtin_bit_cast(u16, (__bf16)f); }
__device__ __forceinline__ float rbf(float f) { return __uint_as_float(bfbits(f) << 16); }
__device__ __forceinline__ float lo16(unsigned w) { return __uint_as_float(w << 16); }
__device__ __forceinline__ float hi16(unsigned w) { return __uint_as_float(w & 0xFFFF0000u); }
__device__ __forceinline__ float dot2(unsigned a, unsigned b, float c) {
    return __builtin_amdgcn_fdot2_f32_bf16(__builtin_bit_cast(bf16x2, a), __builtin_bit_cast(bf16x2, b), c, false);
}
template <int CTRL> __device__ __forceinline__ float dpp(float x) {
    return __builtin_bit_cast(float, __builtin_amdgcn_mov_dpp(__builtin_bit_cast(int, x), CTRL, 0xf, 0xf, true));
}
constexpr int XOR1 = 0xB1, XOR2 = 0x4E, XOR7 = 0x141, XOR8 = 0x128;
__device__ __forceinline__ float sw16_sum(float x) {
    auto s = __builtin_amdgcn_permlane16_swap(__float_as_uint(x), __float_as_uint(x), false, false);
    return __uint_as_float(s[0]) + __uint_as_float(s[1]);
}
__device__ __forceinline__ float sw32_sum(float x) {
    auto s = __builtin_amdgcn_permlane32_swap(__float_as_uint(x), __float_as_uint(x), false, false);
    return __uint_as_float(s[0]) + __uint_as_float(s[1]);
}
__device__ __forceinline__ float sw16_max(float x) {
    auto s = __builtin_amdgcn_permlane16_swap(__float_as_uint(x), __float_as_uint(x), false, false);
    return fmaxf(__uint_as_float(s[0]), __uint_as_float(s[1]));
}
__device__ __forceinline__ float sw32_max(float x) {
    auto s = __builtin_amdgcn_permlane32_swap(__float_as_uint(x), __float_as_uint(x), false, false);
    return fmaxf(__uint_as_float(s[0]), __uint_as_float(s[1]));
}
__device__ __forceinline__ float wave_sum(float x) {
    x += dpp<XOR1>(x); x += dpp<XOR2>(x); x += dpp<XOR7>(x); x += dpp<XOR8>(x);
    return sw32_sum(sw16_sum(x));
}
__device__ __forceinline__ float wave_max(float x) {
    x = fmaxf(x, dpp<XOR1>(x)); x = fmaxf(x, dpp<XOR2>(x)); x = fmaxf(x, dpp<XOR7>(x)); x = fmaxf(x, dpp<XOR8>(x));
    return sw32_max(sw16_max(x));
}
__device__ __forceinline__ rsrc_t mkrs(const void* p, unsigned bytes) { return __builtin_amdgcn_make_buffer_rsrc((void*)p, 0, bytes, 0x00020000); }

__device__ __forceinline__ void dma16(rsrc_t rs, unsigned voff, unsigned soff, unsigned lds_dst) {
    asm volatile("s_mov_b32 m0, %0\n\ts_nop 0\n\tbuffer_load_dwordx4 %1, %2, %3 offen nt lds"
                 :: "s"(lds_dst), "v"(voff), "s"(rs), "s"(soff) : "memory");
}
template <int N> __device__ __forceinline__ void waitvm() { asm volatile("s_waitcnt vmcnt(%0)" :: "n"(N) : "memory"); }
#define LGKM_DRAIN() asm volatile("s_waitcnt lgkmcnt(0)" ::: "memory")
#define MEMBAR() asm volatile("" ::: "memory")
#define BAR() do { __builtin_amdgcn_sched_barrier(0); asm volatile("s_barrier" ::: "memory"); __builtin_amdgcn_sched_barrier(0); } while (0)

__device__ __forceinline__ void lds_signal(int c) {
    const unsigned addr = L_CNT + 4 * c, one = 1u;
    asm volatile("s_mov_b64 exec, 1\n\tds_add_u32 %0, %1\n\ts_mov_b64 exec, -1" :: "v"(addr), "v"(one) : "memory");
}
__device__ __forceinline__ void lds_set(int c, unsigned val) {
    const unsigned addr = L_CNT + 4 * c;
    asm volatile("s_mov_b64 exec, 1\n\tds_write_b32 %0, %1\n\ts_mov_b64 exec, -1" :: "v"(addr), "v"(val) : "memory");
}
template <bool SLEEP, class F> __device__ __forceinline__ void lds_wait_rd(LAS unsigned char* L, int c, unsigned n, F rd) {
    volatile LAS unsigned* p = (volatile LAS unsigned*)(L + L_CNT) + c;
    unsigned cap = SPIN_LDS;
#pragma unroll 1
    do {
        const unsigned f = *p;
        MEMBAR();
        rd();
        MEMBAR();
        if ((unsigned)__builtin_amdgcn_readfirstlane(f) >= n) break;
        if (SLEEP) __builtin_amdgcn_s_sleep(1);
        MEMBAR();
    } while (--cap != 0);
    MEMBAR();
}
template <bool SLEEP> __device__ __forceinline__ void lds_wait(LAS unsigned char* L, int c, unsigned n) { lds_wait_rd<SLEEP>(L, c, n, [] {}); }
__device__ __forceinline__ void until(u64 t) { while (__builtin_amdgcn_s_memrealtime() < t) __builtin_amdgcn_s_sleep(1); }
__device__ __forceinline__ void delay(int ticks) { until(__builtin_amdgcn_s_memrealtime() + ticks); }
__device__ __forceinline__ void nap(int n) { for (int i = 0; i < n; ++i) __builtin_amdgcn_s_sleep(1); }
#define PIN(x) asm volatile("" : "+v"(x))
#define SB() __builtin_amdgcn_sched_barrier(0)
__device__ __forceinline__ float fv(unsigned u) { return __uint_as_float(u); }
__device__ __forceinline__ void put1(rsrc_t rs, unsigned off, unsigned tag, unsigned val) { __builtin_amdgcn_raw_buffer_store_b64(v2u{val, tag}, rs, off, 0, 16); }
__device__ __forceinline__ void put2(rsrc_t rs, unsigned off, unsigned tag, float a, float b) {
    __builtin_amdgcn_raw_buffer_store_b128(v4u{__float_as_uint(a), tag, __float_as_uint(b), tag}, rs, off, 0, 16);
}
__device__ __forceinline__ u64 badmask(v2u x, unsigned tag) {
    u64 m;
    asm volatile("v_cmp_ne_u32_e64 %0, %1, %2" : "=s"(m) : "s"(tag), "v"(x[1]));
    return m;
}
__device__ __forceinline__ u64 badmask(v4u x, unsigned tag) {
    u64 m;
    const unsigned t = __builtin_amdgcn_bitop3_b32(x[1], x[3], tag, 0x7E);
    asm volatile("v_cmp_ne_u32_e64 %0, 0, %1" : "=s"(m) : "v"(t));
    return m;
}
__device__ __forceinline__ void gload(v2u& x, u64 m, rsrc_t rs, unsigned voff, unsigned soff) {
    asm volatile("s_mov_b64 exec, %2\n\tbuffer_load_dwordx2 %0, %1, %3, %4 offen sc1\n\ts_mov_b64 exec, -1" : "+v"(x) : "v"(voff), "s"(m), "s"(rs), "s"(soff) : "memory");
}
__device__ __forceinline__ void gload(v4u& x, u64 m, rsrc_t rs, unsigned voff, unsigned soff) {
    asm volatile("s_mov_b64 exec, %2\n\tbuffer_load_dwordx4 %0, %1, %3, %4 offen sc1\n\ts_mov_b64 exec, -1" : "+v"(x) : "v"(voff), "s"(m), "s"(rs), "s"(soff) : "memory");
}
template <int DP, int SLP, int N, class T> __device__ __forceinline__ void gather(rsrc_t rs, unsigned voff, unsigned base, unsigned stride, unsigned tag, T (&x)[N]) {
    u64 m[N];
#pragma unroll
    for (int k = 0; k < N; ++k) { PIN(x[k]); m[k] = badmask(x[k], tag); }
    unsigned cap = SPIN_GLB;
#pragma unroll 1
    do {
#pragma unroll
        for (int k = 0; k < N; ++k) gload(x[k], m[k], rs, voff, base + k * stride);
        waitvm<0>();
#pragma unroll
        for (int k = 0; k < N; ++k) PIN(x[k]);
        u64 any = 0;
#pragma unroll
        for (int k = 0; k < N; ++k) { m[k] = badmask(x[k], tag); any |= m[k]; }
        if (!any) break;
        if (SLP) __builtin_amdgcn_s_sleep(SLP);
        MEMBAR();
    } while (--cap != 0);
}
template <int N, int PM> __device__ __forceinline__ void sum_pairs(rsrc_t rs, unsigned voff, unsigned base, unsigned tag, float& a0, float& a1);
template <int PM> __device__ __forceinline__ float combine_o(rsrc_t rs, unsigned voff, unsigned base, unsigned tag, int j, unsigned own, f32x4 w0, f32x4 w1, unsigned napn);
template <> __device__ __forceinline__ void sum_pairs<4, 0>(rsrc_t rs, unsigned voff, unsigned base, unsigned tag, float& a0, float& a1) {
    unsigned cap = SPIN_GLB;
    asm volatile(
        "buffer_load_dwordx4 v[144:147], %[vo], %[rs], %[b0] offen offset:0 sc1\n\t"
        "buffer_load_dwordx4 v[148:151], %[vo], %[rs], %[b0] offen offset:1024 sc1\n\t"
        "buffer_load_dwordx4 v[152:155], %[vo], %[rs], %[b0] offen offset:2048 sc1\n\t"
        "buffer_load_dwordx4 v[156:159], %[vo], %[rs], %[b0] offen offset:3072 sc1\n\t"
        "s_waitcnt vmcnt(0)\n\t"
        "v_bitop3_b32 v140, v145, v147, %[tag] bitop3:0x7e\n\t"
        "v_bitop3_b32 v141, v149, v151, %[tag] bitop3:0x7e\n\t"
        "v_bitop3_b32 v142, v153, v155, %[tag] bitop3:0x7e\n\t"
        "v_bitop3_b32 v143, v157, v159, %[tag] bitop3:0x7e\n\t"
        "v_cmp_ne_u32_e64 s[78:79], 0, v140\n\t"
        "v_cmp_ne_u32_e64 s[80:81], 0, v141\n\t"
        "v_cmp_ne_u32_e64 s[82:83], 0, v142\n\t"
        "v_cmp_ne_u32_e64 s[84:85], 0, v143\n\t"
        "s_or_b64 s[92:93], s[78:79], s[80:81]\n\t"
        "s_or_b64 s[92:93], s[92:93], s[82:83]\n\t"
        "s_or_b64 s[92:93], s[92:93], s[84:85]\n\t"
        "s_cbranch_scc0 3f\n\t"
        "1:\n\t"
        "s_mov_b64 exec, s[78:79]\n\t"
        "buffer_load_dwordx4 v[144:147], %[vo], %[rs], %[b0] offen offset:0 sc1\n\t"
        "s_mov_b64 exec, s[80:81]\n\t"
        "buffer_load_dwordx4 v[148:151], %[vo], %[rs], %[b0] offen offset:1024 sc1\n\t"
        "s_mov_b64 exec, s[82:83]\n\t"
        "buffer_load_dwordx4 v[152:155], %[vo], %[rs], %[b0] offen offset:2048 sc1\n\t"
        "s_mov_b64 exec, s[84:85]\n\t"
        "buffer_load_dwordx4 v[156:159], %[vo], %[rs], %[b0] offen offset:3072 sc1\n\t"
        "s_mov_b64 exec, -1\n\t"
        "s_sub_u32 %[cap], %[cap], 1\n\t"
        "s_cbranch_scc1 3f\n\t"
        "s_waitcnt vmcnt(0)\n\t"
        "v_bitop3_b32 v140, v145, v147, %[tag] bitop3:0x7e\n\t"
        "v_bitop3_b32 v141, v149, v151, %[tag] bitop3:0x7e\n\t"
        "v_bitop3_b32 v142, v153, v155, %[tag] bitop3:0x7e\n\t"
        "v_bitop3_b32 v143, v157, v159, %[tag] bitop3:0x7e\n\t"
        "v_cmp_ne_u32_e64 s[78:79], 0, v140\n\t"
        "v_cmp_ne_u32_e64 s[80:81], 0, v141\n\t"
        "v_cmp_ne_u32_e64 s[82:83], 0, v142\n\t"
        "v_cmp_ne_u32_e64 s[84:85], 0, v143\n\t"
        "s_or_b64 s[92:93], s[78:79], s[80:81]\n\t"
        "s_or_b64 s[92:93], s[92:93], s[82:83]\n\t"
        "s_or_b64 s[92:93], s[92:93], s[84:85]\n\t"
        "s_cbranch_scc1 1b\n\t"
        "3:\n\t"
        "s_waitcnt vmcnt(0)\n\t"
        "v_add_f32 %[a0], v144, v148\n\t"
        "v_add_f32 %[a1], v146, v150\n\t"
        "v_add_f32 v140, v152, v156\n\t"
        "v_add_f32 v141, v154, v158\n\t"
        "v_add_f32 %[a0], %[a0], v140\n\t"
        "v_add_f32 %[a1], %[a1], v141\n\t"
        "s_nop 1"
        : [a0] "=&v"(a0), [a1] "=&v"(a1), [cap] "+&s"(cap) : [vo] "v"(voff), [rs] "s"(rs), [b0] "s"(base), [b1] "s"(base + 4096), [tag] "s"(tag)
        : "v140", "v141", "v142", "v143", "v144", "v145", "v146", "v147", "v148", "v149", "v150", "v151", "v152", "v153", "v154", "v155", "v156", "v157", "v158", "v159", "s78", "s79", "s80", "s81", "s82", "s83", "s84", "s85", "s92", "s93", "scc", "memory");
}
template <> __device__ __forceinline__ void sum_pairs<4, 1>(rsrc_t rs, unsigned voff, unsigned base, unsigned tag, float& a0, float& a1) {
    unsigned cap = SPIN_GLB;
    asm volatile(
        "buffer_load_dwordx4 v[144:147], %[vo], %[rs], %[b0] offen offset:0 sc1\n\t"
        "buffer_load_dwordx4 v[148:151], %[vo], %[rs], %[b0] offen offset:1024 sc1\n\t"
        "buffer_load_dwordx4 v[152:155], %[vo], %[rs], %[b0] offen offset:2048 sc1\n\t"
        "buffer_load_dwordx4 v[156:159], %[vo], %[rs], %[b0] offen offset:3072 sc1\n\t"
        "s_waitcnt vmcnt(0)\n\t"
        "v_bitop3_b32 v140, v145, v147, %[tag] bitop3:0x7e\n\t"
        "v_bitop3_b32 v141, v149, v151, %[tag] bitop3:0x7e\n\t"
        "v_bitop3_b32 v142, v153, v155, %[tag] bitop3:0x7e\n\t"
        "v_bitop3_b32 v143, v157, v159, %[tag] bitop3:0x7e\n\t"
        "v_cmp_ne_u32_e64 s[78:79], 0, v140\n\t"
        "v_cmp_ne_u32_e64 s[80:81], 0, v141\n\t"
        "v_cmp_ne_u32_e64 s[82:83], 0, v142\n\t"
        "v_cmp_ne_u32_e64 s[84:85], 0, v143\n\t"
        "s_or_b64 s[92:93], s[78:79], s[80:81]\n\t"
        "s_or_b64 s[92:93], s[92:93], s[82:83]\n\t"
        "s_or_b64 s[92:93], s[92:93], s[84:85]\n\t"
        "s_cbranch_scc0 3f\n\t"
        "s_mov_b64 exec, s[78:79]\n\t"
        "buffer_load_dwordx4 v[144:147], %[vo], %[rs], %[b0] offen offset:0 sc1\n\t"
        "s_mov_b64 exec, s[80:81]\n\t"
        "buffer_load_dwordx4 v[148:151], %[vo], %[rs], %[b0] offen offset:1024 sc1\n\t"
        "s_mov_b64 exec, s[82:83]\n\t"
        "buffer_load_dwordx4 v[152:155], %[vo], %[rs], %[b0] offen offset:2048 sc1\n\t"
        "s_mov_b64 exec, s[84:85]\n\t"
        "buffer_load_dwordx4 v[156:159], %[vo], %[rs], %[b0] offen offset:3072 sc1\n\t"
        "s_mov_b64 exec, -1\n\t"
        "s_sleep 2\n\t"
        "1:\n\t"
        "s_mov_b64 exec, s[78:79]\n\t"
        "buffer_load_dwordx4 v[144:147], %[vo], %[rs], %[b0] offen offset:0 sc1\n\t"
        "s_mov_b64 exec, s[80:81]\n\t"
        "buffer_load_dwordx4 v[148:151], %[vo], %[rs], %[b0] offen offset:1024 sc1\n\t"
        "s_mov_b64 exec, s[82:83]\n\t"
        "buffer_load_dwordx4 v[152:155], %[vo], %[rs], %[b0] offen offset:2048 sc1\n\t"
        "s_mov_b64 exec, s[84:85]\n\t"
        "buffer_load_dwordx4 v[156:159], %[vo], %[rs], %[b0] offen offset:3072 sc1\n\t"
        "s_mov_b64 exec, -1\n\t"
        "s_sub_u32 %[cap], %[cap], 1\n\t"
        "s_cbranch_scc1 3f\n\t"
        "s_waitcnt vmcnt(4)\n\t"
        "v_bitop3_b32 v140, v145, v147, %[tag] bitop3:0x7e\n\t"
        "v_bitop3_b32 v141, v149, v151, %[tag] bitop3:0x7e\n\t"
        "v_bitop3_b32 v142, v153, v155, %[tag] bitop3:0x7e\n\t"
        "v_bitop3_b32 v143, v157, v159, %[tag] bitop3:0x7e\n\t"
        "v_cmp_ne_u32_e64 s[78:79], 0, v140\n\t"
        "v_cmp_ne_u32_e64 s[80:81], 0, v141\n\t"
        "v_cmp_ne_u32_e64 s[82:83], 0, v142\n\t"
        "v_cmp_ne_u32_e64 s[84:85], 0, v143\n\t"
        "s_or_b64 s[92:93], s[78:79], s[80:81]\n\t"
        "s_or_b64 s[92:93], s[92:93], s[82:83]\n\t"
        "s_or_b64 s[92:93], s[92:93], s[84:85]\n\t"
        "s_cbranch_scc1 1b\n\t"
        "3:\n\t"
        "s_waitcnt vmcnt(0)\n\t"
        "v_add_f32 %[a0], v144, v148\n\t"
        "v_add_f32 %[a1], v146, v150\n\t"
        "v_add_f32 v140, v152, v156\n\t"
        "v_add_f32 v141, v154, v158\n\t"
        "v_add_f32 %[a0], %[a0], v140\n\t"
        "v_add_f32 %[a1], %[a1], v141\n\t"
        "s_nop 1"
        : [a0] "=&v"(a0), [a1] "=&v"(a1), [cap] "+&s"(cap) : [vo] "v"(voff), [rs] "s"(rs), [b0] "s"(base), [b1] "s"(base + 4096), [tag] "s"(tag)
        : "v140", "v141", "v142", "v143", "v144", "v145", "v146", "v147", "v148", "v149", "v150", "v151", "v152", "v153", "v154", "v155", "v156", "v157", "v158", "v159", "s78", "s79", "s80", "s81", "s82", "s83", "s84", "s85", "s92", "s93", "scc", "memory");
}
template <> __device__ __forceinline__ void sum_pairs<5, 0>(rsrc_t rs, unsigned voff, unsigned base, unsigned tag, float& a0, float& a1) {
    unsigned cap = SPIN_GLB;
    asm volatile(
        "buffer_load_dwordx4 v[144:147], %[vo], %[rs], %[b0] offen offset:0 sc1\n\t"
        "buffer_load_dwordx4 v[148:151], %[vo], %[rs], %[b0] offen offset:1024 sc1\n\t"
        "buffer_load_dwordx4 v[152:155], %[vo], %[rs], %[b0] offen offset:2048 sc1\n\t"
        "buffer_load_dwordx4 v[156:159], %[vo], %[rs], %[b0] offen offset:3072 sc1\n\t"
        "buffer_load_dwordx4 v[160:163], %[vo], %[rs], %[b1] offen offset:0 sc1\n\t"
        "s_waitcnt vmcnt(0)\n\t"
        "v_bitop3_b32 v140, v145, v147, %[tag] bitop3:0x7e\n\t"
        "v_bitop3_b32 v141, v149, v151, %[tag] bitop3:0x7e\n\t"
        "v_bitop3_b32 v142, v153, v155, %[tag] bitop3:0x7e\n\t"
        "v_bitop3_b32 v143, v157, v159, %[tag] bitop3:0x7e\n\t"
        "v_cmp_ne_u32_e64 s[78:79], 0, v140\n\t"
        "v_cmp_ne_u32_e64 s[80:81], 0, v141\n\t"
        "v_cmp_ne_u32_e64 s[82:83], 0, v142\n\t"
        "v_cmp_ne_u32_e64 s[84:85], 0, v143\n\t"
        "v_bitop3_b32 v140, v161, v163, %[tag] bitop3:0x7e\n\t"
        "v_cmp_ne_u32_e64 s[86:87], 0, v140\n\t"
        "s_or_b64 s[92:93], s[78:79], s[80:81]\n\t"
        "s_or_b64 s[92:93], s[92:93], s[82:83]\n\t"
        "s_or_b64 s[92:93], s[92:93], s[84:85]\n\t"
        "s_or_b64 s[92:93], s[92:93], s[86:87]\n\t"
        "s_cbranch_scc0 3f\n\t"
        "1:\n\t"
        "s_mov_b64 exec, s[78:79]\n\t"
        "buffer_load_dwordx4 v[144:147], %[vo], %[rs], %[b0] offen offset:0 sc1\n\t"
        "s_mov_b64 exec, s[80:81]\n\t"
        "buffer_load_dwordx4 v[148:151], %[vo], %[rs], %[b0] offen offset:1024 sc1\n\t"
        "s_mov_b64 exec, s[82:83]\n\t"
        "buffer_load_dwordx4 v[152:155], %[vo], %[rs], %[b0] offen offset:2048 sc1\n\t"
        "s_mov_b64 exec, s[84:85]\n\t"
        "buffer_load_dwordx4 v[156:159], %[vo], %[rs], %[b0] offen offset:3072 sc1\n\t"
        "s_mov_b64 exec, s[86:87]\n\t"
        "buffer_load_dwordx4 v[160:163], %[vo], %[rs], %[b1] offen offset:0 sc1\n\t"
        "s_mov_b64 exec, -1\n\t"
        "s_sub_u32 %[cap], %[cap], 1\n\t"
        "s_cbranch_scc1 3f\n\t"
        "s_waitcnt vmcnt(0)\n\t"
        "v_bitop3_b32 v140, v145, v147, %[tag] bitop3:0x7e\n\t"
        "v_bitop3_b32 v141, v149, v151, %[tag] bitop3:0x7e\n\t"
        "v_bitop3_b32 v142, v153, v155, %[tag] bitop3:0x7e\n\t"
        "v_bitop3_b32 v143, v157, v159, %[tag] bitop3:0x7e\n\t"
        "v_cmp_ne_u32_e64 s[78:79], 0, v140\n\t"
        "v_cmp_ne_u32_e64 s[80:81], 0, v141\n\t"
        "v_cmp_ne_u32_e64 s[82:83], 0, v142\n\t"
        "v_cmp_ne_u32_e64 s[84:85], 0, v143\n\t"
        "v_bitop3_b32 v140, v161, v163, %[tag] bitop3:0x7e\n\t"
        "v_cmp_ne_u32_e64 s[86:87], 0, v140\n\t"
        "s_or_b64 s[92:93], s[78:79], s[80:81]\n\t"
        "s_or_b64 s[92:93], s[92:93], s[82:83]\n\t"
        "s_or_b64 s[92:93], s[92:93], s[84:85]\n\t"
        "s_or_b64 s[92:93], s[92:93], s[86:87]\n\t"
        "s_cbranch_scc1 1b\n\t"
        "3:\n\t"
        "s_waitcnt vmcnt(0)\n\t"
        "v_add_f32 %[a0], v144, v148\n\t"
        "v_add_f32 %[a1], v146, v150\n\t"
        "v_add_f32 v140, v152, v156\n\t"
        "v_add_f32 v141, v154, v158\n\t"
        "v_add_f32 %[a0], %[a0], v140\n\t"
        "v_add_f32 %[a1], %[a1], v141\n\t"
        "v_add_f32 %[a0], %[a0], v160\n\t"
        "v_add_f32 %[a1], %[a1], v162\n\t"
        "s_nop 1"
        : [a0] "=&v"(a0), [a1] "=&v"(a1), [cap] "+&s"(cap) : [vo] "v"(voff), [rs] "s"(rs), [b0] "s"(base), [b1] "s"(base + 4096), [tag] "s"(tag)
        : "v140", "v141", "v142", "v143", "v144", "v145", "v146", "v147", "v148", "v149", "v150", "v151", "v152", "v153", "v154", "v155", "v156", "v157", "v158", "v159", "v160", "v161", "v162", "v163", "s78", "s79", "s80", "s81", "s82", "s83", "s84", "s85", "s86", "s87", "s92", "s93", "scc", "memory");
}
template <> __device__ __forceinline__ void sum_pairs<5, 1>(rsrc_t rs, unsigned voff, unsigned base, unsigned tag, float& a0, float& a1) {
    unsigned cap = SPIN_GLB;
    asm volatile(
        "buffer_load_dwordx4 v[144:147], %[vo], %[rs], %[b0] offen offset:0 sc1\n\t"
        "buffer_load_dwordx4 v[148:151], %[vo], %[rs], %[b0] offen offset:1024 sc1\n\t"
        "buffer_load_dwordx4 v[152:155], %[vo], %[rs], %[b0] offen offset:2048 sc1\n\t"
        "buffer_load_dwordx4 v[156:159], %[vo], %[rs], %[b0] offen offset:3072 sc1\n\t"
        "buffer_load_dwordx4 v[160:163], %[vo], %[rs], %[b1] offen offset:0 sc1\n\t"
        "s_waitcnt vmcnt(0)\n\t"
        "v_bitop3_b32 v140, v145, v147, %[tag] bitop3:0x7e\n\t"
        "v_bitop3_b32 v141, v149, v151, %[tag] bitop3:0x7e\n\t"
        "v_bitop3_b32 v142, v153, v155, %[tag] bitop3:0x7e\n\t"
        "v_bitop3_b32 v143, v157, v159, %[tag] bitop3:0x7e\n\t"
        "v_cmp_ne_u32_e64 s[78:79], 0, v140\n\t"
        "v_cmp_ne_u32_e64 s[80:81], 0, v141\n\t"
        "v_cmp_ne_u32_e64 s[82:83], 0, v142\n\t"
        "v_cmp_ne_u32_e64 s[84:85], 0, v143\n\t"
        "v_bitop3_b32 v140, v161, v163, %[tag] bitop3:0x7e\n\t"
        "v_cmp_ne_u32_e64 s[86:87], 0, v140\n\t"
        "s_or_b64 s[92:93], s[78:79], s[80:81]\n\t"
        "s_or_b64 s[92:93], s[92:93], s[82:83]\n\t"
        "s_or_b64 s[92:93], s[92:93], s[84:85]\n\t"
        "s_or_b64 s[92:93], s[92:93], s[86:87]\n\t"
        "s_cbranch_scc0 3f\n\t"
        "s_mov_b64 exec, s[78:79]\n\t"
        "buffer_load_dwordx4 v[144:147], %[vo], %[rs], %[b0] offen offset:0 sc1\n\t"
        "s_mov_b64 exec, s[80:81]\n\t"
        "buffer_load_dwordx4 v[148:151], %[vo], %[rs], %[b0] offen offset:1024 sc1\n\t"
        "s_mov_b64 exec, s[82:83]\n\t"
        "buffer_load_dwordx4 v[152:155], %[vo], %[rs], %[b0] offen offset:2048 sc1\n\t"
        "s_mov_b64 exec, s[84:85]\n\t"
        "buffer_load_dwordx4 v[156:159], %[vo], %[rs], %[b0] offen offset:3072 sc1\n\t"
        "s_mov_b64 exec, s[86:87]\n\t"
        "buffer_load_dwordx4 v[160:163], %[vo], %[rs], %[b1] offen offset:0 sc1\n\t"
        "s_mov_b64 exec, -1\n\t"
        "s_sleep 2\n\t"
        "1:\n\t"
        "s_mov_b64 exec, s[78:79]\n\t"
        "buffer_load_dwordx4 v[144:147], %[vo], %[rs], %[b0] offen offset:0 sc1\n\t"
        "s_mov_b64 exec, s[80:81]\n\t"
        "buffer_load_dwordx4 v[148:151], %[vo], %[rs], %[b0] offen offset:1024 sc1\n\t"
        "s_mov_b64 exec, s[82:83]\n\t"
        "buffer_load_dwordx4 v[152:155], %[vo], %[rs], %[b0] offen offset:2048 sc1\n\t"
        "s_mov_b64 exec, s[84:85]\n\t"
        "buffer_load_dwordx4 v[156:159], %[vo], %[rs], %[b0] offen offset:3072 sc1\n\t"
        "s_mov_b64 exec, s[86:87]\n\t"
        "buffer_load_dwordx4 v[160:163], %[vo], %[rs], %[b1] offen offset:0 sc1\n\t"
        "s_mov_b64 exec, -1\n\t"
        "s_sub_u32 %[cap], %[cap], 1\n\t"
        "s_cbranch_scc1 3f\n\t"
        "s_waitcnt vmcnt(5)\n\t"
        "v_bitop3_b32 v140, v145, v147, %[tag] bitop3:0x7e\n\t"
        "v_bitop3_b32 v141, v149, v151, %[tag] bitop3:0x7e\n\t"
        "v_bitop3_b32 v142, v153, v155, %[tag] bitop3:0x7e\n\t"
        "v_bitop3_b32 v143, v157, v159, %[tag] bitop3:0x7e\n\t"
        "v_cmp_ne_u32_e64 s[78:79], 0, v140\n\t"
        "v_cmp_ne_u32_e64 s[80:81], 0, v141\n\t"
        "v_cmp_ne_u32_e64 s[82:83], 0, v142\n\t"
        "v_cmp_ne_u32_e64 s[84:85], 0, v143\n\t"
        "v_bitop3_b32 v140, v161, v163, %[tag] bitop3:0x7e\n\t"
        "v_cmp_ne_u32_e64 s[86:87], 0, v140\n\t"
        "s_or_b64 s[92:93], s[78:79], s[80:81]\n\t"
        "s_or_b64 s[92:93], s[92:93], s[82:83]\n\t"
        "s_or_b64 s[92:93], s[92:93], s[84:85]\n\t"
        "s_or_b64 s[92:93], s[92:93], s[86:87]\n\t"
        "s_cbranch_scc1 1b\n\t"
        "3:\n\t"
        "s_waitcnt vmcnt(0)\n\t"
        "v_add_f32 %[a0], v144, v148\n\t"
        "v_add_f32 %[a1], v146, v150\n\t"
        "v_add_f32 v140, v152, v156\n\t"
        "v_add_f32 v141, v154, v158\n\t"
        "v_add_f32 %[a0], %[a0], v140\n\t"
        "v_add_f32 %[a1], %[a1], v141\n\t"
        "v_add_f32 %[a0], %[a0], v160\n\t"
        "v_add_f32 %[a1], %[a1], v162\n\t"
        "s_nop 1"
        : [a0] "=&v"(a0), [a1] "=&v"(a1), [cap] "+&s"(cap) : [vo] "v"(voff), [rs] "s"(rs), [b0] "s"(base), [b1] "s"(base + 4096), [tag] "s"(tag)
        : "v140", "v141", "v142", "v143", "v144", "v145", "v146", "v147", "v148", "v149", "v150", "v151", "v152", "v153", "v154", "v155", "v156", "v157", "v158", "v159", "v160", "v161", "v162", "v163", "s78", "s79", "s80", "s81", "s82", "s83", "s84", "s85", "s86", "s87", "s92", "s93", "scc", "memory");
}
template <> __device__ __forceinline__ void sum_pairs<6, 0>(rsrc_t rs, unsigned voff, unsigned base, unsigned tag, float& a0, float& a1) {
    unsigned cap = SPIN_GLB;
    asm volatile(
        "buffer_load_dwordx4 v[144:147], %[vo], %[rs], %[b0] offen offset:0 sc1\n\t"
        "buffer_load_dwordx4 v[148:151], %[vo], %[rs], %[b0] offen offset:1024 sc1\n\t"
        "buffer_load_dwordx4 v[152:155], %[vo], %[rs], %[b0] offen offset:2048 sc1\n\t"
        "buffer_load_dwordx4 v[156:159], %[vo], %[rs], %[b0] offen offset:3072 sc1\n\t"
        "buffer_load_dwordx4 v[160:163], %[vo], %[rs], %[b1] offen offset:0 sc1\n\t"
        "buffer_load_dwordx4 v[164:167], %[vo], %[rs], %[b1] offen offset:1024 sc1\n\t"
        "s_waitcnt vmcnt(0)\n\t"
        "v_bitop3_b32 v140, v145, v147, %[tag] bitop3:0x7e\n\t"
        "v_bitop3_b32 v141, v149, v151, %[tag] bitop3:0x7e\n\t"
        "v_bitop3_b32 v142, v153, v155, %[tag] bitop3:0x7e\n\t"
        "v_bitop3_b32 v143, v157, v159, %[tag] bitop3:0x7e\n\t"
        "v_cmp_ne_u32_e64 s[78:79], 0, v140\n\t"
        "v_cmp_ne_u32_e64 s[80:81], 0, v141\n\t"
        "v_cmp_ne_u32_e64 s[82:83], 0, v142\n\t"
        "v_cmp_ne_u32_e64 s[84:85], 0, v143\n\t"
        "v_bitop3_b32 v140, v161, v163, %[tag] bitop3:0x7e\n\t"
        "v_bitop3_b32 v141, v165, v167, %[tag] bitop3:0x7e\n\t"
        "v_cmp_ne_u32_e64 s[86:87], 0, v140\n\t"
        "v_cmp_ne_u32_e64 s[88:89], 0, v141\n\t"
        "s_or_b64 s[92:93], s[78:79], s[80:81]\n\t"
        "s_or_b64 s[92:93], s[92:93], s[82:83]\n\t"
        "s_or_b64 s[92:93], s[92:93], s[84:85]\n\t"
        "s_or_b64 s[92:93], s[92:93], s[86:87]\n\t"
        "s_or_b64 s[92:93], s[92:93], s[88:89]\n\t"
        "s_cbranch_scc0 3f\n\t"
        "1:\n\t"
        "s_mov_b64 exec, s[78:79]\n\t"
        "buffer_load_dwordx4 v[144:147], %[vo], %[rs], %[b0] offen offset:0 sc1\n\t"
        "s_mov_b64 exec, s[80:81]\n\t"
        "buffer_load_dwordx4 v[148:151], %[vo], %[rs], %[b0] offen offset:1024 sc1\n\t"
        "s_mov_b64 exec, s[82:83]\n\t"
        "buffer_load_dwordx4 v[152:155], %[vo], %[rs], %[b0] offen offset:2048 sc1\n\t"
        "s_mov_b64 exec, s[84:85]\n\t"
        "buffer_load_dwordx4 v[156:159], %[vo], %[rs], %[b0] offen offset:3072 sc1\n\t"
        "s_mov_b64 exec, s[86:87]\n\t"
        "buffer_load_dwordx4 v[160:163], %[vo], %[rs], %[b1] offen offset:0 sc1\n\t"
        "s_mov_b64 exec, s[88:89]\n\t"
        "buffer_load_dwordx4 v[164:167], %[vo], %[rs], %[b1] offen offset:1024 sc1\n\t"
        "s_mov_b64 exec, -1\n\t"
        "s_sub_u32 %[cap], %[cap], 1\n\t"
        "s_cbranch_scc1 3f\n\t"
        "s_waitcnt vmcnt(0)\n\t"
        "v_bitop3_b32 v140, v145, v147, %[tag] bitop3:0x7e\n\t"
        "v_bitop3_b32 v141, v149, v151, %[tag] bitop3:0x7e\n\t"
        "v_bitop3_b32 v142, v153, v155, %[tag] bitop3:0x7e\n\t"
        "v_bitop3_b32 v143, v157, v159, %[tag] bitop3:0x7e\n\t"
        "v_cmp_ne_u32_e64 s[78:79], 0, v140\n\t"
        "v_cmp_ne_u32_e64 s[80:81], 0, v141\n\t"
        "v_cmp_ne_u32_e64 s[82:83], 0, v142\n\t"
        "v_cmp_ne_u32_e64 s[84:85], 0, v143\n\t"
        "v_bitop3_b32 v140, v161, v163, %[tag] bitop3:0x7e\n\t"
        "v_bitop3_b32 v141, v165, v167, %[tag] bitop3:0x7e\n\t"
        "v_cmp_ne_u32_e64 s[86:87], 0, v140\n\t"
        "v_cmp_ne_u32_e64 s[88:89], 0, v141\n\t"
        "s_or_b64 s[92:93], s[78:79], s[80:81]\n\t"
        "s_or_b64 s[92:93], s[92:93], s[82:83]\n\t"
        "s_or_b64 s[92:93], s[92:93], s[84:85]\n\t"
        "s_or_b64 s[92:93], s[92:93], s[86:87]\n\t"
        "s_or_b64 s[92:93], s[92:93], s[88:89]\n\t"
        "s_cbranch_scc1 1b\n\t"
        "3:\n\t"
        "s_waitcnt vmcnt(0)\n\t"
        "v_add_f32 %[a0], v144, v148\n\t"
        "v_add_f32 %[a1], v146, v150\n\t"
        "v_add_f32 v140, v152, v156\n\t"
        "v_add_f32 v141, v154, v158\n\t"
        "v_add_f32 v142, v160, v164\n\t"
        "v_add_f32 v143, v162, v166\n\t"
        "v_add_f32 %[a0], %[a0], v140\n\t"
        "v_add_f32 %[a1], %[a1], v141\n\t"
        "v_add_f32 %[a0], %[a0], v142\n\t"
        "v_add_f32 %[a1], %[a1], v143\n\t"
        "s_nop 1"
        : [a0] "=&v"(a0), [a1] "=&v"(a1), [cap] "+&s"(cap) : [vo] "v"(voff), [rs] "s"(rs), [b0] "s"(base), [b1] "s"(base + 4096), [tag] "s"(tag)
        : "v140", "v141", "v142", "v143", "v144", "v145", "v146", "v147", "v148", "v149", "v150", "v151", "v152", "v153", "v154", "v155", "v156", "v157", "v158", "v159", "v160", "v161", "v162", "v163", "v164", "v165", "v166", "v167", "s78", "s79", "s80", "s81", "s82", "s83", "s84", "s85", "s86", "s87", "s88", "s89", "s92", "s93", "scc", "memory");
}
template <> __device__ __forceinline__ void sum_pairs<6, 1>(rsrc_t rs, unsigned voff, unsigned base, unsigned tag, float& a0, float& a1) {
    unsigned cap = SPIN_GLB;
    asm volatile(
        "buffer_load_dwordx4 v[144:147], %[vo], %[rs], %[b0] offen offset:0 sc1\n\t"
        "buffer_load_dwordx4 v[148:151], %[vo], %[rs], %[b0] offen offset:1024 sc1\n\t"
        "buffer_load_dwordx4 v[152:155], %[vo], %[rs], %[b0] offen offset:2048 sc1\n\t"
        "buffer_load_dwordx4 v[156:159], %[vo], %[rs], %[b0] offen offset:3072 sc1\n\t"
        "buffer_load_dwordx4 v[160:163], %[vo], %[rs], %[b1] offen offset:0 sc1\n\t"
        "buffer_load_dwordx4 v[164:167], %[vo], %[rs], %[b1] offen offset:1024 sc1\n\t"
        "s_waitcnt vmcnt(0)\n\t"
        "v_bitop3_b32 v140, v145, v147, %[tag] bitop3:0x7e\n\t"
        "v_bitop3_b32 v141, v149, v151, %[tag] bitop3:0x7e\n\t"
        "v_bitop3_b32 v142, v153, v155, %[tag] bitop3:0x7e\n\t"
        "v_bitop3_b32 v143, v157, v159, %[tag] bitop3:0x7e\n\t"
        "v_cmp_ne_u32_e64 s[78:79], 0, v140\n\t"
        "v_cmp_ne_u32_e64 s[80:81], 0, v141\n\t"
        "v_cmp_ne_u32_e64 s[82:83], 0, v142\n\t"
        "v_cmp_ne_u32_e64 s[84:85], 0, v143\n\t"
        "v_bitop3_b32 v140, v161, v163, %[tag] bitop3:0x7e\n\t"
        "v_bitop3_b32 v141, v165, v167, %[tag] bitop3:0x7e\n\t"
        "v_cmp_ne_u32_e64 s[86:87], 0, v140\n\t"
        "v_cmp_ne_u32_e64 s[88:89], 0, v141\n\t"
        "s_or_b64 s[92:93], s[78:79], s[80:81]\n\t"
        "s_or_b64 s[92:93], s[92:93], s[82:83]\n\t"
        "s_or_b64 s[92:93], s[92:93], s[84:85]\n\t"
        "s_or_b64 s[92:93], s[92:93], s[86:87]\n\t"
        "s_or_b64 s[92:93], s[92:93], s[88:89]\n\t"
        "s_cbranch_scc0 3f\n\t"
        "s_mov_b64 exec, s[78:79]\n\t"
        "buffer_load_dwordx4 v[144:147], %[vo], %[rs], %[b0] offen offset:0 sc1\n\t"
        "s_mov_b64 exec, s[80:81]\n\t"
        "buffer_load_dwordx4 v[148:151], %[vo], %[rs], %[b0] offen offset:1024 sc1\n\t"
        "s_mov_b64 exec, s[82:83]\n\t"
        "buffer_load_dwordx4 v[152:155], %[vo], %[rs], %[b0] offen offset:2048 sc1\n\t"
        "s_mov_b64 exec, s[84:85]\n\t"
        "buffer_load_dwordx4 v[156:159], %[vo], %[rs], %[b0] offen offset:3072 sc1\n\t"
        "s_mov_b64 exec, s[86:87]\n\t"
        "buffer_load_dwordx4 v[160:163], %[vo], %[rs], %[b1] offen offset:0 sc1\n\t"
        "s_mov_b64 exec, s[88:89]\n\t"
        "buffer_load_dwordx4 v[164:167], %[vo], %[rs], %[b1] offen offset:1024 sc1\n\t"
        "s_mov_b64 exec, -1\n\t"
        "s_sleep 2\n\t"
        "1:\n\t"
        "s_mov_b64 exec, s[78:79]\n\t"
        "buffer_load_dwordx4 v[144:147], %[vo], %[rs], %[b0] offen offset:0 sc1\n\t"
        "s_mov_b64 exec, s[80:81]\n\t"
        "buffer_load_dwordx4 v[148:151], %[vo], %[rs], %[b0] offen offset:1024 sc1\n\t"
        "s_mov_b64 exec, s[82:83]\n\t"
        "buffer_load_dwordx4 v[152:155], %[vo], %[rs], %[b0] offen offset:2048 sc1\n\t"
        "s_mov_b64 exec, s[84:85]\n\t"
        "buffer_load_dwordx4 v[156:159], %[vo], %[rs], %[b0] offen offset:3072 sc1\n\t"
        "s_mov_b64 exec, s[86:87]\n\t"
        "buffer_load_dwordx4 v[160:163], %[vo], %[rs], %[b1] offen offset:0 sc1\n\t"
        "s_mov_b64 exec, s[88:89]\n\t"
        "buffer_load_dwordx4 v[164:167], %[vo], %[rs], %[b1] offen offset:1024 sc1\n\t"
        "s_mov_b64 exec, -1\n\t"
        "s_sub_u32 %[cap], %[cap], 1\n\t"
        "s_cbranch_scc1 3f\n\t"
        "s_waitcnt vmcnt(6)\n\t"
        "v_bitop3_b32 v140, v145, v147, %[tag] bitop3:0x7e\n\t"
        "v_bitop3_b32 v141, v149, v151, %[tag] bitop3:0x7e\n\t"
        "v_bitop3_b32 v142, v153, v155, %[tag] bitop3:0x7e\n\t"
        "v_bitop3_b32 v143, v157, v159, %[tag] bitop3:0x7e\n\t"
        "v_cmp_ne_u32_e64 s[78:79], 0, v140\n\t"
        "v_cmp_ne_u32_e64 s[80:81], 0, v141\n\t"
        "v_cmp_ne_u32_e64 s[82:83], 0, v142\n\t"
        "v_cmp_ne_u32_e64 s[84:85], 0, v143\n\t"
        "v_bitop3_b32 v140, v161, v163, %[tag] bitop3:0x7e\n\t"
        "v_bitop3_b32 v141, v165, v167, %[tag] bitop3:0x7e\n\t"
        "v_cmp_ne_u32_e64 s[86:87], 0, v140\n\t"
        "v_cmp_ne_u32_e64 s[88:89], 0, v141\n\t"
        "s_or_b64 s[92:93], s[78:79], s[80:81]\n\t"
        "s_or_b64 s[92:93], s[92:93], s[82:83]\n\t"
        "s_or_b64 s[92:93], s[92:93], s[84:85]\n\t"
        "s_or_b64 s[92:93], s[92:93], s[86:87]\n\t"
        "s_or_b64 s[92:93], s[92:93], s[88:89]\n\t"
        "s_cbranch_scc1 1b\n\t"
        "3:\n\t"
        "s_waitcnt vmcnt(0)\n\t"
        "v_add_f32 %[a0], v144, v148\n\t"
        "v_add_f32 %[a1], v146, v150\n\t"
        "v_add_f32 v140, v152, v156\n\t"
        "v_add_f32 v141, v154, v158\n\t"
        "v_add_f32 v142, v160, v164\n\t"
        "v_add_f32 v143, v162, v166\n\t"
        "v_add_f32 %[a0], %[a0], v140\n\t"
        "v_add_f32 %[a1], %[a1], v141\n\t"
        "v_add_f32 %[a0], %[a0], v142\n\t"
        "v_add_f32 %[a1], %[a1], v143\n\t"
        "s_nop 1"
        : [a0] "=&v"(a0), [a1] "=&v"(a1), [cap] "+&s"(cap) : [vo] "v"(voff), [rs] "s"(rs), [b0] "s"(base), [b1] "s"(base + 4096), [tag] "s"(tag)
        : "v140", "v141", "v142", "v143", "v144", "v145", "v146", "v147", "v148", "v149", "v150", "v151", "v152", "v153", "v154", "v155", "v156", "v157", "v158", "v159", "v160", "v161", "v162", "v163", "v164", "v165", "v166", "v167", "s78", "s79", "s80", "s81", "s82", "s83", "s84", "s85", "s86", "s87", "s88", "s89", "s92", "s93", "scc", "memory");
}
template <> __device__ __forceinline__ float combine_o<0>(rsrc_t rs, unsigned voff, unsigned base, unsigned tag, int j, unsigned own, f32x4 w0, f32x4 w1, unsigned napn) {
    unsigned cap = SPIN_GLB;
    float o;
    asm volatile(
        "s_cmp_eq_u32 %[j], 0\n\t"
        "s_cselect_b64 s[74:75], 0, %[lm]\n\t"
        "v_mov_b32 v144, %[own]\n\t"
        "v_mov_b32 v145, %[tag]\n\t"
        "s_cmp_eq_u32 %[j], 1\n\t"
        "s_cselect_b64 s[76:77], 0, %[lm]\n\t"
        "v_mov_b32 v146, %[own]\n\t"
        "v_mov_b32 v147, %[tag]\n\t"
        "s_cmp_eq_u32 %[j], 2\n\t"
        "s_cselect_b64 s[78:79], 0, %[lm]\n\t"
        "v_mov_b32 v148, %[own]\n\t"
        "v_mov_b32 v149, %[tag]\n\t"
        "s_cmp_eq_u32 %[j], 3\n\t"
        "s_cselect_b64 s[80:81], 0, %[lm]\n\t"
        "v_mov_b32 v150, %[own]\n\t"
        "v_mov_b32 v151, %[tag]\n\t"
        "s_cmp_eq_u32 %[j], 4\n\t"
        "s_cselect_b64 s[82:83], 0, %[lm]\n\t"
        "v_mov_b32 v152, %[own]\n\t"
        "v_mov_b32 v153, %[tag]\n\t"
        "s_cmp_eq_u32 %[j], 5\n\t"
        "s_cselect_b64 s[84:85], 0, %[lm]\n\t"
        "v_mov_b32 v154, %[own]\n\t"
        "v_mov_b32 v155, %[tag]\n\t"
        "s_cmp_eq_u32 %[j], 6\n\t"
        "s_cselect_b64 s[86:87], 0, %[lm]\n\t"
        "v_mov_b32 v156, %[own]\n\t"
        "v_mov_b32 v157, %[tag]\n\t"
        "s_cmp_eq_u32 %[j], 7\n\t"
        "s_cselect_b64 s[88:89], 0, %[lm]\n\t"
        "v_mov_b32 v158, %[own]\n\t"
        "v_mov_b32 v159, %[tag]\n\t"
        "s_cmp_eq_u32 %[nap], 0\n\t"
        "s_cbranch_scc1 5f\n\t"
        "4:\n\t"
        "s_sleep 1\n\t"
        "s_sub_u32 %[nap], %[nap], 1\n\t"
        "s_cmp_lg_u32 %[nap], 0\n\t"
        "s_cbranch_scc1 4b\n\t"
        "5:\n\t"
        "s_mov_b64 exec, s[74:75]\n\t"
        "buffer_load_dwordx2 v[144:145], %[vo], %[rs], %[b0] offen offset:0 sc1\n\t"
        "s_mov_b64 exec, s[76:77]\n\t"
        "buffer_load_dwordx2 v[146:147], %[vo], %[rs], %[b0] offen offset:1536 sc1\n\t"
        "s_mov_b64 exec, s[78:79]\n\t"
        "buffer_load_dwordx2 v[148:149], %[vo], %[rs], %[b0] offen offset:3072 sc1\n\t"
        "s_mov_b64 exec, s[80:81]\n\t"
        "buffer_load_dwordx2 v[150:151], %[vo], %[rs], %[b1] offen offset:0 sc1\n\t"
        "s_mov_b64 exec, s[82:83]\n\t"
        "buffer_load_dwordx2 v[152:153], %[vo], %[rs], %[b1] offen offset:1536 sc1\n\t"
        "s_mov_b64 exec, s[84:85]\n\t"
        "buffer_load_dwordx2 v[154:155], %[vo], %[rs], %[b1] offen offset:3072 sc1\n\t"
        "s_mov_b64 exec, s[86:87]\n\t"
        "buffer_load_dwordx2 v[156:157], %[vo], %[rs], %[b2] offen offset:0 sc1\n\t"
        "s_mov_b64 exec, s[88:89]\n\t"
        "buffer_load_dwordx2 v[158:159], %[vo], %[rs], %[b2] offen offset:1536 sc1\n\t"
        "s_mov_b64 exec, -1\n\t"
        "s_waitcnt vmcnt(0)\n\t"
        "v_cmp_ne_u32_e64 s[74:75], %[tag], v145\n\t"
        "v_cmp_ne_u32_e64 s[76:77], %[tag], v147\n\t"
        "v_cmp_ne_u32_e64 s[78:79], %[tag], v149\n\t"
        "v_cmp_ne_u32_e64 s[80:81], %[tag], v151\n\t"
        "v_cmp_ne_u32_e64 s[82:83], %[tag], v153\n\t"
        "v_cmp_ne_u32_e64 s[84:85], %[tag], v155\n\t"
        "v_cmp_ne_u32_e64 s[86:87], %[tag], v157\n\t"
        "v_cmp_ne_u32_e64 s[88:89], %[tag], v159\n\t"
        "s_or_b64 s[92:93], s[74:75], s[76:77]\n\t"
        "s_or_b64 s[92:93], s[92:93], s[78:79]\n\t"
        "s_or_b64 s[92:93], s[92:93], s[80:81]\n\t"
        "s_or_b64 s[92:93], s[92:93], s[82:83]\n\t"
        "s_or_b64 s[92:93], s[92:93], s[84:85]\n\t"
        "s_or_b64 s[92:93], s[92:93], s[86:87]\n\t"
        "s_or_b64 s[92:93], s[92:93], s[88:89]\n\t"
        "s_cbranch_scc0 3f\n\t"
        "1:\n\t"
        "s_mov_b64 exec, s[74:75]\n\t"
        "buffer_load_dwordx2 v[144:145], %[vo], %[rs], %[b0] offen offset:0 sc1\n\t"
        "s_mov_b64 exec, s[76:77]\n\t"
        "buffer_load_dwordx2 v[146:147], %[vo], %[rs], %[b0] offen offset:1536 sc1\n\t"
        "s_mov_b64 exec, s[78:79]\n\t"
        "buffer_load_dwordx2 v[148:149], %[vo], %[rs], %[b0] offen offset:3072 sc1\n\t"
        "s_mov_b64 exec, s[80:81]\n\t"
        "buffer_load_dwordx2 v[150:151], %[vo], %[rs], %[b1] offen offset:0 sc1\n\t"
        "s_mov_b64 exec, s[82:83]\n\t"
        "buffer_load_dwordx2 v[152:153], %[vo], %[rs], %[b1] offen offset:1536 sc1\n\t"
        "s_mov_b64 exec, s[84:85]\n\t"
        "buffer_load_dwordx2 v[154:155], %[vo], %[rs], %[b1] offen offset:3072 sc1\n\t"
        "s_mov_b64 exec, s[86:87]\n\t"
        "buffer_load_dwordx2 v[156:157], %[vo], %[rs], %[b2] offen offset:0 sc1\n\t"
        "s_mov_b64 exec, s[88:89]\n\t"
        "buffer_load_dwordx2 v[158:159], %[vo], %[rs], %[b2] offen offset:1536 sc1\n\t"
        "s_mov_b64 exec, -1\n\t"
        "s_sub_u32 %[cap], %[cap], 1\n\t"
        "s_cbranch_scc1 3f\n\t"
        "s_waitcnt vmcnt(0)\n\t"
        "v_cmp_ne_u32_e64 s[74:75], %[tag], v145\n\t"
        "v_cmp_ne_u32_e64 s[76:77], %[tag], v147\n\t"
        "v_cmp_ne_u32_e64 s[78:79], %[tag], v149\n\t"
        "v_cmp_ne_u32_e64 s[80:81], %[tag], v151\n\t"
        "v_cmp_ne_u32_e64 s[82:83], %[tag], v153\n\t"
        "v_cmp_ne_u32_e64 s[84:85], %[tag], v155\n\t"
        "v_cmp_ne_u32_e64 s[86:87], %[tag], v157\n\t"
        "v_cmp_ne_u32_e64 s[88:89], %[tag], v159\n\t"
        "s_or_b64 s[92:93], s[74:75], s[76:77]\n\t"
        "s_or_b64 s[92:93], s[92:93], s[78:79]\n\t"
        "s_or_b64 s[92:93], s[92:93], s[80:81]\n\t"
        "s_or_b64 s[92:93], s[92:93], s[82:83]\n\t"
        "s_or_b64 s[92:93], s[92:93], s[84:85]\n\t"
        "s_or_b64 s[92:93], s[92:93], s[86:87]\n\t"
        "s_or_b64 s[92:93], s[92:93], s[88:89]\n\t"
        "s_cbranch_scc1 1b\n\t"
        "3:\n\t"
        "s_waitcnt vmcnt(0)\n\t"
        "v_mul_f32 %[o], %[w0], v144\n\t"
        "v_mul_f32 v140, %[w4], v152\n\t"
        "v_fmac_f32 %[o], %[w1], v146\n\t"
        "v_fmac_f32 v140, %[w5], v154\n\t"
        "v_fmac_f32 %[o], %[w2], v148\n\t"
        "v_fmac_f32 v140, %[w6], v156\n\t"
        "v_fmac_f32 %[o], %[w3], v150\n\t"
        "v_fmac_f32 v140, %[w7], v158\n\t"
        "v_add_f32 %[o], %[o], v140\n\t"
        : [o] "=&v"(o), [cap] "+&s"(cap), [nap] "+&s"(napn)
        : [vo] "v"(voff), [rs] "s"(rs), [b0] "s"(base), [b1] "s"(base + 4608), [b2] "s"(base + 9216), [tag] "s"(tag), [j] "s"(j), [own] "v"(own),
          [lm] "s"(0xFFFFFFFFFFFFull), [w0] "v"(w0[0]), [w1] "v"(w0[1]), [w2] "v"(w0[2]), [w3] "v"(w0[3]), [w4] "v"(w1[0]), [w5] "v"(w1[1]), [w6] "v"(w1[2]), [w7] "v"(w1[3])
        : "v140", "v144", "v145", "v146", "v147", "v148", "v149", "v150", "v151", "v152", "v153", "v154", "v155", "v156", "v157", "v158", "v159", "s74", "s75", "s76", "s77", "s78", "s79", "s80", "s81", "s82", "s83", "s84", "s85", "s86", "s87", "s88", "s89", "s92", "s93", "scc", "memory");
    return o;
}
template <> __device__ __forceinline__ float combine_o<1>(rsrc_t rs, unsigned voff, unsigned base, unsigned tag, int j, unsigned own, f32x4 w0, f32x4 w1, unsigned napn) {
    unsigned cap = SPIN_GLB;
    float o;
    asm volatile(
        "s_cmp_eq_u32 %[j], 0\n\t"
        "s_cselect_b64 s[74:75], 0, %[lm]\n\t"
        "v_mov_b32 v144, %[own]\n\t"
        "v_mov_b32 v145, %[tag]\n\t"
        "s_cmp_eq_u32 %[j], 1\n\t"
        "s_cselect_b64 s[76:77], 0, %[lm]\n\t"
        "v_mov_b32 v146, %[own]\n\t"
        "v_mov_b32 v147, %[tag]\n\t"
        "s_cmp_eq_u32 %[j], 2\n\t"
        "s_cselect_b64 s[78:79], 0, %[lm]\n\t"
        "v_mov_b32 v148, %[own]\n\t"
        "v_mov_b32 v149, %[tag]\n\t"
        "s_cmp_eq_u32 %[j], 3\n\t"
        "s_cselect_b64 s[80:81], 0, %[lm]\n\t"
        "v_mov_b32 v150, %[own]\n\t"
        "v_mov_b32 v151, %[tag]\n\t"
        "s_cmp_eq_u32 %[j], 4\n\t"
        "s_cselect_b64 s[82:83], 0, %[lm]\n\t"
        "v_mov_b32 v152, %[own]\n\t"
        "v_mov_b32 v153, %[tag]\n\t"
        "s_cmp_eq_u32 %[j], 5\n\t"
        "s_cselect_b64 s[84:85], 0, %[lm]\n\t"
        "v_mov_b32 v154, %[own]\n\t"
        "v_mov_b32 v155, %[tag]\n\t"
        "s_cmp_eq_u32 %[j], 6\n\t"
        "s_cselect_b64 s[86:87], 0, %[lm]\n\t"
        "v_mov_b32 v156, %[own]\n\t"
        "v_mov_b32 v157, %[tag]\n\t"
        "s_cmp_eq_u32 %[j], 7\n\t"
        "s_cselect_b64 s[88:89], 0, %[lm]\n\t"
        "v_mov_b32 v158, %[own]\n\t"
        "v_mov_b32 v159, %[tag]\n\t"
        "s_cmp_eq_u32 %[nap], 0\n\t"
        "s_cbranch_scc1 5f\n\t"
        "4:\n\t"
        "s_sleep 1\n\t"
        "s_sub_u32 %[nap], %[nap], 1\n\t"
        "s_cmp_lg_u32 %[nap], 0\n\t"
        "s_cbranch_scc1 4b\n\t"
        "5:\n\t"
        "s_mov_b64 exec, s[74:75]\n\t"
        "buffer_load_dwordx2 v[144:145], %[vo], %[rs], %[b0] offen offset:0 sc1\n\t"
        "s_mov_b64 exec, s[76:77]\n\t"
        "buffer_load_dwordx2 v[146:147], %[vo], %[rs], %[b0] offen offset:1536 sc1\n\t"
        "s_mov_b64 exec, s[78:79]\n\t"
        "buffer_load_dwordx2 v[148:149], %[vo], %[rs], %[b0] offen offset:3072 sc1\n\t"
        "s_mov_b64 exec, s[80:81]\n\t"
        "buffer_load_dwordx2 v[150:151], %[vo], %[rs], %[b1] offen offset:0 sc1\n\t"
        "s_mov_b64 exec, s[82:83]\n\t"
        "buffer_load_dwordx2 v[152:153], %[vo], %[rs], %[b1] offen offset:1536 sc1\n\t"
        "s_mov_b64 exec, s[84:85]\n\t"
        "buffer_load_dwordx2 v[154:155], %[vo], %[rs], %[b1] offen offset:3072 sc1\n\t"
        "s_mov_b64 exec, s[86:87]\n\t"
        "buffer_load_dwordx2 v[156:157], %[vo], %[rs], %[b2] offen offset:0 sc1\n\t"
        "s_mov_b64 exec, s[88:89]\n\t"
        "buffer_load_dwordx2 v[158:159], %[vo], %[rs], %[b2] offen offset:1536 sc1\n\t"
        "s_mov_b64 exec, -1\n\t"
        "s_waitcnt vmcnt(0)\n\t"
        "v_cmp_ne_u32_e64 s[74:75], %[tag], v145\n\t"
        "v_cmp_ne_u32_e64 s[76:77], %[tag], v147\n\t"
        "v_cmp_ne_u32_e64 s[78:79], %[tag], v149\n\t"
        "v_cmp_ne_u32_e64 s[80:81], %[tag], v151\n\t"
        "v_cmp_ne_u32_e64 s[82:83], %[tag], v153\n\t"
        "v_cmp_ne_u32_e64 s[84:85], %[tag], v155\n\t"
        "v_cmp_ne_u32_e64 s[86:87], %[tag], v157\n\t"
        "v_cmp_ne_u32_e64 s[88:89], %[tag], v159\n\t"
        "s_or_b64 s[92:93], s[74:75], s[76:77]\n\t"
        "s_or_b64 s[92:93], s[92:93], s[78:79]\n\t"
        "s_or_b64 s[92:93], s[92:93], s[80:81]\n\t"
        "s_or_b64 s[92:93], s[92:93], s[82:83]\n\t"
        "s_or_b64 s[92:93], s[92:93], s[84:85]\n\t"
        "s_or_b64 s[92:93], s[92:93], s[86:87]\n\t"
        "s_or_b64 s[92:93], s[92:93], s[88:89]\n\t"
        "s_cbranch_scc0 3f\n\t"
        "s_mov_b64 exec, s[74:75]\n\t"
        "buffer_load_dwordx2 v[144:145], %[vo], %[rs], %[b0] offen offset:0 sc1\n\t"
        "s_mov_b64 exec, s[76:77]\n\t"
        "buffer_load_dwordx2 v[146:147], %[vo], %[rs], %[b0] offen offset:1536 sc1\n\t"
        "s_mov_b64 exec, s[78:79]\n\t"
        "buffer_load_dwordx2 v[148:149], %[vo], %[rs], %[b0] offen offset:3072 sc1\n\t"
        "s_mov_b64 exec, s[80:81]\n\t"
        "buffer_load_dwordx2 v[150:151], %[vo], %[rs], %[b1] offen offset:0 sc1\n\t"
        "s_mov_b64 exec, s[82:83]\n\t"
        "buffer_load_dwordx2 v[152:153], %[vo], %[rs], %[b1] offen offset:1536 sc1\n\t"
        "s_mov_b64 exec, s[84:85]\n\t"
        "buffer_load_dwordx2 v[154:155], %[vo], %[rs], %[b1] offen offset:3072 sc1\n\t"
        "s_mov_b64 exec, s[86:87]\n\t"
        "buffer_load_dwordx2 v[156:157], %[vo], %[rs], %[b2] offen offset:0 sc1\n\t"
        "s_mov_b64 exec, s[88:89]\n\t"
        "buffer_load_dwordx2 v[158:159], %[vo], %[rs], %[b2] offen offset:1536 sc1\n\t"
        "s_mov_b64 exec, -1\n\t"
        "s_sleep 2\n\t"
        "1:\n\t"
        "s_mov_b64 exec, s[74:75]\n\t"
        "buffer_load_dwordx2 v[144:145], %[vo], %[rs], %[b0] offen offset:0 sc1\n\t"
        "s_mov_b64 exec, s[76:77]\n\t"
        "buffer_load_dwordx2 v[146:147], %[vo], %[rs], %[b0] offen offset:1536 sc1\n\t"
        "s_mov_b64 exec, s[78:79]\n\t"
        "buffer_load_dwordx2 v[148:149], %[vo], %[rs], %[b0] offen offset:3072 sc1\n\t"
        "s_mov_b64 exec, s[80:81]\n\t"
        "buffer_load_dwordx2 v[150:151], %[vo], %[rs], %[b1] offen offset:0 sc1\n\t"
        "s_mov_b64 exec, s[82:83]\n\t"
        "buffer_load_dwordx2 v[152:153], %[vo], %[rs], %[b1] offen offset:1536 sc1\n\t"
        "s_mov_b64 exec, s[84:85]\n\t"
        "buffer_load_dwordx2 v[154:155], %[vo], %[rs], %[b1] offen offset:3072 sc1\n\t"
        "s_mov_b64 exec, s[86:87]\n\t"
        "buffer_load_dwordx2 v[156:157], %[vo], %[rs], %[b2] offen offset:0 sc1\n\t"
        "s_mov_b64 exec, s[88:89]\n\t"
        "buffer_load_dwordx2 v[158:159], %[vo], %[rs], %[b2] offen offset:1536 sc1\n\t"
        "s_mov_b64 exec, -1\n\t"
        "s_sub_u32 %[cap], %[cap], 1\n\t"
        "s_cbranch_scc1 3f\n\t"
        "s_waitcnt vmcnt(8)\n\t"
        "v_cmp_ne_u32_e64 s[74:75], %[tag], v145\n\t"
        "v_cmp_ne_u32_e64 s[76:77], %[tag], v147\n\t"
        "v_cmp_ne_u32_e64 s[78:79], %[tag], v149\n\t"
        "v_cmp_ne_u32_e64 s[80:81], %[tag], v151\n\t"
        "v_cmp_ne_u32_e64 s[82:83], %[tag], v153\n\t"
        "v_cmp_ne_u32_e64 s[84:85], %[tag], v155\n\t"
        "v_cmp_ne_u32_e64 s[86:87], %[tag], v157\n\t"
        "v_cmp_ne_u32_e64 s[88:89], %[tag], v159\n\t"
        "s_or_b64 s[92:93], s[74:75], s[76:77]\n\t"
        "s_or_b64 s[92:93], s[92:93], s[78:79]\n\t"
        "s_or_b64 s[92:93], s[92:93], s[80:81]\n\t"
        "s_or_b64 s[92:93], s[92:93], s[82:83]\n\t"
        "s_or_b64 s[92:93], s[92:93], s[84:85]\n\t"
        "s_or_b64 s[92:93], s[92:93], s[86:87]\n\t"
        "s_or_b64 s[92:93], s[92:93], s[88:89]\n\t"
        "s_cbranch_scc1 1b\n\t"
        "3:\n\t"
        "s_waitcnt vmcnt(0)\n\t"
        "v_mul_f32 %[o], %[w0], v144\n\t"
        "v_mul_f32 v140, %[w4], v152\n\t"
        "v_fmac_f32 %[o], %[w1], v146\n\t"
        "v_fmac_f32 v140, %[w5], v154\n\t"
        "v_fmac_f32 %[o], %[w2], v148\n\t"
        "v_fmac_f32 v140, %[w6], v156\n\t"
        "v_fmac_f32 %[o], %[w3], v150\n\t"
        "v_fmac_f32 v140, %[w7], v158\n\t"
        "v_add_f32 %[o], %[o], v140\n\t"
        : [o] "=&v"(o), [cap] "+&s"(cap), [nap] "+&s"(napn)
        : [vo] "v"(voff), [rs] "s"(rs), [b0] "s"(base), [b1] "s"(base + 4608), [b2] "s"(base + 9216), [tag] "s"(tag), [j] "s"(j), [own] "v"(own),
          [lm] "s"(0xFFFFFFFFFFFFull), [w0] "v"(w0[0]), [w1] "v"(w0[1]), [w2] "v"(w0[2]), [w3] "v"(w0[3]), [w4] "v"(w1[0]), [w5] "v"(w1[1]), [w6] "v"(w1[2]), [w7] "v"(w1[3])
        : "v140", "v144", "v145", "v146", "v147", "v148", "v149", "v150", "v151", "v152", "v153", "v154", "v155", "v156", "v157", "v158", "v159", "s74", "s75", "s76", "s77", "s78", "s79", "s80", "s81", "s82", "s83", "s84", "s85", "s86", "s87", "s88", "s89", "s92", "s93", "scc", "memory");
    return o;
}
#define PUMP 0
__device__ __forceinline__ s16x4 trd(LAS unsigned char* L, unsigned off) { return __builtin_amdgcn_ds_read_tr16_b64_v4i16((LAS s16x4*)(L + off)); }
__device__ __forceinline__ bf16x8 cat8(s16x4 a, s16x4 b) { return __builtin_bit_cast(bf16x8, __builtin_shufflevector(a, b, 0, 1, 2, 3, 4, 5, 6, 7)); }
__device__ __forceinline__ f32x4 mfma32(bf16x8 a, bf16x8 b, f32x4 c) { return __builtin_amdgcn_mfma_f32_16x16x32_bf16(a, b, c, 0, 0, 0); }
__device__ __forceinline__ bf16x8 asb(v4u a) { return __builtin_bit_cast(bf16x8, a); }

__device__ __forceinline__ void ln_stats(const unsigned (&xp)[5], float& mb, float& rden) {
    const float c = lo16(__builtin_amdgcn_readfirstlane(xp[0]));
    float s = 0.f, s2 = 0.f;
#pragma unroll
    for (int i = 0; i < 5; ++i) {
        const float a = lo16(xp[i]) - c, b = hi16(xp[i]) - c;
        s += a + b; s2 = fmaf(a, a, s2); s2 = fmaf(b, b, s2);
    }
    s = wave_sum(s); s2 = wave_sum(s2);
    const float dm = s * (1.0f / D);
    const float var = fmaxf(s2 * (1.0f / D) - dm * dm, 0.f);
    mb = rbf(c + dm);
    const float den = rbf(__builtin_amdgcn_sqrtf(rbf(rbf(var) + 1e-5f)));
    rden = __builtin_amdgcn_rcpf(den);
}
__device__ __forceinline__ unsigned ln_pair(unsigned w, float mb, float rden) {
    const unsigned d = pk2(lo16(w) - mb, hi16(w) - mb);
    return pk2(lo16(d) * rden, hi16(d) * rden);
}

template <int N> __device__ __forceinline__ unsigned reduce_cols(rsrc_t rs, unsigned voff, unsigned base, unsigned tag, unsigned res) {
    float r0 = lo16(res), r1 = hi16(res);
    PIN(r0); PIN(r1);
    float s0, s1;
    sum_pairs<N, PUMP>(rs, voff, base, tag, s0, s1);
    auto sw = __builtin_amdgcn_permlane32_swap(__float_as_uint(s0), __float_as_uint(s1), false, false);
    float t = fv(sw[0]) + fv(sw[1]);
    t += dpp<XOR8>(t);
    t = sw16_sum(t);
    auto s2 = __builtin_amdgcn_permlane32_swap(__float_as_uint(t), __float_as_uint(t), false, false);
    const unsigned sp = pk2(fv(s2[0]), fv(s2[1]));
    return pk2(r0 + lo16(sp), r1 + hi16(sp));
}
__device__ __forceinline__ float fold5(const v4u (&p)[5]) {
    const float a0 = ((fv(p[0][0]) + fv(p[1][0])) + (fv(p[2][0]) + fv(p[3][0]))) + fv(p[4][0]);
    const float a1 = ((fv(p[0][2]) + fv(p[1][2])) + (fv(p[2][2]) + fv(p[3][2]))) + fv(p[4][2]);
    auto sw = __builtin_amdgcn_permlane32_swap(__float_as_uint(a0), __float_as_uint(a1), false, false);
    return fv(sw[0]) + fv(sw[1]);
}
__device__ __forceinline__ unsigned lateness(rsrc_t rs, int lane, u64 t_start) {
    const v2u t = __builtin_amdgcn_raw_buffer_load_b64(rs, WS_T0 + (lane & 7) * 128, 0, 16);
    const u64 d = t_start - (((u64)t[1] << 32) | t[0]);
    float l = d < 150ull ? (float)(unsigned)d : 0.f;
    l = fmaxf(l, dpp<XOR1>(l)); l = fmaxf(l, dpp<XOR2>(l)); l = fmaxf(l, dpp<XOR7>(l));
    return __builtin_amdgcn_readfirstlane((unsigned)l);
}
__device__ __forceinline__ void observe_ap(rsrc_t rs, int lane, int cg, unsigned tag) {
    v2u o[1] = {v2u{0u, lane < 32 ? ~tag : tag}};
    gather<1, 1>(rs, lane * 128, WS_AP + cg * 4096, 0, tag, o);
}
#define STAMP(i) do { } while (0)

#define LD_REPORT \
    "s_getreg_b32 s92, hwreg(HW_REG_IB_STS)\n\t" \
    "s_and_b32 s93, s92, 15\n\t" \
    "s_bfe_u32 s92, s92, 0x20016\n\t" \
    "s_lshl4_add_u32 s93, s92, s93\n\t" \
    "s_sub_u32 s92, %[iss], s93\n\t" \
    "v_mov_b32 %[vt], s92\n\t"
#define LD_WRITE \
    "s_mov_b64 exec, 1\n\t" \
    "ds_write_b32 %[va], %[vt]\n\t" \
    "s_mov_b64 exec, -1\n\t"
struct Ld { unsigned issued, tnext, pace, va, sh, w0; };
template <bool FLIP, int POL = 0> __device__ __forceinline__ void ld_stream(Ld& s, rsrc_t rs, unsigned (&o)[6], unsigned soff, unsigned sstep, unsigned dst, int ng, unsigned wrap = ~0u) {
    unsigned vt;
    const unsigned end = __builtin_amdgcn_readfirstlane(s.issued + 6 * ng);
    soff = __builtin_amdgcn_readfirstlane(soff); dst = __builtin_amdgcn_readfirstlane(dst);
    s.issued = __builtin_amdgcn_readfirstlane(s.issued); s.tnext = __builtin_amdgcn_readfirstlane(s.tnext); s.pace = __builtin_amdgcn_readfirstlane(s.pace);
    asm volatile(
        "s_memrealtime s[90:91]\n\t"
        "1:\n\t"
        LD_REPORT
        "s_waitcnt lgkmcnt(0)\n\t"
        "s_lshl_b32 s92, s90, 2\n\t"
        "s_memrealtime s[90:91]\n\t"
        LD_WRITE
        "s_sub_u32 s92, s92, %[tn]\n\t"
        "s_cmp_lt_i32 s92, 0\n\t"
        "s_cbranch_scc1 1b\n\t"
        "s_lshr_b32 s92, s92, %[sh]\n\t"
        "s_add_u32 s92, s92, %[w0]\n\t"
        "s_min_u32 s92, s92, %[wm]\n\t"
        "s_cmp_gt_u32 s93, s92\n\t"
        "s_cbranch_scc1 1b\n\t"
        "s_mov_b32 m0, %[dst]\n\t"
        "s_add_u32 %[tn], %[tn], %[pace]\n\t"
        ".if %c[pol]\n\tbuffer_load_dwordx4 %[o0], %[rs], %[so] offen lds\n\t.else\n\tbuffer_load_dwordx4 %[o0], %[rs], %[so] offen nt lds\n\t.endif\n\t"
        ".if %c[pol]\n\tbuffer_load_dwordx4 %[o1], %[rs], %[so] offen offset:1024 lds\n\t.else\n\tbuffer_load_dwordx4 %[o1], %[rs], %[so] offen offset:1024 nt lds\n\t.endif\n\t"
        ".if %c[pol]\n\tbuffer_load_dwordx4 %[o2], %[rs], %[so] offen offset:2048 lds\n\t.else\n\tbuffer_load_dwordx4 %[o2], %[rs], %[so] offen offset:2048 nt lds\n\t.endif\n\t"
        "s_add_u32 m0, %[dst], 3072\n\t"
        "s_add_u32 %[iss], %[iss], 6\n\t"
        ".if %c[pol]\n\tbuffer_load_dwordx4 %[o3], %[rs], %[so] offen lds\n\t.else\n\tbuffer_load_dwordx4 %[o3], %[rs], %[so] offen nt lds\n\t.endif\n\t"
        ".if %c[pol]\n\tbuffer_load_dwordx4 %[o4], %[rs], %[so] offen offset:1024 lds\n\t.else\n\tbuffer_load_dwordx4 %[o4], %[rs], %[so] offen offset:1024 nt lds\n\t.endif\n\t"
        ".if %c[pol]\n\tbuffer_load_dwordx4 %[o5], %[rs], %[so] offen offset:2048 lds\n\t.else\n\tbuffer_load_dwordx4 %[o5], %[rs], %[so] offen offset:2048 nt lds\n\t.endif\n\t"
        "s_add_u32 %[dst], %[dst], 6144\n\t"
        "s_add_u32 %[so], %[so], %[ss]\n\t"
        "s_cmp_eq_u32 %[so], %[wrap]\n\t"
        "s_cselect_b32 %[so], 0, %[so]\n\t"
        ".if %c[flip]\n\t"
        "v_xor_b32 %[o0], 0x80, %[o0]\n\tv_xor_b32 %[o1], 0x80, %[o1]\n\tv_xor_b32 %[o2], 0x80, %[o2]\n\t"
        "v_xor_b32 %[o3], 0x80, %[o3]\n\tv_xor_b32 %[o4], 0x80, %[o4]\n\tv_xor_b32 %[o5], 0x80, %[o5]\n\t"
        ".endif\n\t"
        "s_cmp_lg_u32 %[iss], %[end]\n\t"
        "s_cbranch_scc1 1b\n\t"
        "s_waitcnt lgkmcnt(0)"
        : [iss] "+&s"(s.issued), [tn] "+&s"(s.tnext), [dst] "+&s"(dst), [so] "+&s"(soff), [vt] "=&v"(vt),
          [o0] "+&v"(o[0]), [o1] "+&v"(o[1]), [o2] "+&v"(o[2]), [o3] "+&v"(o[3]), [o4] "+&v"(o[4]), [o5] "+&v"(o[5])
        : [rs] "s"(rs), [ss] "s"(sstep), [end] "s"(end), [pace] "s"(s.pace), [va] "v"(s.va), [wrap] "s"(wrap), [wm] "n"(WMAX - 6), [sh] "s"(s.sh), [w0] "s"(s.w0), [flip] "n"(FLIP ? 1 : 0), [pol] "n"(POL)
        : "s90", "s91", "s92", "s93", "scc", "memory");
    s.issued = end;
    s.tnext = __builtin_amdgcn_readfirstlane(s.tnext);
}
__device__ __forceinline__ void ld_gate(Ld& s, int c, unsigned n) {
    unsigned vt, vg, cap = SPIN_LDS;
    const unsigned ga = L_CNT + 4 * c;
    asm volatile(
        "1:\n\t"
        "ds_read_b32 %[vg], %[ga]\n\t"
        LD_REPORT
        LD_WRITE
        "s_sub_u32 %[cap], %[cap], 1\n\t"
        "s_cbranch_scc1 2f\n\t"
        "s_waitcnt lgkmcnt(1)\n\t"
        "v_readfirstlane_b32 s92, %[vg]\n\t"
        "s_cmp_lt_u32 s92, %[n]\n\t"
        "s_cbranch_scc1 1b\n\t"
        "2:\n\t"
        "s_waitcnt lgkmcnt(0)"
        : [vt] "=&v"(vt), [vg] "=&v"(vg), [cap] "+&s"(cap)
        : [iss] "s"(s.issued), [va] "v"(s.va), [ga] "v"(ga), [n] "s"(n)
        : "s92", "s93", "scc", "memory");
}
__device__ __forceinline__ void ld_drain(Ld& s) {
    unsigned vt;
    asm volatile(
        "1:\n\t"
        LD_REPORT
        LD_WRITE
        "s_cmp_lg_u32 s93, 0\n\t"
        "s_cbranch_scc1 1b\n\t"
        : [vt] "=&v"(vt) : [iss] "s"(s.issued), [va] "v"(s.va) : "s92", "s93", "scc", "memory");
}

__global__ void __launch_bounds__(640) k_block(const u16* __restrict__ x, const u16* __restrict__ kc, const u16* __restrict__ vc,
                                               const u16* __restrict__ wq, const u16* __restrict__ wo, const u16* __restrict__ wg,
                                               const u16* __restrict__ wu, const u16* __restrict__ wd, u16* __restrict__ out,
                                               unsigned char* ws KNARG) {
    extern __shared__ __attribute__((aligned(16))) unsigned char lds_raw[];
    LAS unsigned char* L = (LAS unsigned char*)lds_raw;
    if (threadIdx.x < 20) ((LAS unsigned*)(L + L_CNT))[threadIdx.x] = 0u;
    asm volatile("s_waitcnt lgkmcnt(0)\n\ts_barrier" :: "s"(x), "s"(kc), "s"(vc), "s"(wq), "s"(wo), "s"(wg), "s"(wu), "s"(wd), "s"(out), "s"(ws) : "memory");
    const u64 t_start = __builtin_amdgcn_s_memrealtime();
    const int b = blockIdx.x, xcd = b & 7, idx = b >> 3;
    const int h = xcd * 4 + (idx >> 3), j = idx & 7;
    const int tid = threadIdx.x, lane = tid & 63, wave = __builtin_amdgcn_readfirstlane(tid >> 6);
    const int g = lane >> 4, tq = (lane & 15) >> 2, tp = lane & 3, i16 = lane & 15;
    const bool qcu = j >= 2;
    const int qr = (j + 3) >> 3, qc = j - 2 - 3 * qr;
    const int gi = (idx >> 3) * 2 + j;
    const int gcu = (!qcu && gi < 6) ? xcd * 6 + gi : -1;
    const bool pre = qcu || gcu >= 0;
    const rsrc_t rws = mkrs(ws, WS_END);
    if (__builtin_expect(wave == 7 && idx == 0, 1)) __builtin_amdgcn_raw_buffer_store_b64(v2u{(unsigned)t_start, (unsigned)(t_start >> 32)}, rws, lane == 0 ? WS_T0 + xcd * 128 : OOB, 0, 16);
    unsigned tag, xp[5];
    asm volatile("" : "=v"(xp[0]), "=v"(xp[1]), "=v"(xp[2]), "=v"(xp[3]), "=v"(xp[4]), "=v"(tag));
    if (__builtin_expect(wave == 9, 1)) {
        const unsigned xo = lane * 4, zo = 0;
        asm volatile("global_load_dword %0, %6, %7\n\tglobal_load_dword %1, %6, %7 offset:256\n\tglobal_load_dword %2, %6, %7 offset:512\n\t"
                     "global_load_dword %3, %6, %7 offset:768\n\tglobal_load_dword %4, %6, %7 offset:1024\n\tglobal_load_dword %5, %8, %9 sc1"
                     : "+v"(xp[0]), "+v"(xp[1]), "+v"(xp[2]), "+v"(xp[3]), "+v"(xp[4]), "+v"(tag) : "v"(xo), "s"(x), "v"(zo), "s"(ws) : "memory");
    }
#define CODEPF 19400
    if (__builtin_expect(wave == 8, 1)) {
        const rsrc_t rc = mkrs((const void*)(__builtin_amdgcn_s_getpc() & ~255ull), CODEPF);
        asm volatile("s_mov_b32 m0, %0\n\ts_nop 0\n\tbuffer_load_dwordx4 %1, %2, %3 offen lds" :: "s"(L_JUNK), "v"(lane * 16), "s"(rc), "s"(idx * 1024) : "memory");
    }
    if (__builtin_expect(wave == 4 && pre, 1)) {
        const rsrc_t rq = qcu ? mkrs(wq + (size_t)(320 * qr) * (H * DH) + h * DH + 64 * qc, 319 * 12288 + 128) : mkrs(wd + (size_t)(32 * gcu) * D, 40960);
        const unsigned d0 = g * 256 + (i16 ^ (g << 1)) * 16;
        const unsigned q0 = qcu ? (lane >> 3) * 12288 + ((lane & 7) ^ (g & 1) * 2) * 16 : d0, q1 = qcu ? (q0 ^ 64) + 98304 - 1024 : d0;
        const unsigned q2 = qcu ? q0 + 2 * (98304 - 1024) : d0 ^ 128, q3 = qcu ? q1 + 2 * (98304 - 1024) : d0 ^ 128;
        const unsigned qs = qcu ? 393216 : 4096;
#pragma unroll 1
        for (int n = 0; n < 10; ++n, nap(KN(20)))
            asm volatile("s_mov_b32 m0, %0\n\ts_nop 0\n\tbuffer_load_dwordx4 %1, %5, %6 offen nt lds\n\tbuffer_load_dwordx4 %2, %5, %6 offen offset:1024 nt lds\n\t"
                         "buffer_load_dwordx4 %3, %5, %6 offen offset:2048 nt lds\n\tbuffer_load_dwordx4 %4, %5, %6 offen offset:3072 nt lds"
                         :: "s"(L_W + n * 4096), "v"(q0), "v"(q1), "v"(q2), "v"(q3), "s"(rq), "s"(n * qs) : "memory");
        waitvm<0>();
        lds_signal(C_WQ);
    }
    const bool wocu = j < 5;
    const int oi = (idx >> 3) * 3 + (j - 2), oc = (idx >> 3) * 3 + (j - 5);
    const int rg = (!wocu && oc < 10) ? oc : -1;
    const int ocu = (wocu && qcu && oi < 5) ? xcd * 5 + oi : -1;
    const int n1 = __builtin_amdgcn_readfirstlane(!pre ? N1G + 4 : wocu ? N1G : 0);
    const int pv0 = P_K + 6 * n1;
    const f32x4 z4 = {0.f, 0.f, 0.f, 0.f};

    if (__builtin_expect(wave == 0, 1)) {
        __builtin_amdgcn_s_setprio(3);
        STAMP(14);
        unsigned ko[6], vo[6], po[6];
#pragma unroll
        for (int c = 0; c < 3; ++c) {
            const unsigned P = 64 * c + lane, r = P / 24, ch = P - r * 24;
            ko[c] = r * 384 + (ch ^ (r >> 1)) * 16 - c * 1024; ko[c + 3] = (ko[c] ^ 64) + 3072;
        }
        const size_t kvoff = (KN7 & 32) ? ((size_t)idx * SK + (SK - W) + xcd * 256) * DH : ((size_t)h * SK + (SK - W) + j * 256) * DH;
        Ld s;
        s.issued = 0; s.va = L_CNT + 4 * C_PROG; s.w0 = KN(22); s.sh = KN(23);
        nap(KN(21));
        MEMBAR();
        const unsigned late = (KN7 & 64) ? 0u : lateness(rws, lane, t_start);
        lds_set(C_LATE, late);
        s.pace = ((unsigned)(TE - (pre ? TAQ : TA0) - late) * (!pre ? 262144 / (35 + N1G) : wocu ? 262144 / (31 + N1G) : 262144 / 31)) >> 16;
        s.tnext = ((unsigned)t_start + (pre ? TAQ : TA0)) * 4;
        const unsigned rot = 0;
        ld_stream<false, POLK>(s, mkrs(kc + kvoff, 98304), ko, rot, 6144, L_V, 16, 98304);
        STAMP(4);
#pragma unroll
        for (int c = 0; c < 3; ++c) {
            const unsigned P = 64 * c + lane, r = P / 24, ch = P - r * 24;
            vo[c] = r * 384 + (ch ^ (r & 2u)) * 16 - c * 1024; vo[c + 3] = (vo[c] ^ 64) + 3072;
            const unsigned wb = (i16 ^ (g << 1)) * 16;
            po[c] = wocu ? (4 * c + g) * 1280 + (wb ^ ((c >> 1) << 7)) - c * 1024 : vo[c] + r * (3072 - 384);
            po[c + 3] = wocu ? (4 * c + 12 + g) * 1280 + (wb ^ ((((c + 3) >> 1) & 1) << 7)) - c * 1024 : vo[c + 3] + (r + 8) * (3072 - 384);
        }
        const rsrc_t rv = mkrs(vc + kvoff, 98304);
        const rsrc_t ro = mkrs(wo + (size_t)(h * DH) * D + j * 128, 191 * 1280 + 256);
        ld_gate(s, C_QP, __builtin_amdgcn_readfirstlane(qcu ? 4 : pre ? 8 : 0));
        if (n1) ld_stream<true, POLW>(s, ro, po, 0, 30720, L_W, n1);
        ld_gate(s, C_KA, 4);
        ld_stream<false, POLV>(s, rv, vo, rot, 6144, L_V, 8, 98304);
        ld_gate(s, C_KB, 4);
        ld_stream<false, POLV>(s, rv, vo, rot >= 49152 ? rot - 49152 : rot + 49152, 6144, L_V + 49152, 8, 98304);
        STAMP(6);
        s.pace = KN(15);
        if (wocu) {
            if (n1 < 8) ld_stream<true, POLW>(s, ro, po, n1 * 30720, 30720, L_W + n1 * 6144, 8 - n1);
        } else if (rg >= 0) {
            const rsrc_t r0 = mkrs(wg + (size_t)(64 * rg) * F + 192 * xcd, 63 * 3072 + 384), r1 = mkrs(wu + (size_t)(64 * rg) * F + 192 * xcd, 63 * 3072 + 384);
            s.tnext += FDELAY * 4; s.pace = PACE2;
            ld_stream<false, POLW>(s, r0, po, 0, 49152, L_W, 4);
            ld_stream<false, POLW>(s, r1, po, 0, 49152, L_W + 24576, 4);
        }
        ld_drain(s);
        STAMP(9);
        tag = __builtin_amdgcn_readfirstlane(*(LAS unsigned*)(L + L_TAG));
        if (gcu >= 0) {
            STAMP(17);
            unsigned ha = L_HH + (lane & 31) * 2;
            PIN(ha);
            float cs;
            lds_wait_rd<true>(L, C_CS, 1, [&] { cs = *(volatile LAS float*)(L + L_LN + lane * 4); });
            PIN(cs);
            lds_wait<false>(L, C_GO, 1);
            STAMP(18);
            delay(D3);
            float ga0, ga1;
            sum_pairs<5, PUMP>(rws, lane * 16, WS_GP + gcu * 5120, tag, ga0, ga1);
            f32x4 st = *(volatile LAS f32x4*)(L + L_ST);
            STAMP(12);
            auto sw0 = __builtin_amdgcn_permlane32_swap(__float_as_uint(ga0), __float_as_uint(ga1), false, false);
            float sv = fv(sw0[0]) + fv(sw0[1]);
            PIN(sv);
#pragma unroll 1
            for (unsigned sp = 0; sp < SPIN_LDS && __builtin_amdgcn_readfirstlane(__float_as_uint(st[2])) == 0u; ++sp) st = *(volatile LAS f32x4*)(L + L_ST);
            const float v = rbf((sv - st[0] * cs) * st[1]);
            auto sw = __builtin_amdgcn_permlane32_swap(__float_as_uint(v), __float_as_uint(v), false, false);
            const float sg = rbf(__builtin_amdgcn_rcpf(1.0f + __expf(-fv(sw[0]))));
            *(LAS u16*)(L + ha) = (u16)bfbits(sg * fv(sw[1]));
            lds_signal(C_HH);
        } else if (ocu >= 0) {
            const rsrc_t rout = mkrs(out, 1280);
            unsigned oo = lane < 8 ? (8 * ocu + lane) * 4 : OOB;
            PIN(oo);
            lds_wait<true>(L, C_O, 4);
            v2u hres[1] = {v2u{0u, ~tag}};
            delay(DOBS);
            observe_ap(rws, lane, ocu, tag);
            const u64 t4 = __builtin_amdgcn_s_memrealtime() + D3 + D4;
            delay(D3);
            gather<1, 4>(rws, (lane & 7) * 8, WS_HB + xcd * 4096 + 64 * ocu, 0, tag, hres);
            until(t4);
            STAMP(22);
            const unsigned ov = reduce_cols<6>(rws, lane * 16, WS_DP + ocu * 6144, tag, hres[0][0]);
            __builtin_amdgcn_raw_buffer_store_b32(ov, rout, oo, 0, 0);
            if (ocu == 0 && lane == 0) __hip_atomic_store((gu32*)(ws + WS_EPOCH), tag, RLX_AGENT);
            STAMP(16);
        }
    } else if (wave == 9) {
        asm volatile("s_waitcnt vmcnt(0)" : "+v"(xp[0]), "+v"(xp[1]), "+v"(xp[2]), "+v"(xp[3]), "+v"(xp[4]), "+v"(tag) :: "memory");
        float mb, rden;
        ln_stats(xp, mb, rden);
#pragma unroll
        for (int i = 0; i < 5; ++i) *(LAS unsigned*)(L + L_LN + (lane + 64 * i) * 4) = ln_pair(xp[i], mb, rden);
        tag = __builtin_amdgcn_readfirstlane(tag) + 1u;
        if (lane == 0) *(LAS unsigned*)(L + L_TAG) = tag;
        lds_signal(C_X);
        STAMP(23);
        {
            until(t_start + TQ);
            v4u qp[3];
#pragma unroll
            for (int k = 0; k < 3; ++k) qp[k] = v4u{0u, ~tag, 0u, ~tag};
            if (!(KN7 & 1)) gather<1, 8>(rws, lane * 16, WS_QP + h * 3072, 1024, tag, qp);
#pragma unroll
            for (int k = 0; k < 3; ++k) *(LAS unsigned*)(L + L_Q + k * 128 + (lane & 31) * 4) = pk2(sw32_sum(fv(qp[k][0])), sw32_sum(fv(qp[k][2])));
            lds_signal(C_Q);
            STAMP(3);
        }
        lds_wait<true>(L, C_P, 4);
        const f32x4 mx = *(LAS f32x4*)(L + L_MX), ls = *(LAS f32x4*)(L + L_MX + 16);
        const float m = fmaxf(fmaxf(mx[0], mx[1]), fmaxf(mx[2], mx[3]));
        const float l = (ls[0] + ls[1]) + (ls[2] + ls[3]);
        if (!(KN7 & 8)) put2(rws, lane == 0 ? WS_ST + (h * 8 + j) * 128 : OOB, tag, m, l);
        if (wocu) {
            until(t_start - __builtin_amdgcn_readfirstlane(*(volatile LAS unsigned*)(L + L_CNT + 4 * C_LATE)) + TS);
            const unsigned tt = lane < 8 ? ~tag : tag;
            v4u st[1] = {v4u{0u, tt, 0u, tt}};
            if (!(KN7 & 4)) gather<1, 8>(rws, lane * 128, WS_ST + h * 1024, 0, tag, st);
            const float ms = fv(st[0][0]);
            float M = fmaxf(ms, dpp<XOR1>(ms)); M = fmaxf(M, dpp<XOR2>(M)); M = fmaxf(M, dpp<XOR7>(M));
            const float e = __expf(ms - M);
            float Ls = e * fv(st[0][2]);
            Ls += dpp<XOR1>(Ls); Ls += dpp<XOR2>(Ls); Ls += dpp<XOR7>(Ls);
            if (lane < 8) *(LAS float*)(L + L_WGT + lane * 4) = e * __builtin_amdgcn_rcpf(Ls);
            lds_signal(C_W);
            STAMP(19);
            if (gcu >= 0) {
                lds_wait<true>(L, C_O, 4);
                delay(DOBS);
                observe_ap(rws, lane, gi * 6, tag);
                lds_signal(C_GO);
                delay(D3 - 22);
                v2u hv[5];
#pragma unroll
                for (int i = 0; i < 5; ++i) hv[i] = v2u{0u, ~tag};
                gather<1, 0>(rws, lane * 8, WS_HB + xcd * 4096, 512, tag, hv);
#pragma unroll
                for (int i = 0; i < 5; ++i) xp[i] = hv[i][0];
                ln_stats(xp, mb, rden);
                if (lane == 0) *(LAS f32x4*)(L + L_ST) = f32x4{mb, rden, 1.0f, 0.f};
                STAMP(15);
            }
        }
    } else {
        bf16x8 dw[5];
        if (gcu >= 0) {
            lds_wait<true>(L, C_WQ, 1);
#pragma unroll
            for (int t = 0; t < 5; ++t) {
                const int tt = 5 * (wave - 1) + t;
                unsigned ad[2];
#pragma unroll
                for (int u = 0; u < 2; ++u) {
                    const int r = 8 * g + 4 * u + tq, B = 5 * r + (tt >> 3);
                    ad[u] = L_W + r * 1280 + (tt ^ ((B & 3) | ((B >> 1) & 4))) * 32 + tp * 8;
                }
                dw[t] = cat8(trd(L, ad[0]), trd(L, ad[1]));
            }
            lds_signal(C_QP);
        }
        if (wave < 5) {
            const int a = wave - 1;
            unsigned xres = 0;
            if (rg >= 0) xres = ((const unsigned*)x)[32 * rg + 8 * a + (lane & 7)];
            lds_wait<true>(L, C_X, 1);
            tag = __builtin_amdgcn_readfirstlane(*(LAS unsigned*)(L + L_TAG));
            if (qcu) {
                bf16x8 xa[10];
#pragma unroll
                for (int s = 0; s < 10; ++s) xa[s] = *(LAS bf16x8*)(L + L_LN + (320 * qr + 32 * s + 8 * g) * 2);
                lds_wait<true>(L, C_WQ, 1);
                if (a == 0) STAMP(1);
                const unsigned ca = L_W + (8 * g + tq) * 128 + ((a ^ ((tq >> 1) | ((g & 1) << 1))) * 2 + (tp >> 1)) * 16 + (tp & 1) * 8;
                f32x4 q0 = z4, q1 = z4;
#pragma unroll
                for (int s = 0; s < 10; s += 2) {
                    q0 = mfma32(xa[s], cat8(trd(L, ca + s * 4096), trd(L, ca + s * 4096 + 512)), q0);
                    q1 = mfma32(xa[s + 1], cat8(trd(L, ca + s * 4096 + 4096), trd(L, ca + s * 4096 + 4608)), q1);
                }
                lds_signal(C_QP);
                if (!(KN7 & 2)) put1(rws, lane < 16 ? WS_QP + h * 3072 + qc * 1024 + qr * 512 + (16 * a + lane) * 8 : OOB, tag, __float_as_uint(q0[0] + q1[0]));
                if (a == 0) STAMP(2);
            }
            if (wocu) {
                unsigned oa = L_O + (lane < 48 ? 48 * a + lane : 192 + lane) * 2;
                PIN(oa);
                lds_wait<true>(L, C_W, 1);
                f32x4 w0 = *(LAS f32x4*)(L + L_WGT), w1 = *(LAS f32x4*)(L + L_WGT + 16);
                PIN(w0); PIN(w1);
                lds_wait<true>(L, C_PROG, pv0 + 84);
                unsigned own;
                lds_wait_rd<false>(L, C_OWN, 4, [&] { own = *(volatile LAS unsigned*)(L + L_OWN + (64 * a + lane) * 4); });
                const float o = combine_o<PUMP>(rws, lane * 8, WS_OP + (h * 8 * 192 + 48 * a) * 8, tag, j, own, w0, w1, D1);
                *(LAS u16*)(L + oa) = (u16)bfbits(o);
                lds_signal(C_O);
                if (a == 0) STAMP(8);
                if (gcu >= 0 && a == 0) {
                    v4u p[5];
#pragma unroll
                    for (int k = 0; k < 5; ++k) p[k] = v4u{0u, ~tag, 0u, ~tag};
                    delay(DOBS);
                    gather<1, 8>(rws, lane * 16, WS_CP + gcu * 5120, 1024, tag, p);
                    *(LAS float*)(L + L_LN + lane * 4) = fold5(p);
                    lds_signal(C_CS);
                }
            } else if (rg >= 0) {
                unsigned ha = L_O + (8 * a + (lane & 7)) * 4, ho = lane < 8 ? WS_HB + xcd * 4096 + (32 * rg + 8 * a + lane) * 8 : OOB;
                PIN(ha); PIN(ho);
                lds_wait<true>(L, C_OWN, 4);
                delay(D2);
                lds_wait<true>(L, C_PROG, P_WO);
                const unsigned hp = reduce_cols<4>(rws, lane * 16, WS_AP + (4 * rg + a) * 4096, tag, xres);
                put1(rws, ho, tag, hp);
                *(LAS unsigned*)(L + ha) = hp;
                lds_signal(C_O);
                if (a == 0) STAMP(11);
            }
        } else {
            const int kw = wave - 5;
            bf16x8 kf[4][6];
            {
                const unsigned kb = L_V + (16 * kw + i16) * 384;
#pragma unroll
                for (int n = 0; n < 4; ++n) {
                    lds_wait<true>(L, C_PROG, 12 * ((kw >> 1) + 2 * n + 1));
#pragma unroll
                    for (int s = 0; s < 6; ++s) kf[n][s] = *(LAS bf16x8*)(L + kb + n * 24576 + ((4 * s + g) ^ (i16 >> 1)) * 16);
                    if (n & 1) lds_signal(n == 1 ? C_KA : C_KB);
                }
            }
            lds_wait<true>(L, C_Q, 1);
            tag = __builtin_amdgcn_readfirstlane(*(LAS unsigned*)(L + L_TAG));
            f32x4 sa[4] = {z4, z4, z4, z4};
#pragma unroll
            for (int s = 0; s < 6; ++s) {
                const bf16x8 qf = *(LAS bf16x8*)(L + L_Q + (32 * s + 8 * g) * 2);
#pragma unroll
                for (int n = 0; n < 4; ++n) sa[n] = mfma32(qf, kf[n][s], sa[n]);
            }
            const float raw = g == 0 ? sa[0][0] : g == 1 ? sa[1][0] : g == 2 ? sa[2][0] : sa[3][0];
            const float s = rbf(rbf(raw) * 0.07216878364870322f);
            const float mw = wave_max(s);
            if (lane == 0) *(LAS float*)(L + L_MX + kw * 4) = mw;
            lds_signal(C_MAX);
            lds_wait<false>(L, C_MAX, 4);
            const f32x4 mx = *(LAS f32x4*)(L + L_MX);
            const float m = fmaxf(fmaxf(mx[0], mx[1]), fmaxf(mx[2], mx[3]));
            const float e = __expf(s - m);
            *(LAS u16*)(L + L_P + (16 * (kw + 4 * g) + i16) * 2) = (u16)bfbits(e);
            const float lw = wave_sum(e);
            if (lane == 0) *(LAS float*)(L + L_MX + 16 + kw * 4) = lw;
            lds_signal(C_P);
            lds_wait<false>(L, C_P, 4);
            if (kw == 0) STAMP(5);
            bf16x8 pf[8];
#pragma unroll
            for (int s8 = 0; s8 < 8; ++s8) pf[s8] = *(LAS bf16x8*)(L + L_P + (32 * s8 + 8 * g) * 2);
            const int fvs = (tq >> 1) | ((g & 1) << 1);
            unsigned vb[3], vl[3];
#pragma unroll
            for (int t = 0; t < 3; ++t) { vb[t] = (8 * g + tq) * 384 + (((3 * kw + t) ^ fvs) * 2 + (tp >> 1)) * 16 + (tp & 1) * 8; vl[t] = L_V + 6 * 12288 + vb[t]; PIN(vl[t]); }
            unsigned oo = lane < 48 ? WS_OP + ((h * 8 + j) * 192 + 48 * kw + lane) * 8 : OOB, ow = L_OWN + (64 * kw + lane) * 4;
            PIN(oo); PIN(ow);
            f32x4 oa[3] = {z4, z4, z4};
            lds_wait<true>(L, C_PROG, pv0 + 72);
#pragma unroll
            for (int s8 = 0; s8 < 6; ++s8)
#pragma unroll
                for (int t = 0; t < 3; ++t) oa[t] = mfma32(pf[s8], cat8(trd(L, L_V + vb[t] + s8 * 12288), trd(L, L_V + vb[t] + s8 * 12288 + 1536)), oa[t]);
#pragma unroll
            for (int s8 = 6; s8 < 8; ++s8) {
                bf16x8 vf[3];
                lds_wait_rd<false>(L, C_PROG, pv0 + 12 * s8 + 12, [&] {
#pragma unroll
                    for (int t = 0; t < 3; ++t) vf[t] = cat8(trd(L, vl[t] + (s8 - 6) * 12288), trd(L, vl[t] + (s8 - 6) * 12288 + 1536));
                });
                if (s8 == 7 && kw == 0) STAMP(20);
#pragma unroll
                for (int t = 0; t < 3; ++t) oa[t] = mfma32(pf[s8], vf[t], oa[t]);
            }
            const float op = g == 0 ? oa[0][0] : g == 1 ? oa[1][0] : oa[2][0];
            put1(rws, oo, tag, __float_as_uint(op));
            *(LAS float*)(L + ow) = op;
            lds_signal(C_OWN);
            if (kw == 0) STAMP(7);
            if (wocu) {
                unsigned ao = lane < 32 ? WS_AP + (((8 * j + 2 * kw + g) * 32 + h) * 16 + i16) * 8 : OOB, al = L_O + 16 * g;
                PIN(ao); PIN(al);
                lds_wait<false>(L, C_PROG, P_WO);
                bf16x8 bw[2][6], af[6];
                const int f3 = tq | ((g & 1) << 2);
#pragma unroll
                for (int t = 0; t < 2; ++t) {
                    const unsigned cb = L_W + (8 * g + tq) * 256 + (((2 * kw + t) ^ f3) * 2 + (tp >> 1)) * 16 + (tp & 1) * 8;
#pragma unroll
                    for (int s6 = 0; s6 < 6; ++s6) bw[t][s6] = cat8(trd(L, cb + s6 * 8192), trd(L, cb + s6 * 8192 + 1024));
                }
                lds_wait_rd<false>(L, C_O, 4, [&] {
#pragma unroll
                    for (int s6 = 0; s6 < 6; ++s6) af[s6] = *(volatile LAS bf16x8*)(L + al + 64 * s6);
                });
                f32x4 a0 = z4, a1 = z4;
#pragma unroll
                for (int s6 = 0; s6 < 6; ++s6) { a0 = mfma32(af[s6], bw[0][s6], a0); a1 = mfma32(af[s6], bw[1][s6], a1); }
                put1(rws, ao, tag, __float_as_uint(g ? a1[0] : a0[0]));
                if (kw == 0) STAMP(10);
            } else if (rg >= 0) {
                lds_wait<true>(L, C_PROG, P_WO);
                bf16x8 bw[2][3][2];
#pragma unroll
                for (int mt = 0; mt < 2; ++mt)
#pragma unroll
                    for (int t = 0; t < 3; ++t)
#pragma unroll
                        for (int s2 = 0; s2 < 2; ++s2) bw[mt][t][s2] = cat8(trd(L, L_W + mt * 24576 + vb[t] + s2 * 12288), trd(L, L_W + mt * 24576 + vb[t] + s2 * 12288 + 1536));
                const int f = 48 * kw + lane;
                unsigned go = lane < 48 ? WS_CP + ((6 * xcd + (f >> 5)) * 10 + rg) * 512 + (f & 31) * 16 : OOB, al = L_O + 16 * g;
                PIN(al);
                const v4u ones = {0x3F803F80u, 0x3F803F80u, 0x3F803F80u, 0x3F803F80u};
                bf16x8 af0 = asb(ones), af1 = asb(ones);
#pragma unroll 1
                for (int pass = 0; pass < 2; ++pass) {
                    float r[2];
#pragma unroll
                    for (int mt = 0; mt < 2; ++mt) {
                        f32x4 d[3];
#pragma unroll
                        for (int t = 0; t < 3; ++t) d[t] = mfma32(af1, bw[mt][t][1], mfma32(af0, bw[mt][t][0], z4));
                        r[mt] = g == 0 ? d[0][0] : g == 1 ? d[1][0] : d[2][0];
                    }
                    put2(rws, go, tag, r[0], r[1]);
                    if (pass) break;
                    go += WS_GP - WS_CP;
                    PIN(go);
                    lds_wait_rd<false>(L, C_O, 4, [&] { af0 = *(volatile LAS bf16x8*)(L + al); af1 = *(volatile LAS bf16x8*)(L + al + 64); });
                }
                if (kw == 0) STAMP(21);
            }
        }
        if (gcu >= 0) {
            const int w8 = wave - 1;
            unsigned d0 = WS_DP + (((5 * w8 + g) * 48 + gcu) * 16 + i16) * 8, d1 = lane < 16 ? WS_DP + (((5 * w8 + 4) * 48 + gcu) * 16 + i16) * 8 : OOB, al = L_HH + 16 * g;
            PIN(d0); PIN(d1); PIN(al);
            bf16x8 af;
            lds_wait<true>(L, C_GO, 1);
            lds_wait_rd<false>(L, C_HH, 1, [&] { af = *(volatile LAS bf16x8*)(L + al); });
            f32x4 d[5];
#pragma unroll
            for (int t = 0; t < 5; ++t) d[t] = mfma32(af, dw[(t + 4) % 5], z4);
            put1(rws, d1, tag, __float_as_uint(d[0][0]));
            put1(rws, d0, tag, __float_as_uint(g == 0 ? d[1][0] : g == 1 ? d[2][0] : g == 2 ? d[3][0] : d[4][0]));
            if (w8 == 0) STAMP(13);
        }
    }
}

extern "C" void kernel_launch(void* const* d_in, const int* in_sizes, int n_in, void* d_out, int out_size, void* d_ws, size_t ws_size, hipStream_t stream) {
    (void)hipFuncSetAttribute((const void*)k_block, hipFuncAttributeMaxDynamicSharedMemorySize, L_END);
    k_block<<<256, 640, L_END, stream>>>((const u16*)d_in[0], (const u16*)d_in[1], (const u16*)d_in[2], (const u16*)d_in[3], (const u16*)d_in[4],
                                         (const u16*)d_in[6], (const u16*)d_in[7], (const u16*)d_in[8], (u16*)d_out, (unsigned char*)d_ws
        );
}
```

```cpp
#include <hip/hip_runtime.h>
#include <stdint.h>

typedef unsigned short u16;
typedef unsigned long long u64;
typedef unsigned v4u __attribute__((ext_vector_type(4)));
typedef unsigned v2u __attribute__((ext_vector_type(2)));
typedef float f32x4 __attribute__((ext_vector_type(4)));
typedef short s16x4 __attribute__((ext_vector_type(4)));
typedef __bf16 bf16x8 __attribute__((ext_vector_type(8)));
typedef __bf16 bf16x2 __attribute__((ext_vector_type(2)));
#define GAS __attribute__((address_space(1)))
#define LAS __attribute__((address_space(3)))
typedef GAS u64 gu64;
typedef GAS unsigned gu32;
typedef __amdgpu_buffer_rsrc_t rsrc_t;
#define RLX_AGENT __ATOMIC_RELAXED, __HIP_MEMORY_SCOPE_AGENT

constexpr int D = 640, H = 32, SK = 8192, DH = 192, W = 2048, F = 1536;

constexpr unsigned WS_EPOCH = 0;
constexpr unsigned WS_T0 = 1024;
constexpr unsigned WS_QP = 4096;
constexpr unsigned WS_OP = WS_QP + 32 * 3072;
constexpr unsigned WS_ST = WS_OP + 32 * 8 * 192 * 8;
constexpr unsigned WS_AP = WS_ST + 32 * 8 * 128;
constexpr unsigned WS_HB = WS_AP + 40 * 512 * 8;
constexpr unsigned WS_CP = WS_HB + 8 * 4096;
constexpr unsigned WS_GP = WS_CP + 48 * 4096;
constexpr unsigned WS_DP = WS_GP + 48 * 4096;
constexpr unsigned WS_END = WS_DP + 40 * 6144;
constexpr unsigned OOB = 0x7FFFFF00u;

constexpr unsigned L_W = 0;
constexpr unsigned L_V = 49152;
constexpr unsigned L_Q = L_V + 98304;
constexpr unsigned L_P = L_Q + 384;
constexpr unsigned L_O = L_P + 512;
constexpr unsigned L_LN = L_O + 512;
constexpr unsigned L_OWN = L_LN + 1280;
constexpr unsigned L_HH = L_OWN + 1024;
constexpr unsigned L_MX = L_HH + 64;
constexpr unsigned L_WGT = L_MX + 32;
constexpr unsigned L_CNT = L_WGT + 32;
constexpr unsigned L_ST = L_CNT + 64;
constexpr unsigned L_TAG = L_ST + 16;
constexpr unsigned L_STAMP = L_TAG + 16;
constexpr unsigned L_JUNK = L_STAMP + 256;
constexpr unsigned L_END = L_JUNK + 1024;
static_assert(L_END <= 163840, "LDS");
enum { C_X = 0, C_PROG, C_KA, C_KB, C_Q, C_MAX, C_P, C_W, C_OWN, C_O, C_HH, C_WQ, C_QP, C_CS, C_GO, C_LATE };
constexpr int P_K = 96, P_WO = 240, P_WD = 282;

#define KTE 875
#define KNOB_DEFAULTS {2, 40, 100, KTE, 80, 400, -80, 0, 10, 180, 40, 95, 110, 0, 0, 0, 30, 30, 0, 0, 2, 4, 16, 4}
constexpr int kn_c[24] = KNOB_DEFAULTS;
#define KN(i) kn_c[i]
#define KNARG
#define N1G KN(0)
#define TA0 KN(1)
#define TAQ KN(2)
#define TE KN(3)
#define FDELAY KN(4)
#define TQ KN(5)
#define TS (TE + KN(6))
#define KN7 KN(7)
#define D1 KN(8)
#define D2 KN(9)
#define D3 KN(10)
#define D4 KN(11)
#define DOBS KN(12)
#define PACE2 KN(13)
#define RSL KN(14)
#define THR1 KN(16)
#define THR2 KN(17)
#define THR3 KN(18)
#define THR4 KN(19)
#define POLK 0
#define POLV 0
#define POLW 0
#define WMAX 36
#define DEPTH 2

constexpr unsigned SPIN_LDS = 1u << 20;
constexpr unsigned SPIN_GLB = 1u << 15;

__device__ __forceinline__ unsigned pk2(float lo, float hi) {
    const bf16x2 r = {(__bf16)lo, (__bf16)hi};
    return __builtin_bit_cast(unsigned, r);
}
__device__ __forceinline__ unsigned bfbits(float f) { return (unsigned)__builtin_bit_cast(u16, (__bf16)f); }
__device__ __forceinline__ float rbf(float f) { return __uint_as_float(bfbits(f) << 16); }
__device__ __forceinline__ float lo16(unsigned w) { return __uint_as_float(w << 16); }
__device__ __forceinline__ float hi16(unsigned w) { return __uint_as_float(w & 0xFFFF0000u); }
__device__ __forceinline__ float dot2(unsigned a, unsigned b, float c) {
    return __builtin_amdgcn_fdot2_f32_bf16(__builtin_bit_cast(bf16x2, a), __builtin_bit_cast(bf16x2, b), c, false);
}
template <int CTRL> __device__ __forceinline__ float dpp(float x) {
    return __builtin_bit_cast(float, __builtin_amdgcn_mov_dpp(__builtin_bit_cast(int, x), CTRL, 0xf, 0xf, true));
}
constexpr int XOR1 = 0xB1, XOR2 = 0x4E, XOR7 = 0x141, XOR8 = 0x128;
__device__ __forceinline__ float sw16_sum(float x) {
    auto s = __builtin_amdgcn_permlane16_swap(__float_as_uint(x), __float_as_uint(x), false, false);
    return __uint_as_float(s[0]) + __uint_as_float(s[1]);
}
__device__ __forceinline__ float sw32_sum(float x) {
    auto s = __builtin_amdgcn_permlane32_swap(__float_as_uint(x), __float_as_uint(x), false, false);
    return __uint_as_float(s[0]) + __uint_as_float(s[1]);
}
__device__ __forceinline__ float sw16_max(float x) {
    auto s = __builtin_amdgcn_permlane16_swap(__float_as_uint(x), __float_as_uint(x), false, false);
    return fmaxf(__uint_as_float(s[0]), __uint_as_float(s[1]));
}
__device__ __forceinline__ float sw32_max(float x) {
    auto s = __builtin_amdgcn_permlane32_swap(__float_as_uint(x), __float_as_uint(x), false, false);
    return fmaxf(__uint_as_float(s[0]), __uint_as_float(s[1]));
}
__device__ __forceinline__ float wave_sum(float x) {
    x += dpp<XOR1>(x); x += dpp<XOR2>(x); x += dpp<XOR7>(x); x += dpp<XOR8>(x);
    return sw32_sum(sw16_sum(x));
}
__device__ __forceinline__ float wave_max(float x) {
    x = fmaxf(x, dpp<XOR1>(x)); x = fmaxf(x, dpp<XOR2>(x)); x = fmaxf(x, dpp<XOR7>(x)); x = fmaxf(x, dpp<XOR8>(x));
    return sw32_max(sw16_max(x));
}
__device__ __forceinline__ rsrc_t mkrs(const void* p, unsigned bytes) { return __builtin_amdgcn_make_buffer_rsrc((void*)p, 0, bytes, 0x00020000); }

__device__ __forceinline__ void dma16(rsrc_t rs, unsigned voff, unsigned soff, unsigned lds_dst) {
    asm volatile("s_mov_b32 m0, %0\n\ts_nop 0\n\tbuffer_load_dwordx4 %1, %2, %3 offen nt lds"
                 :: "s"(lds_dst), "v"(voff), "s"(rs), "s"(soff) : "memory");
}
template <int N> __device__ __forceinline__ void waitvm() { asm volatile("s_waitcnt vmcnt(%0)" :: "n"(N) : "memory"); }
#define LGKM_DRAIN() asm volatile("s_waitcnt lgkmcnt(0)" ::: "memory")
#define MEMBAR() asm volatile("" ::: "memory")
#define BAR() do { __builtin_amdgcn_sched_barrier(0); asm volatile("s_barrier" ::: "memory"); __builtin_amdgcn_sched_barrier(0); } while (0)

__device__ __forceinline__ void lds_signal(int c) {
    const unsigned addr = L_CNT + 4 * c, one = 1u;
    asm volatile("s_mov_b64 exec, 1\n\tds_add_u32 %0, %1\n\ts_mov_b64 exec, -1" :: "v"(addr), "v"(one) : "memory");
}
__device__ __forceinline__ void lds_set(int c, unsigned val) {
    const unsigned addr = L_CNT + 4 * c;
    asm volatile("s_mov_b64 exec, 1\n\tds_write_b32 %0, %1\n\ts_mov_b64 exec, -1" :: "v"(addr), "v"(val) : "memory");
}
template <bool SLEEP, class F> __device__ __forceinline__ void lds_wait_rd(LAS unsigned char* L, int c, unsigned n, F rd) {
    volatile LAS unsigned* p = (volatile LAS unsigned*)(L + L_CNT) + c;
    unsigned cap = SPIN_LDS;
#pragma unroll 1
    do {
        const unsigned f = *p;
        MEMBAR();
        rd();
        MEMBAR();
        if ((unsigned)__builtin_amdgcn_readfirstlane(f) >= n) break;
        if (SLEEP) __builtin_amdgcn_s_sleep(1);
        MEMBAR();
    } while (--cap != 0);
    MEMBAR();
}
template <bool SLEEP> __device__ __forceinline__ void lds_wait(LAS unsigned char* L, int c, unsigned n) { lds_wait_rd<SLEEP>(L, c, n, [] {}); }
__device__ __forceinline__ void until(u64 t) { while (__builtin_amdgcn_s_memrealtime() < t) __builtin_amdgcn_s_sleep(1); }
__device__ __forceinline__ void delay(int ticks) { until(__builtin_amdgcn_s_memrealtime() + ticks); }
__device__ __forceinline__ void nap(int n) { for (int i = 0; i < n; ++i) __builtin_amdgcn_s_sleep(1); }
#define PIN(x) asm volatile("" : "+v"(x))
#define SB() __builtin_amdgcn_sched_barrier(0)
__device__ __forceinline__ float fv(unsigned u) { return __uint_as_float(u); }
__device__ __forceinline__ void put1(rsrc_t rs, unsigned off, unsigned tag, unsigned val) { __builtin_amdgcn_raw_buffer_store_b64(v2u{val, tag}, rs, off, 0, 16); }
__device__ __forceinline__ void put2(rsrc_t rs, unsigned off, unsigned tag, float a, float b) {
    __builtin_amdgcn_raw_buffer_store_b128(v4u{__float_as_uint(a), tag, __float_as_uint(b), tag}, rs, off, 0, 16);
}
__device__ __forceinline__ u64 badmask(v2u x, unsigned tag) {
    u64 m;
    asm volatile("v_cmp_ne_u32_e64 %0, %1, %2" : "=s"(m) : "s"(tag), "v"(x[1]));
    return m;
}
__device__ __forceinline__ u64 badmask(v4u x, unsigned tag) {
    u64 m;
    const unsigned t = __builtin_amdgcn_bitop3_b32(x[1], x[3], tag, 0x7E);
    asm volatile("v_cmp_ne_u32_e64 %0, 0, %1" : "=s"(m) : "v"(t));
    return m;
}
__device__ __forceinline__ void gload(v2u& x, u64 m, rsrc_t rs, unsigned voff, unsigned soff) {
    asm volatile("s_mov_b64 exec, %2\n\tbuffer_load_dwordx2 %0, %1, %3, %4 offen sc1\n\ts_mov_b64 exec, -1" : "+v"(x) : "v"(voff), "s"(m), "s"(rs), "s"(soff) : "memory");
}
__device__ __forceinline__ void gload(v4u& x, u64 m, rsrc_t rs, unsigned voff, unsigned soff) {
    asm volatile("s_mov_b64 exec, %2\n\tbuffer_load_dwordx4 %0, %1, %3, %4 offen sc1\n\ts_mov_b64 exec, -1" : "+v"(x) : "v"(voff), "s"(m), "s"(rs), "s"(soff) : "memory");
}
template <int DP, int SLP, int N, class T> __device__ __forceinline__ void gather(rsrc_t rs, unsigned voff, unsigned base, unsigned stride, unsigned tag, T (&x)[N]) {
    u64 m[N];
#pragma unroll
    for (int k = 0; k < N; ++k) { PIN(x[k]); m[k] = badmask(x[k], tag); }
    unsigned cap = SPIN_GLB;
#pragma unroll 1
    do {
#pragma unroll
        for (int k = 0; k < N; ++k) gload(x[k], m[k], rs, voff, base + k * stride);
        waitvm<0>();
#pragma unroll
        for (int k = 0; k < N; ++k) PIN(x[k]);
        u64 any = 0;
#pragma unroll
        for (int k = 0; k < N; ++k) { m[k] = badmask(x[k], tag); any |= m[k]; }
        if (!any) break;
        if (SLP) __builtin_amdgcn_s_sleep(SLP);
        MEMBAR();
    } while (--cap != 0);
}
template <int N, int PM> __device__ __forceinline__ void sum_pairs(rsrc_t rs, unsigned voff, unsigned base, unsigned tag, float& a0, float& a1);
template <int PM> __device__ __forceinline__ float combine_o(rsrc_t rs, unsigned voff, unsigned base, unsigned tag, int j, unsigned own, f32x4 w0, f32x4 w1, unsigned napn);
template <> __device__ __forceinline__ void sum_pairs<4, 0>(rsrc_t rs, unsigned voff, unsigned base, unsigned tag, float& a0, float& a1) {
    unsigned cap = SPIN_GLB;
    asm volatile(
        "buffer_load_dwordx4 v[144:147], %[vo], %[rs], %[b0] offen offset:0 sc1\n\t"
        "buffer_load_dwordx4 v[148:151], %[vo], %[rs], %[b0] offen offset:1024 sc1\n\t"
        "buffer_load_dwordx4 v[152:155], %[vo], %[rs], %[b0] offen offset:2048 sc1\n\t"
        "buffer_load_dwordx4 v[156:159], %[vo], %[rs], %[b0] offen offset:3072 sc1\n\t"
        "s_waitcnt vmcnt(0)\n\t"
        "v_bitop3_b32 v140, v145, v147, %[tag] bitop3:0x7e\n\t"
        "v_bitop3_b32 v141, v149, v151, %[tag] bitop3:0x7e\n\t"
        "v_bitop3_b32 v142, v153, v155, %[tag] bitop3:0x7e\n\t"
        "v_bitop3_b32 v143, v157, v159, %[tag] bitop3:0x7e\n\t"
        "v_cmp_ne_u32_e64 s[78:79], 0, v140\n\t"
        "v_cmp_ne_u32_e64 s[80:81], 0, v141\n\t"
        "v_cmp_ne_u32_e64 s[82:83], 0, v142\n\t"
        "v_cmp_ne_u32_e64 s[84:85], 0, v143\n\t"
        "s_or_b64 s[92:93], s[78:79], s[80:81]\n\t"
        "s_or_b64 s[92:93], s[92:93], s[82:83]\n\t"
        "s_or_b64 s[92:93], s[92:93], s[84:85]\n\t"
        "s_cbranch_scc0 3f\n\t"
        "1:\n\t"
        "s_mov_b64 exec, s[78:79]\n\t"
        "buffer_load_dwordx4 v[144:147], %[vo], %[rs], %[b0] offen offset:0 sc1\n\t"
        "s_mov_b64 exec, s[80:81]\n\t"
        "buffer_load_dwordx4 v[148:151], %[vo], %[rs], %[b0] offen offset:1024 sc1\n\t"
        "s_mov_b64 exec, s[82:83]\n\t"
        "buffer_load_dwordx4 v[152:155], %[vo], %[rs], %[b0] offen offset:2048 sc1\n\t"
        "s_mov_b64 exec, s[84:85]\n\t"
        "buffer_load_dwordx4 v[156:159], %[vo], %[rs], %[b0] offen offset:3072 sc1\n\t"
        "s_mov_b64 exec, -1\n\t"
        "s_sub_u32 %[cap], %[cap], 1\n\t"
        "s_cbranch_scc1 3f\n\t"
        "s_waitcnt vmcnt(0)\n\t"
        "v_bitop3_b32 v140, v145, v147, %[tag] bitop3:0x7e\n\t"
        "v_bitop3_b32 v141, v149, v151, %[tag] bitop3:0x7e\n\t"
        "v_bitop3_b32 v142, v153, v155, %[tag] bitop3:0x7e\n\t"
        "v_bitop3_b32 v143, v157, v159, %[tag] bitop3:0x7e\n\t"
        "v_cmp_ne_u32_e64 s[78:79], 0, v140\n\t"
        "v_cmp_ne_u32_e64 s[80:81], 0, v141\n\t"
        "v_cmp_ne_u32_e64 s[82:83], 0, v142\n\t"
        "v_cmp_ne_u32_e64 s[84:85], 0, v143\n\t"
        "s_or_b64 s[92:93], s[78:79], s[80:81]\n\t"
        "s_or_b64 s[92:93], s[92:93], s[82:83]\n\t"
        "s_or_b64 s[92:93], s[92:93], s[84:85]\n\t"
        "s_cbranch_scc1 1b\n\t"
        "3:\n\t"
        "s_waitcnt vmcnt(0)\n\t"
        "v_add_f32 %[a0], v144, v148\n\t"
        "v_add_f32 %[a1], v146, v150\n\t"
        "v_add_f32 v140, v152, v156\n\t"
        "v_add_f32 v141, v154, v158\n\t"
        "v_add_f32 %[a0], %[a0], v140\n\t"
        "v_add_f32 %[a1], %[a1], v141\n\t"
        "s_nop 1"
        : [a0] "=&v"(a0), [a1] "=&v"(a1), [cap] "+&s"(cap) : [vo] "v"(voff), [rs] "s"(rs), [b0] "s"(base), [b1] "s"(base + 4096), [tag] "s"(tag)
        : "v140", "v141", "v142", "v143", "v144", "v145", "v146", "v147", "v148", "v149", "v150", "v151", "v152", "v153", "v154", "v155", "v156", "v157", "v158", "v159", "s78", "s79", "s80", "s81", "s82", "s83", "s84", "s85", "s92", "s93", "scc", "memory");
}
template <> __device__ __forceinline__ void sum_pairs<4, 1>(rsrc_t rs, unsigned voff, unsigned base, unsigned tag, float& a0, float& a1) {
    unsigned cap = SPIN_GLB;
    asm volatile(
        "buffer_load_dwordx4 v[144:147], %[vo], %[rs], %[b0] offen offset:0 sc1\n\t"
        "buffer_load_dwordx4 v[148:151], %[vo], %[rs], %[b0] offen offset:1024 sc1\n\t"
        "buffer_load_dwordx4 v[152:155], %[vo], %[rs], %[b0] offen offset:2048 sc1\n\t"
        "buffer_load_dwordx4 v[156:159], %[vo], %[rs], %[b0] offen offset:3072 sc1\n\t"
        "s_waitcnt vmcnt(0)\n\t"
        "v_bitop3_b32 v140, v145, v147, %[tag] bitop3:0x7e\n\t"
        "v_bitop3_b32 v141, v149, v151, %[tag] bitop3:0x7e\n\t"
        "v_bitop3_b32 v142, v153, v155, %[tag] bitop3:0x7e\n\t"
        "v_bitop3_b32 v143, v157, v159, %[tag] bitop3:0x7e\n\t"
        "v_cmp_ne_u32_e64 s[78:79], 0, v140\n\t"
        "v_cmp_ne_u32_e64 s[80:81], 0, v141\n\t"
        "v_cmp_ne_u32_e64 s[82:83], 0, v142\n\t"
        "v_cmp_ne_u32_e64 s[84:85], 0, v143\n\t"
        "s_or_b64 s[92:93], s[78:79], s[80:81]\n\t"
        "s_or_b64 s[92:93], s[92:93], s[82:83]\n\t"
        "s_or_b64 s[92:93], s[92:93], s[84:85]\n\t"
        "s_cbranch_scc0 3f\n\t"
        "s_mov_b64 exec, s[78:79]\n\t"
        "buffer_load_dwordx4 v[144:147], %[vo], %[rs], %[b0] offen offset:0 sc1\n\t"
        "s_mov_b64 exec, s[80:81]\n\t"
        "buffer_load_dwordx4 v[148:151], %[vo], %[rs], %[b0] offen offset:1024 sc1\n\t"
        "s_mov_b64 exec, s[82:83]\n\t"
        "buffer_load_dwordx4 v[152:155], %[vo], %[rs], %[b0] offen offset:2048 sc1\n\t"
        "s_mov_b64 exec, s[84:85]\n\t"
        "buffer_load_dwordx4 v[156:159], %[vo], %[rs], %[b0] offen offset:3072 sc1\n\t"
        "s_mov_b64 exec, -1\n\t"
        "s_sleep 2\n\t"
        "1:\n\t"
        "s_mov_b64 exec, s[78:79]\n\t"
        "buffer_load_dwordx4 v[144:147], %[vo], %[rs], %[b0] offen offset:0 sc1\n\t"
        "s_mov_b64 exec, s[80:81]\n\t"
        "buffer_load_dwordx4 v[148:151], %[vo], %[rs], %[b0] offen offset:1024 sc1\n\t"
        "s_mov_b64 exec, s[82:83]\n\t"
        "buffer_load_dwordx4 v[152:155], %[vo], %[rs], %[b0] offen offset:2048 sc1\n\t"
        "s_mov_b64 exec, s[84:85]\n\t"
        "buffer_load_dwordx4 v[156:159], %[vo], %[rs], %[b0] offen offset:3072 sc1\n\t"
        "s_mov_b64 exec, -1\n\t"
        "s_sub_u32 %[cap], %[cap], 1\n\t"
        "s_cbranch_scc1 3f\n\t"
        "s_waitcnt vmcnt(4)\n\t"
        "v_bitop3_b32 v140, v145, v147, %[tag] bitop3:0x7e\n\t"
        "v_bitop3_b32 v141, v149, v151, %[tag] bitop3:0x7e\n\t"
        "v_bitop3_b32 v142, v153, v155, %[tag] bitop3:0x7e\n\t"
        "v_bitop3_b32 v143, v157, v159, %[tag] bitop3:0x7e\n\t"
        "v_cmp_ne_u32_e64 s[78:79], 0, v140\n\t"
        "v_cmp_ne_u32_e64 s[80:81], 0, v141\n\t"
        "v_cmp_ne_u32_e64 s[82:83], 0, v142\n\t"
        "v_cmp_ne_u32_e64 s[84:85], 0, v143\n\t"
        "s_or_b64 s[92:93], s[78:79], s[80:81]\n\t"
        "s_or_b64 s[92:93], s[92:93], s[82:83]\n\t"
        "s_or_b64 s[92:93], s[92:93], s[84:85]\n\t"
        "s_cbranch_scc1 1b\n\t"
        "3:\n\t"
        "s_waitcnt vmcnt(0)\n\t"
        "v_add_f32 %[a0], v144, v148\n\t"
        "v_add_f32 %[a1], v146, v150\n\t"
        "v_add_f32 v140, v152, v156\n\t"
        "v_add_f32 v141, v154, v158\n\t"
        "v_add_f32 %[a0], %[a0], v140\n\t"
        "v_add_f32 %[a1], %[a1], v141\n\t"
        "s_nop 1"
        : [a0] "=&v"(a0), [a1] "=&v"(a1), [cap] "+&s"(cap) : [vo] "v"(voff), [rs] "s"(rs), [b0] "s"(base), [b1] "s"(base + 4096), [tag] "s"(tag)
        : "v140", "v141", "v142", "v143", "v144", "v145", "v146", "v147", "v148", "v149", "v150", "v151", "v152", "v153", "v154", "v155", "v156", "v157", "v158", "v159", "s78", "s79", "s80", "s81", "s82", "s83", "s84", "s85", "s92", "s93", "scc", "memory");
}
template <> __device__ __forceinline__ void sum_pairs<5, 0>(rsrc_t rs, unsigned voff, unsigned base, unsigned tag, float& a0, float& a1) {
    unsigned cap = SPIN_GLB;
    asm volatile(
        "buffer_load_dwordx4 v[144:147], %[vo], %[rs], %[b0] offen offset:0 sc1\n\t"
        "buffer_load_dwordx4 v[148:151], %[vo], %[rs], %[b0] offen offset:1024 sc1\n\t"
        "buffer_load_dwordx4 v[152:155], %[vo], %[rs], %[b0] offen offset:2048 sc1\n\t"
        "buffer_load_dwordx4 v[156:159], %[vo], %[rs], %[b0] offen offset:3072 sc1\n\t"
        "buffer_load_dwordx4 v[160:163], %[vo], %[rs], %[b1] offen offset:0 sc1\n\t"
        "s_waitcnt vmcnt(0)\n\t"
        "v_bitop3_b32 v140, v145, v147, %[tag] bitop3:0x7e\n\t"
        "v_bitop3_b32 v141, v149, v151, %[tag] bitop3:0x7e\n\t"
        "v_bitop3_b32 v142, v153, v155, %[tag] bitop3:0x7e\n\t"
        "v_bitop3_b32 v143, v157, v159, %[tag] bitop3:0x7e\n\t"
        "v_cmp_ne_u32_e64 s[78:79], 0, v140\n\t"
        "v_cmp_ne_u32_e64 s[80:81], 0, v141\n\t"
        "v_cmp_ne_u32_e64 s[82:83], 0, v142\n\t"
        "v_cmp_ne_u32_e64 s[84:85], 0, v143\n\t"
        "v_bitop3_b32 v140, v161, v163, %[tag] bitop3:0x7e\n\t"
        "v_cmp_ne_u32_e64 s[86:87], 0, v140\n\t"
        "s_or_b64 s[92:93], s[78:79], s[80:81]\n\t"
        "s_or_b64 s[92:93], s[92:93], s[82:83]\n\t"
        "s_or_b64 s[92:93], s[92:93], s[84:85]\n\t"
        "s_or_b64 s[92:93], s[92:93], s[86:87]\n\t"
        "s_cbranch_scc0 3f\n\t"
        "1:\n\t"
        "s_mov_b64 exec, s[78:79]\n\t"
        "buffer_load_dwordx4 v[144:147], %[vo], %[rs], %[b0] offen offset:0 sc1\n\t"
        "s_mov_b64 exec, s[80:81]\n\t"
        "buffer_load_dwordx4 v[148:151], %[vo], %[rs], %[b0] offen offset:1024 sc1\n\t"
        "s_mov_b64 exec, s[82:83]\n\t"
        "buffer_load_dwordx4 v[152:155], %[vo], %[rs], %[b0] offen offset:2048 sc1\n\t"
        "s_mov_b64 exec, s[84:85]\n\t"
        "buffer_load_dwordx4 v[156:159], %[vo], %[rs], %[b0] offen offset:3072 sc1\n\t"
        "s_mov_b64 exec, s[86:87]\n\t"
        "buffer_load_dwordx4 v[160:163], %[vo], %[rs], %[b1] offen offset:0 sc1\n\t"
        "s_mov_b64 exec, -1\n\t"
        "s_sub_u32 %[cap], %[cap], 1\n\t"
        "s_cbranch_scc1 3f\n\t"
        "s_waitcnt vmcnt(0)\n\t"
        "v_bitop3_b32 v140, v145, v147, %[tag] bitop3:0x7e\n\t"
        "v_bitop3_b32 v141, v149, v151, %[tag] bitop3:0x7e\n\t"
        "v_bitop3_b32 v142, v153, v155, %[tag] bitop3:0x7e\n\t"
        "v_bitop3_b32 v143, v157, v159, %[tag] bitop3:0x7e\n\t"
        "v_cmp_ne_u32_e64 s[78:79], 0, v140\n\t"
        "v_cmp_ne_u32_e64 s[80:81], 0, v141\n\t"
        "v_cmp_ne_u32_e64 s[82:83], 0, v142\n\t"
        "v_cmp_ne_u32_e64 s[84:85], 0, v143\n\t"
        "v_bitop3_b32 v140, v161, v163, %[tag] bitop3:0x7e\n\t"
        "v_cmp_ne_u32_e64 s[86:87], 0, v140\n\t"
        "s_or_b64 s[92:93], s[78:79], s[80:81]\n\t"
        "s_or_b64 s[92:93], s[92:93], s[82:83]\n\t"
        "s_or_b64 s[92:93], s[92:93], s[84:85]\n\t"
        "s_or_b64 s[92:93], s[92:93], s[86:87]\n\t"
        "s_cbranch_scc1 1b\n\t"
        "3:\n\t"
        "s_waitcnt vmcnt(0)\n\t"
        "v_add_f32 %[a0], v144, v148\n\t"
        "v_add_f32 %[a1], v146, v150\n\t"
        "v_add_f32 v140, v152, v156\n\t"
        "v_add_f32 v141, v154, v158\n\t"
        "v_add_f32 %[a0], %[a0], v140\n\t"
        "v_add_f32 %[a1], %[a1], v141\n\t"
        "v_add_f32 %[a0], %[a0], v160\n\t"
        "v_add_f32 %[a1], %[a1], v162\n\t"
        "s_nop 1"
        : [a0] "=&v"(a0), [a1] "=&v"(a1), [cap] "+&s"(cap) : [vo] "v"(voff), [rs] "s"(rs), [b0] "s"(base), [b1] "s"(base + 4096), [tag] "s"(tag)
        : "v140", "v141", "v142", "v143", "v144", "v145", "v146", "v147", "v148", "v149", "v150", "v151", "v152", "v153", "v154", "v155", "v156", "v157", "v158", "v159", "v160", "v161", "v162", "v163", "s78", "s79", "s80", "s81", "s82", "s83", "s84", "s85", "s86", "s87", "s92", "s93", "scc", "memory");
}
template <> __device__ __forceinline__ void sum_pairs<5, 1>(rsrc_t rs, unsigned voff, unsigned base, unsigned tag, float& a0, float& a1) {
    unsigned cap = SPIN_GLB;
    asm volatile(
        "buffer_load_dwordx4 v[144:147], %[vo], %[rs], %[b0] offen offset:0 sc1\n\t"
        "buffer_load_dwordx4 v[148:151], %[vo], %[rs], %[b0] offen offset:1024 sc1\n\t"
        "buffer_load_dwordx4 v[152:155], %[vo], %[rs], %[b0] offen offset:2048 sc1\n\t"
        "buffer_load_dwordx4 v[156:159], %[vo], %[rs], %[b0] offen offset:3072 sc1\n\t"
        "buffer_load_dwordx4 v[160:163], %[vo], %[rs], %[b1] offen offset:0 sc1\n\t"
        "s_waitcnt vmcnt(0)\n\t"
        "v_bitop3_b32 v140, v145, v147, %[tag] bitop3:0x7e\n\t"
        "v_bitop3_b32 v141, v149, v151, %[tag] bitop3:0x7e\n\t"
        "v_bitop3_b32 v142, v153, v155, %[tag] bitop3:0x7e\n\t"
        "v_bitop3_b32 v143, v157, v159, %[tag] bitop3:0x7e\n\t"
        "v_cmp_ne_u32_e64 s[78:79], 0, v140\n\t"
        "v_cmp_ne_u32_e64 s[80:81], 0, v141\n\t"
        "v_cmp_ne_u32_e64 s[82:83], 0, v142\n\t"
        "v_cmp_ne_u32_e64 s[84:85], 0, v143\n\t"
        "v_bitop3_b32 v140, v161, v163, %[tag] bitop3:0x7e\n\t"
        "v_cmp_ne_u32_e64 s[86:87], 0, v140\n\t"
        "s_or_b64 s[92:93], s[78:79], s[80:81]\n\t"
        "s_or_b64 s[92:93], s[92:93], s[82:83]\n\t"
        "s_or_b64 s[92:93], s[92:93], s[84:85]\n\t"
        "s_or_b64 s[92:93], s[92:93], s[86:87]\n\t"
        "s_cbranch_scc0 3f\n\t"
        "s_mov_b64 exec, s[78:79]\n\t"
        "buffer_load_dwordx4 v[144:147], %[vo], %[rs], %[b0] offen offset:0 sc1\n\t"
        "s_mov_b64 exec, s[80:81]\n\t"
        "buffer_load_dwordx4 v[148:151], %[vo], %[rs], %[b0] offen offset:1024 sc1\n\t"
        "s_mov_b64 exec, s[82:83]\n\t"
        "buffer_load_dwordx4 v[152:155], %[vo], %[rs], %[b0] offen offset:2048 sc1\n\t"
        "s_mov_b64 exec, s[84:85]\n\t"
        "buffer_load_dwordx4 v[156:159], %[vo], %[rs], %[b0] offen offset:3072 sc1\n\t"
        "s_mov_b64 exec, s[86:87]\n\t"
        "buffer_load_dwordx4 v[160:163], %[vo], %[rs], %[b1] offen offset:0 sc1\n\t"
        "s_mov_b64 exec, -1\n\t"
        "s_sleep 2\n\t"
        "1:\n\t"
        "s_mov_b64 exec, s[78:79]\n\t"
        "buffer_load_dwordx4 v[144:147], %[vo], %[rs], %[b0] offen offset:0 sc1\n\t"
        "s_mov_b64 exec, s[80:81]\n\t"
        "buffer_load_dwordx4 v[148:151], %[vo], %[rs], %[b0] offen offset:1024 sc1\n\t"
        "s_mov_b64 exec, s[82:83]\n\t"
        "buffer_load_dwordx4 v[152:155], %[vo], %[rs], %[b0] offen offset:2048 sc1\n\t"
        "s_mov_b64 exec, s[84:85]\n\t"
        "buffer_load_dwordx4 v[156:159], %[vo], %[rs], %[b0] offen offset:3072 sc1\n\t"
        "s_mov_b64 exec, s[86:87]\n\t"
        "buffer_load_dwordx4 v[160:163], %[vo], %[rs], %[b1] offen offset:0 sc1\n\t"
        "s_mov_b64 exec, -1\n\t"
        "s_sub_u32 %[cap], %[cap], 1\n\t"
        "s_cbranch_scc1 3f\n\t"
        "s_waitcnt vmcnt(5)\n\t"
        "v_bitop3_b32 v140, v145, v147, %[tag] bitop3:0x7e\n\t"
        "v_bitop3_b32 v141, v149, v151, %[tag] bitop3:0x7e\n\t"
        "v_bitop3_b32 v142, v153, v155, %[tag] bitop3:0x7e\n\t"
        "v_bitop3_b32 v143, v157, v159, %[tag] bitop3:0x7e\n\t"
        "v_cmp_ne_u32_e64 s[78:79], 0, v140\n\t"
        "v_cmp_ne_u32_e64 s[80:81], 0, v141\n\t"
        "v_cmp_ne_u32_e64 s[82:83], 0, v142\n\t"
        "v_cmp_ne_u32_e64 s[84:85], 0, v143\n\t"
        "v_bitop3_b32 v140, v161, v163, %[tag] bitop3:0x7e\n\t"
        "v_cmp_ne_u32_e64 s[86:87], 0, v140\n\t"
        "s_or_b64 s[92:93], s[78:79], s[80:81]\n\t"
        "s_or_b64 s[92:93], s[92:93], s[82:83]\n\t"
        "s_or_b64 s[92:93], s[92:93], s[84:85]\n\t"
        "s_or_b64 s[92:93], s[92:93], s[86:87]\n\t"
        "s_cbranch_scc1 1b\n\t"
        "3:\n\t"
        "s_waitcnt vmcnt(0)\n\t"
        "v_add_f32 %[a0], v144, v148\n\t"
        "v_add_f32 %[a1], v146, v150\n\t"
        "v_add_f32 v140, v152, v156\n\t"
        "v_add_f32 v141, v154, v158\n\t"
        "v_add_f32 %[a0], %[a0], v140\n\t"
        "v_add_f32 %[a1], %[a1], v141\n\t"
        "v_add_f32 %[a0], %[a0], v160\n\t"
        "v_add_f32 %[a1], %[a1], v162\n\t"
        "s_nop 1"
        : [a0] "=&v"(a0), [a1] "=&v"(a1), [cap] "+&s"(cap) : [vo] "v"(voff), [rs] "s"(rs), [b0] "s"(base), [b1] "s"(base + 4096), [tag] "s"(tag)
        : "v140", "v141", "v142", "v143", "v144", "v145", "v146", "v147", "v148", "v149", "v150", "v151", "v152", "v153", "v154", "v155", "v156", "v157", "v158", "v159", "v160", "v161", "v162", "v163", "s78", "s79", "s80", "s81", "s82", "s83", "s84", "s85", "s86", "s87", "s92", "s93", "scc", "memory");
}
template <> __device__ __forceinline__ void sum_pairs<6, 0>(rsrc_t rs, unsigned voff, unsigned base, unsigned tag, float& a0, float& a1) {
    unsigned cap = SPIN_GLB;
    asm volatile(
        "buffer_load_dwordx4 v[144:147], %[vo], %[rs], %[b0] offen offset:0 sc1\n\t"
        "buffer_load_dwordx4 v[148:151], %[vo], %[rs], %[b0] offen offset:1024 sc1\n\t"
        "buffer_load_dwordx4 v[152:155], %[vo], %[rs], %[b0] offen offset:2048 sc1\n\t"
        "buffer_load_dwordx4 v[156:159], %[vo], %[rs], %[b0] offen offset:3072 sc1\n\t"
        "buffer_load_dwordx4 v[160:163], %[vo], %[rs], %[b1] offen offset:0 sc1\n\t"
        "buffer_load_dwordx4 v[164:167], %[vo], %[rs], %[b1] offen offset:1024 sc1\n\t"
        "s_waitcnt vmcnt(0)\n\t"
        "v_bitop3_b32 v140, v145, v147, %[tag] bitop3:0x7e\n\t"
        "v_bitop3_b32 v141, v149, v151, %[tag] bitop3:0x7e\n\t"
        "v_bitop3_b32 v142, v153, v155, %[tag] bitop3:0x7e\n\t"
        "v_bitop3_b32 v143, v157, v159, %[tag] bitop3:0x7e\n\t"
        "v_cmp_ne_u32_e64 s[78:79], 0, v140\n\t"
        "v_cmp_ne_u32_e64 s[80:81], 0, v141\n\t"
        "v_cmp_ne_u32_e64 s[82:83], 0, v142\n\t"
        "v_cmp_ne_u32_e64 s[84:85], 0, v143\n\t"
        "v_bitop3_b32 v140, v161, v163, %[tag] bitop3:0x7e\n\t"
        "v_bitop3_b32 v141, v165, v167, %[tag] bitop3:0x7e\n\t"
        "v_cmp_ne_u32_e64 s[86:87], 0, v140\n\t"
        "v_cmp_ne_u32_e64 s[88:89], 0, v141\n\t"
        "s_or_b64 s[92:93], s[78:79], s[80:81]\n\t"
        "s_or_b64 s[92:93], s[92:93], s[82:83]\n\t"
        "s_or_b64 s[92:93], s[92:93], s[84:85]\n\t"
        "s_or_b64 s[92:93], s[92:93], s[86:87]\n\t"
        "s_or_b64 s[92:93], s[92:93], s[88:89]\n\t"
        "s_cbranch_scc0 3f\n\t"
        "1:\n\t"
        "s_mov_b64 exec, s[78:79]\n\t"
        "buffer_load_dwordx4 v[144:147], %[vo], %[rs], %[b0] offen offset:0 sc1\n\t"
        "s_mov_b64 exec, s[80:81]\n\t"
        "buffer_load_dwordx4 v[148:151], %[vo], %[rs], %[b0] offen offset:1024 sc1\n\t"
        "s_mov_b64 exec, s[82:83]\n\t"
        "buffer_load_dwordx4 v[152:155], %[vo], %[rs], %[b0] offen offset:2048 sc1\n\t"
        "s_mov_b64 exec, s[84:85]\n\t"
        "buffer_load_dwordx4 v[156:159], %[vo], %[rs], %[b0] offen offset:3072 sc1\n\t"
        "s_mov_b64 exec, s[86:87]\n\t"
        "buffer_load_dwordx4 v[160:163], %[vo], %[rs], %[b1] offen offset:0 sc1\n\t"
        "s_mov_b64 exec, s[88:89]\n\t"
        "buffer_load_dwordx4 v[164:167], %[vo], %[rs], %[b1] offen offset:1024 sc1\n\t"
        "s_mov_b64 exec, -1\n\t"
        "s_sub_u32 %[cap], %[cap], 1\n\t"
        "s_cbranch_scc1 3f\n\t"
        "s_waitcnt vmcnt(0)\n\t"
        "v_bitop3_b32 v140, v145, v147, %[tag] bitop3:0x7e\n\t"
        "v_bitop3_b32 v141, v149, v151, %[tag] bitop3:0x7e\n\t"
        "v_bitop3_b32 v142, v153, v155, %[tag] bitop3:0x7e\n\t"
        "v_bitop3_b32 v143, v157, v159, %[tag] bitop3:0x7e\n\t"
        "v_cmp_ne_u32_e64 s[78:79], 0, v140\n\t"
        "v_cmp_ne_u32_e64 s[80:81], 0, v141\n\t"
        "v_cmp_ne_u32_e64 s[82:83], 0, v142\n\t"
        "v_cmp_ne_u32_e64 s[84:85], 0, v143\n\t"
        "v_bitop3_b32 v140, v161, v163, %[tag] bitop3:0x7e\n\t"
        "v_bitop3_b32 v141, v165, v167, %[tag] bitop3:0x7e\n\t"
        "v_cmp_ne_u32_e64 s[86:87], 0, v140\n\t"
        "v_cmp_ne_u32_e64 s[88:89], 0, v141\n\t"
        "s_or_b64 s[92:93], s[78:79], s[80:81]\n\t"
        "s_or_b64 s[92:93], s[92:93], s[82:83]\n\t"
        "s_or_b64 s[92:93], s[92:93], s[84:85]\n\t"
        "s_or_b64 s[92:93], s[92:93], s[86:87]\n\t"
        "s_or_b64 s[92:93], s[92:93], s[88:89]\n\t"
        "s_cbranch_scc1 1b\n\t"
        "3:\n\t"
        "s_waitcnt vmcnt(0)\n\t"
        "v_add_f32 %[a0], v144, v148\n\t"
        "v_add_f32 %[a1], v146, v150\n\t"
        "v_add_f32 v140, v152, v156\n\t"
        "v_add_f32 v141, v154, v158\n\t"
        "v_add_f32 v142, v160, v164\n\t"
        "v_add_f32 v143, v162, v166\n\t"
        "v_add_f32 %[a0], %[a0], v140\n\t"
        "v_add_f32 %[a1], %[a1], v141\n\t"
        "v_add_f32 %[a0], %[a0], v142\n\t"
        "v_add_f32 %[a1], %[a1], v143\n\t"
        "s_nop 1"
        : [a0] "=&v"(a0), [a1] "=&v"(a1), [cap] "+&s"(cap) : [vo] "v"(voff), [rs] "s"(rs), [b0] "s"(base), [b1] "s"(base + 4096), [tag] "s"(tag)
        : "v140", "v141", "v142", "v143", "v144", "v145", "v146", "v147", "v148", "v149", "v150", "v151", "v152", "v153", "v154", "v155", "v156", "v157", "v158", "v159", "v160", "v161", "v162", "v163", "v164", "v165", "v166", "v167", "s78", "s79", "s80", "s81", "s82", "s83", "s84", "s85", "s86", "s87", "s88", "s89", "s92", "s93", "scc", "memory");
}
template <> __device__ __forceinline__ void sum_pairs<6, 1>(rsrc_t rs, unsigned voff, unsigned base, unsigned tag, float& a0, float& a1) {
    unsigned cap = SPIN_GLB;
    asm volatile(
        "buffer_load_dwordx4 v[144:147], %[vo], %[rs], %[b0] offen offset:0 sc1\n\t"
        "buffer_load_dwordx4 v[148:151], %[vo], %[rs], %[b0] offen offset:1024 sc1\n\t"
        "buffer_load_dwordx4 v[152:155], %[vo], %[rs], %[b0] offen offset:2048 sc1\n\t"
        "buffer_load_dwordx4 v[156:159], %[vo], %[rs], %[b0] offen offset:3072 sc1\n\t"
        "buffer_load_dwordx4 v[160:163], %[vo], %[rs], %[b1] offen offset:0 sc1\n\t"
        "buffer_load_dwordx4 v[164:167], %[vo], %[rs], %[b1] offen offset:1024 sc1\n\t"
        "s_waitcnt vmcnt(0)\n\t"
        "v_bitop3_b32 v140, v145, v147, %[tag] bitop3:0x7e\n\t"
        "v_bitop3_b32 v141, v149, v151, %[tag] bitop3:0x7e\n\t"
        "v_bitop3_b32 v142, v153, v155, %[tag] bitop3:0x7e\n\t"
        "v_bitop3_b32 v143, v157, v159, %[tag] bitop3:0x7e\n\t"
        "v_cmp_ne_u32_e64 s[78:79], 0, v140\n\t"
        "v_cmp_ne_u32_e64 s[80:81], 0, v141\n\t"
        "v_cmp_ne_u32_e64 s[82:83], 0, v142\n\t"
        "v_cmp_ne_u32_e64 s[84:85], 0, v143\n\t"
        "v_bitop3_b32 v140, v161, v163, %[tag] bitop3:0x7e\n\t"
        "v_bitop3_b32 v141, v165, v167, %[tag] bitop3:0x7e\n\t"
        "v_cmp_ne_u32_e64 s[86:87], 0, v140\n\t"
        "v_cmp_ne_u32_e64 s[88:89], 0, v141\n\t"
        "s_or_b64 s[92:93], s[78:79], s[80:81]\n\t"
        "s_or_b64 s[92:93], s[92:93], s[82:83]\n\t"
        "s_or_b64 s[92:93], s[92:93], s[84:85]\n\t"
        "s_or_b64 s[92:93], s[92:93], s[86:87]\n\t"
        "s_or_b64 s[92:93], s[92:93], s[88:89]\n\t"
        "s_cbranch_scc0 3f\n\t"
        "s_mov_b64 exec, s[78:79]\n\t"
        "buffer_load_dwordx4 v[144:147], %[vo], %[rs], %[b0] offen offset:0 sc1\n\t"
        "s_mov_b64 exec, s[80:81]\n\t"
        "buffer_load_dwordx4 v[148:151], %[vo], %[rs], %[b0] offen offset:1024 sc1\n\t"
        "s_mov_b64 exec, s[82:83]\n\t"
        "buffer_load_dwordx4 v[152:155], %[vo], %[rs], %[b0] offen offset:2048 sc1\n\t"
        "s_mov_b64 exec, s[84:85]\n\t"
        "buffer_load_dwordx4 v[156:159], %[vo], %[rs], %[b0] offen offset:3072 sc1\n\t"
        "s_mov_b64 exec, s[86:87]\n\t"
        "buffer_load_dwordx4 v[160:163], %[vo], %[rs], %[b1] offen offset:0 sc1\n\t"
        "s_mov_b64 exec, s[88:89]\n\t"
        "buffer_load_dwordx4 v[164:167], %[vo], %[rs], %[b1] offen offset:1024 sc1\n\t"
        "s_mov_b64 exec, -1\n\t"
        "s_sleep 2\n\t"
        "1:\n\t"
        "s_mov_b64 exec, s[78:79]\n\t"
        "buffer_load_dwordx4 v[144:147], %[vo], %[rs], %[b0] offen offset:0 sc1\n\t"
        "s_mov_b64 exec, s[80:81]\n\t"
        "buffer_load_dwordx4 v[148:151], %[vo], %[rs], %[b0] offen offset:1024 sc1\n\t"
        "s_mov_b64 exec, s[82:83]\n\t"
        "buffer_load_dwordx4 v[152:155], %[vo], %[rs], %[b0] offen offset:2048 sc1\n\t"
        "s_mov_b64 exec, s[84:85]\n\t"
        "buffer_load_dwordx4 v[156:159], %[vo], %[rs], %[b0] offen offset:3072 sc1\n\t"
        "s_mov_b64 exec, s[86:87]\n\t"
        "buffer_load_dwordx4 v[160:163], %[vo], %[rs], %[b1] offen offset:0 sc1\n\t"
        "s_mov_b64 exec, s[88:89]\n\t"
        "buffer_load_dwordx4 v[164:167], %[vo], %[rs], %[b1] offen offset:1024 sc1\n\t"
        "s_mov_b64 exec, -1\n\t"
        "s_sub_u32 %[cap], %[cap], 1\n\t"
        "s_cbranch_scc1 3f\n\t"
        "s_waitcnt vmcnt(6)\n\t"
        "v_bitop3_b32 v140, v145, v147, %[tag] bitop3:0x7e\n\t"
        "v_bitop3_b32 v141, v149, v151, %[tag] bitop3:0x7e\n\t"
        "v_bitop3_b32 v142, v153, v155, %[tag] bitop3:0x7e\n\t"
        "v_bitop3_b32 v143, v157, v159, %[tag] bitop3:0x7e\n\t"
        "v_cmp_ne_u32_e64 s[78:79], 0, v140\n\t"
        "v_cmp_ne_u32_e64 s[80:81], 0, v141\n\t"
        "v_cmp_ne_u32_e64 s[82:83], 0, v142\n\t"
        "v_cmp_ne_u32_e64 s[84:85], 0, v143\n\t"
        "v_bitop3_b32 v140, v161, v163, %[tag] bitop3:0x7e\n\t"
        "v_bitop3_b32 v141, v165, v167, %[tag] bitop3:0x7e\n\t"
        "v_cmp_ne_u32_e64 s[86:87], 0, v140\n\t"
        "v_cmp_ne_u32_e64 s[88:89], 0, v141\n\t"
        "s_or_b64 s[92:93], s[78:79], s[80:81]\n\t"
        "s_or_b64 s[92:93], s[92:93], s[82:83]\n\t"
        "s_or_b64 s[92:93], s[92:93], s[84:85]\n\t"
        "s_or_b64 s[92:93], s[92:93], s[86:87]\n\t"
        "s_or_b64 s[92:93], s[92:93], s[88:89]\n\t"
        "s_cbranch_scc1 1b\n\t"
        "3:\n\t"
        "s_waitcnt vmcnt(0)\n\t"
        "v_add_f32 %[a0], v144, v148\n\t"
        "v_add_f32 %[a1], v146, v150\n\t"
        "v_add_f32 v140, v152, v156\n\t"
        "v_add_f32 v141, v154, v158\n\t"
        "v_add_f32 v142, v160, v164\n\t"
        "v_add_f32 v143, v162, v166\n\t"
        "v_add_f32 %[a0], %[a0], v140\n\t"
        "v_add_f32 %[a1], %[a1], v141\n\t"
        "v_add_f32 %[a0], %[a0], v142\n\t"
        "v_add_f32 %[a1], %[a1], v143\n\t"
        "s_nop 1"
        : [a0] "=&v"(a0), [a1] "=&v"(a1), [cap] "+&s"(cap) : [vo] "v"(voff), [rs] "s"(rs), [b0] "s"(base), [b1] "s"(base + 4096), [tag] "s"(tag)
        : "v140", "v141", "v142", "v143", "v144", "v145", "v146", "v147", "v148", "v149", "v150", "v151", "v152", "v153", "v154", "v155", "v156", "v157", "v158", "v159", "v160", "v161", "v162", "v163", "v164", "v165", "v166", "v167", "s78", "s79", "s80", "s81", "s82", "s83", "s84", "s85", "s86", "s87", "s88", "s89", "s92", "s93", "scc", "memory");
}
template <> __device__ __forceinline__ float combine_o<0>(rsrc_t rs, unsigned voff, unsigned base, unsigned tag, int j, unsigned own, f32x4 w0, f32x4 w1, unsigned napn) {
    unsigned cap = SPIN_GLB;
    float o;
    asm volatile(
        "s_cmp_eq_u32 %[j], 0\n\t"
        "s_cselect_b64 s[74:75], 0, %[lm]\n\t"
        "v_mov_b32 v144, %[own]\n\t"
        "v_mov_b32 v145, %[tag]\n\t"
        "s_cmp_eq_u32 %[j], 1\n\t"
        "s_cselect_b64 s[76:77], 0, %[lm]\n\t"
        "v_mov_b32 v146, %[own]\n\t"
        "v_mov_b32 v147, %[tag]\n\t"
        "s_cmp_eq_u32 %[j], 2\n\t"
        "s_cselect_b64 s[78:79], 0, %[lm]\n\t"
        "v_mov_b32 v148, %[own]\n\t"
        "v_mov_b32 v149, %[tag]\n\t"
        "s_cmp_eq_u32 %[j], 3\n\t"
        "s_cselect_b64 s[80:81], 0, %[lm]\n\t"
        "v_mov_b32 v150, %[own]\n\t"
        "v_mov_b32 v151, %[tag]\n\t"
        "s_cmp_eq_u32 %[j], 4\n\t"
        "s_cselect_b64 s[82:83], 0, %[lm]\n\t"
        "v_mov_b32 v152, %[own]\n\t"
        "v_mov_b32 v153, %[tag]\n\t"
        "s_cmp_eq_u32 %[j], 5\n\t"
        "s_cselect_b64 s[84:85], 0, %[lm]\n\t"
        "v_mov_b32 v154, %[own]\n\t"
        "v_mov_b32 v155, %[tag]\n\t"
        "s_cmp_eq_u32 %[j], 6\n\t"
        "s_cselect_b64 s[86:87], 0, %[lm]\n\t"
        "v_mov_b32 v156, %[own]\n\t"
        "v_mov_b32 v157, %[tag]\n\t"
        "s_cmp_eq_u32 %[j], 7\n\t"
        "s_cselect_b64 s[88:89], 0, %[lm]\n\t"
        "v_mov_b32 v158, %[own]\n\t"
        "v_mov_b32 v159, %[tag]\n\t"
        "s_cmp_eq_u32 %[nap], 0\n\t"
        "s_cbranch_scc1 5f\n\t"
        "4:\n\t"
        "s_sleep 1\n\t"
        "s_sub_u32 %[nap], %[nap], 1\n\t"
        "s_cmp_lg_u32 %[nap], 0\n\t"
        "s_cbranch_scc1 4b\n\t"
        "5:\n\t"
        "s_mov_b64 exec, s[74:75]\n\t"
        "buffer_load_dwordx2 v[144:145], %[vo], %[rs], %[b0] offen offset:0 sc1\n\t"
        "s_mov_b64 exec, s[76:77]\n\t"
        "buffer_load_dwordx2 v[146:147], %[vo], %[rs], %[b0] offen offset:1536 sc1\n\t"
        "s_mov_b64 exec, s[78:79]\n\t"
        "buffer_load_dwordx2 v[148:149], %[vo], %[rs], %[b0] offen offset:3072 sc1\n\t"
        "s_mov_b64 exec, s[80:81]\n\t"
        "buffer_load_dwordx2 v[150:151], %[vo], %[rs], %[b1] offen offset:0 sc1\n\t"
        "s_mov_b64 exec, s[82:83]\n\t"
        "buffer_load_dwordx2 v[152:153], %[vo], %[rs], %[b1] offen offset:1536 sc1\n\t"
        "s_mov_b64 exec, s[84:85]\n\t"
        "buffer_load_dwordx2 v[154:155], %[vo], %[rs], %[b1] offen offset:3072 sc1\n\t"
        "s_mov_b64 exec, s[86:87]\n\t"
        "buffer_load_dwordx2 v[156:157], %[vo], %[rs], %[b2] offen offset:0 sc1\n\t"
        "s_mov_b64 exec, s[88:89]\n\t"
        "buffer_load_dwordx2 v[158:159], %[vo], %[rs], %[b2] offen offset:1536 sc1\n\t"
        "s_mov_b64 exec, -1\n\t"
        "s_waitcnt vmcnt(0)\n\t"
        "v_cmp_ne_u32_e64 s[74:75], %[tag], v145\n\t"
        "v_cmp_ne_u32_e64 s[76:77], %[tag], v147\n\t"
        "v_cmp_ne_u32_e64 s[78:79], %[tag], v149\n\t"
        "v_cmp_ne_u32_e64 s[80:81], %[tag], v151\n\t"
        "v_cmp_ne_u32_e64 s[82:83], %[tag], v153\n\t"
        "v_cmp_ne_u32_e64 s[84:85], %[tag], v155\n\t"
        "v_cmp_ne_u32_e64 s[86:87], %[tag], v157\n\t"
        "v_cmp_ne_u32_e64 s[88:89], %[tag], v159\n\t"
        "s_or_b64 s[92:93], s[74:75], s[76:77]\n\t"
        "s_or_b64 s[92:93], s[92:93], s[78:79]\n\t"
        "s_or_b64 s[92:93], s[92:93], s[80:81]\n\t"
        "s_or_b64 s[92:93], s[92:93], s[82:83]\n\t"
        "s_or_b64 s[92:93], s[92:93], s[84:85]\n\t"
        "s_or_b64 s[92:93], s[92:93], s[86:87]\n\t"
        "s_or_b64 s[92:93], s[92:93], s[88:89]\n\t"
        "s_cbranch_scc0 3f\n\t"
        "1:\n\t"
        "s_mov_b64 exec, s[74:75]\n\t"
        "buffer_load_dwordx2 v[144:145], %[vo], %[rs], %[b0] offen offset:0 sc1\n\t"
        "s_mov_b64 exec, s[76:77]\n\t"
        "buffer_load_dwordx2 v[146:147], %[vo], %[rs], %[b0] offen offset:1536 sc1\n\t"
        "s_mov_b64 exec, s[78:79]\n\t"
        "buffer_load_dwordx2 v[148:149], %[vo], %[rs], %[b0] offen offset:3072 sc1\n\t"
        "s_mov_b64 exec, s[80:81]\n\t"
        "buffer_load_dwordx2 v[150:151], %[vo], %[rs], %[b1] offen offset:0 sc1\n\t"
        "s_mov_b64 exec, s[82:83]\n\t"
        "buffer_load_dwordx2 v[152:153], %[vo], %[rs], %[b1] offen offset:1536 sc1\n\t"
        "s_mov_b64 exec, s[84:85]\n\t"
        "buffer_load_dwordx2 v[154:155], %[vo], %[rs], %[b1] offen offset:3072 sc1\n\t"
        "s_mov_b64 exec, s[86:87]\n\t"
        "buffer_load_dwordx2 v[156:157], %[vo], %[rs], %[b2] offen offset:0 sc1\n\t"
        "s_mov_b64 exec, s[88:89]\n\t"
        "buffer_load_dwordx2 v[158:159], %[vo], %[rs], %[b2] offen offset:1536 sc1\n\t"
        "s_mov_b64 exec, -1\n\t"
        "s_sub_u32 %[cap], %[cap], 1\n\t"
        "s_cbranch_scc1 3f\n\t"
        "s_waitcnt vmcnt(0)\n\t"
        "v_cmp_ne_u32_e64 s[74:75], %[tag], v145\n\t"
        "v_cmp_ne_u32_e64 s[76:77], %[tag], v147\n\t"
        "v_cmp_ne_u32_e64 s[78:79], %[tag], v149\n\t"
        "v_cmp_ne_u32_e64 s[80:81], %[tag], v151\n\t"
        "v_cmp_ne_u32_e64 s[82:83], %[tag], v153\n\t"
        "v_cmp_ne_u32_e64 s[84:85], %[tag], v155\n\t"
        "v_cmp_ne_u32_e64 s[86:87], %[tag], v157\n\t"
        "v_cmp_ne_u32_e64 s[88:89], %[tag], v159\n\t"
        "s_or_b64 s[92:93], s[74:75], s[76:77]\n\t"
        "s_or_b64 s[92:93], s[92:93], s[78:79]\n\t"
        "s_or_b64 s[92:93], s[92:93], s[80:81]\n\t"
        "s_or_b64 s[92:93], s[92:93], s[82:83]\n\t"
        "s_or_b64 s[92:93], s[92:93], s[84:85]\n\t"
        "s_or_b64 s[92:93], s[92:93], s[86:87]\n\t"
        "s_or_b64 s[92:93], s[92:93], s[88:89]\n\t"
        "s_cbranch_scc1 1b\n\t"
        "3:\n\t"
        "s_waitcnt vmcnt(0)\n\t"
        "v_mul_f32 %[o], %[w0], v144\n\t"
        "v_mul_f32 v140, %[w4], v152\n\t"
        "v_fmac_f32 %[o], %[w1], v146\n\t"
        "v_fmac_f32 v140, %[w5], v154\n\t"
        "v_fmac_f32 %[o], %[w2], v148\n\t"
        "v_fmac_f32 v140, %[w6], v156\n\t"
        "v_fmac_f32 %[o], %[w3], v150\n\t"
        "v_fmac_f32 v140, %[w7], v158\n\t"
        "v_add_f32 %[o], %[o], v140\n\t"
        : [o] "=&v"(o), [cap] "+&s"(cap), [nap] "+&s"(napn)
        : [vo] "v"(voff), [rs] "s"(rs), [b0] "s"(base), [b1] "s"(base + 4608), [b2] "s"(base + 9216), [tag] "s"(tag), [j] "s"(j), [own] "v"(own),
          [lm] "s"(0xFFFFFFFFFFFFull), [w0] "v"(w0[0]), [w1] "v"(w0[1]), [w2] "v"(w0[2]), [w3] "v"(w0[3]), [w4] "v"(w1[0]), [w5] "v"(w1[1]), [w6] "v"(w1[2]), [w7] "v"(w1[3])
        : "v140", "v144", "v145", "v146", "v147", "v148", "v149", "v150", "v151", "v152", "v153", "v154", "v155", "v156", "v157", "v158", "v159", "s74", "s75", "s76", "s77", "s78", "s79", "s80", "s81", "s82", "s83", "s84", "s85", "s86", "s87", "s88", "s89", "s92", "s93", "scc", "memory");
    return o;
}
template <> __device__ __forceinline__ float combine_o<1>(rsrc_t rs, unsigned voff, unsigned base, unsigned tag, int j, unsigned own, f32x4 w0, f32x4 w1, unsigned napn) {
    unsigned cap = SPIN_GLB;
    float o;
    asm volatile(
        "s_cmp_eq_u32 %[j], 0\n\t"
        "s_cselect_b64 s[74:75], 0, %[lm]\n\t"
        "v_mov_b32 v144, %[own]\n\t"
        "v_mov_b32 v145, %[tag]\n\t"
        "s_cmp_eq_u32 %[j], 1\n\t"
        "s_cselect_b64 s[76:77], 0, %[lm]\n\t"
        "v_mov_b32 v146, %[own]\n\t"
        "v_mov_b32 v147, %[tag]\n\t"
        "s_cmp_eq_u32 %[j], 2\n\t"
        "s_cselect_b64 s[78:79], 0, %[lm]\n\t"
        "v_mov_b32 v148, %[own]\n\t"
        "v_mov_b32 v149, %[tag]\n\t"
        "s_cmp_eq_u32 %[j], 3\n\t"
        "s_cselect_b64 s[80:81], 0, %[lm]\n\t"
        "v_mov_b32 v150, %[own]\n\t"
        "v_mov_b32 v151, %[tag]\n\t"
        "s_cmp_eq_u32 %[j], 4\n\t"
        "s_cselect_b64 s[82:83], 0, %[lm]\n\t"
        "v_mov_b32 v152, %[own]\n\t"
        "v_mov_b32 v153, %[tag]\n\t"
        "s_cmp_eq_u32 %[j], 5\n\t"
        "s_cselect_b64 s[84:85], 0, %[lm]\n\t"
        "v_mov_b32 v154, %[own]\n\t"
        "v_mov_b32 v155, %[tag]\n\t"
        "s_cmp_eq_u32 %[j], 6\n\t"
        "s_cselect_b64 s[86:87], 0, %[lm]\n\t"
        "v_mov_b32 v156, %[own]\n\t"
        "v_mov_b32 v157, %[tag]\n\t"
        "s_cmp_eq_u32 %[j], 7\n\t"
        "s_cselect_b64 s[88:89], 0, %[lm]\n\t"
        "v_mov_b32 v158, %[own]\n\t"
        "v_mov_b32 v159, %[tag]\n\t"
        "s_cmp_eq_u32 %[nap], 0\n\t"
        "s_cbranch_scc1 5f\n\t"
        "4:\n\t"
        "s_sleep 1\n\t"
        "s_sub_u32 %[nap], %[nap], 1\n\t"
        "s_cmp_lg_u32 %[nap], 0\n\t"
        "s_cbranch_scc1 4b\n\t"
        "5:\n\t"
        "s_mov_b64 exec, s[74:75]\n\t"
        "buffer_load_dwordx2 v[144:145], %[vo], %[rs], %[b0] offen offset:0 sc1\n\t"
        "s_mov_b64 exec, s[76:77]\n\t"
        "buffer_load_dwordx2 v[146:147], %[vo], %[rs], %[b0] offen offset:1536 sc1\n\t"
        "s_mov_b64 exec, s[78:79]\n\t"
        "buffer_load_dwordx2 v[148:149], %[vo], %[rs], %[b0] offen offset:3072 sc1\n\t"
        "s_mov_b64 exec, s[80:81]\n\t"
        "buffer_load_dwordx2 v[150:151], %[vo], %[rs], %[b1] offen offset:0 sc1\n\t"
        "s_mov_b64 exec, s[82:83]\n\t"
        "buffer_load_dwordx2 v[152:153], %[vo], %[rs], %[b1] offen offset:1536 sc1\n\t"
        "s_mov_b64 exec, s[84:85]\n\t"
        "buffer_load_dwordx2 v[154:155], %[vo], %[rs], %[b1] offen offset:3072 sc1\n\t"
        "s_mov_b64 exec, s[86:87]\n\t"
        "buffer_load_dwordx2 v[156:157], %[vo], %[rs], %[b2] offen offset:0 sc1\n\t"
        "s_mov_b64 exec, s[88:89]\n\t"
        "buffer_load_dwordx2 v[158:159], %[vo], %[rs], %[b2] offen offset:1536 sc1\n\t"
        "s_mov_b64 exec, -1\n\t"
        "s_waitcnt vmcnt(0)\n\t"
        "v_cmp_ne_u32_e64 s[74:75], %[tag], v145\n\t"
        "v_cmp_ne_u32_e64 s[76:77], %[tag], v147\n\t"
        "v_cmp_ne_u32_e64 s[78:79], %[tag], v149\n\t"
        "v_cmp_ne_u32_e64 s[80:81], %[tag], v151\n\t"
        "v_cmp_ne_u32_e64 s[82:83], %[tag], v153\n\t"
        "v_cmp_ne_u32_e64 s[84:85], %[tag], v155\n\t"
        "v_cmp_ne_u32_e64 s[86:87], %[tag], v157\n\t"
        "v_cmp_ne_u32_e64 s[88:89], %[tag], v159\n\t"
        "s_or_b64 s[92:93], s[74:75], s[76:77]\n\t"
        "s_or_b64 s[92:93], s[92:93], s[78:79]\n\t"
        "s_or_b64 s[92:93], s[92:93], s[80:81]\n\t"
        "s_or_b64 s[92:93], s[92:93], s[82:83]\n\t"
        "s_or_b64 s[92:93], s[92:93], s[84:85]\n\t"
        "s_or_b64 s[92:93], s[92:93], s[86:87]\n\t"
        "s_or_b64 s[92:93], s[92:93], s[88:89]\n\t"
        "s_cbranch_scc0 3f\n\t"
        "s_mov_b64 exec, s[74:75]\n\t"
        "buffer_load_dwordx2 v[144:145], %[vo], %[rs], %[b0] offen offset:0 sc1\n\t"
        "s_mov_b64 exec, s[76:77]\n\t"
        "buffer_load_dwordx2 v[146:147], %[vo], %[rs], %[b0] offen offset:1536 sc1\n\t"
        "s_mov_b64 exec, s[78:79]\n\t"
        "buffer_load_dwordx2 v[148:149], %[vo], %[rs], %[b0] offen offset:3072 sc1\n\t"
        "s_mov_b64 exec, s[80:81]\n\t"
        "buffer_load_dwordx2 v[150:151], %[vo], %[rs], %[b1] offen offset:0 sc1\n\t"
        "s_mov_b64 exec, s[82:83]\n\t"
        "buffer_load_dwordx2 v[152:153], %[vo], %[rs], %[b1] offen offset:1536 sc1\n\t"
        "s_mov_b64 exec, s[84:85]\n\t"
        "buffer_load_dwordx2 v[154:155], %[vo], %[rs], %[b1] offen offset:3072 sc1\n\t"
        "s_mov_b64 exec, s[86:87]\n\t"
        "buffer_load_dwordx2 v[156:157], %[vo], %[rs], %[b2] offen offset:0 sc1\n\t"
        "s_mov_b64 exec, s[88:89]\n\t"
        "buffer_load_dwordx2 v[158:159], %[vo], %[rs], %[b2] offen offset:1536 sc1\n\t"
        "s_mov_b64 exec, -1\n\t"
        "s_sleep 2\n\t"
        "1:\n\t"
        "s_mov_b64 exec, s[74:75]\n\t"
        "buffer_load_dwordx2 v[144:145], %[vo], %[rs], %[b0] offen offset:0 sc1\n\t"
        "s_mov_b64 exec, s[76:77]\n\t"
        "buffer_load_dwordx2 v[146:147], %[vo], %[rs], %[b0] offen offset:1536 sc1\n\t"
        "s_mov_b64 exec, s[78:79]\n\t"
        "buffer_load_dwordx2 v[148:149], %[vo], %[rs], %[b0] offen offset:3072 sc1\n\t"
        "s_mov_b64 exec, s[80:81]\n\t"
        "buffer_load_dwordx2 v[150:151], %[vo], %[rs], %[b1] offen offset:0 sc1\n\t"
        "s_mov_b64 exec, s[82:83]\n\t"
        "buffer_load_dwordx2 v[152:153], %[vo], %[rs], %[b1] offen offset:1536 sc1\n\t"
        "s_mov_b64 exec, s[84:85]\n\t"
        "buffer_load_dwordx2 v[154:155], %[vo], %[rs], %[b1] offen offset:3072 sc1\n\t"
        "s_mov_b64 exec, s[86:87]\n\t"
        "buffer_load_dwordx2 v[156:157], %[vo], %[rs], %[b2] offen offset:0 sc1\n\t"
        "s_mov_b64 exec, s[88:89]\n\t"
        "buffer_load_dwordx2 v[158:159], %[vo], %[rs], %[b2] offen offset:1536 sc1\n\t"
        "s_mov_b64 exec, -1\n\t"
        "s_sub_u32 %[cap], %[cap], 1\n\t"
        "s_cbranch_scc1 3f\n\t"
        "s_waitcnt vmcnt(8)\n\t"
        "v_cmp_ne_u32_e64 s[74:75], %[tag], v145\n\t"
        "v_cmp_ne_u32_e64 s[76:77], %[tag], v147\n\t"
        "v_cmp_ne_u32_e64 s[78:79], %[tag], v149\n\t"
        "v_cmp_ne_u32_e64 s[80:81], %[tag], v151\n\t"
        "v_cmp_ne_u32_e64 s[82:83], %[tag], v153\n\t"
        "v_cmp_ne_u32_e64 s[84:85], %[tag], v155\n\t"
        "v_cmp_ne_u32_e64 s[86:87], %[tag], v157\n\t"
        "v_cmp_ne_u32_e64 s[88:89], %[tag], v159\n\t"
        "s_or_b64 s[92:93], s[74:75], s[76:77]\n\t"
        "s_or_b64 s[92:93], s[92:93], s[78:79]\n\t"
        "s_or_b64 s[92:93], s[92:93], s[80:81]\n\t"
        "s_or_b64 s[92:93], s[92:93], s[82:83]\n\t"
        "s_or_b64 s[92:93], s[92:93], s[84:85]\n\t"
        "s_or_b64 s[92:93], s[92:93], s[86:87]\n\t"
        "s_or_b64 s[92:93], s[92:93], s[88:89]\n\t"
        "s_cbranch_scc1 1b\n\t"
        "3:\n\t"
        "s_waitcnt vmcnt(0)\n\t"
        "v_mul_f32 %[o], %[w0], v144\n\t"
        "v_mul_f32 v140, %[w4], v152\n\t"
        "v_fmac_f32 %[o], %[w1], v146\n\t"
        "v_fmac_f32 v140, %[w5], v154\n\t"
        "v_fmac_f32 %[o], %[w2], v148\n\t"
        "v_fmac_f32 v140, %[w6], v156\n\t"
        "v_fmac_f32 %[o], %[w3], v150\n\t"
        "v_fmac_f32 v140, %[w7], v158\n\t"
        "v_add_f32 %[o], %[o], v140\n\t"
        : [o] "=&v"(o), [cap] "+&s"(cap), [nap] "+&s"(napn)
        : [vo] "v"(voff), [rs] "s"(rs), [b0] "s"(base), [b1] "s"(base + 4608), [b2] "s"(base + 9216), [tag] "s"(tag), [j] "s"(j), [own] "v"(own),
          [lm] "s"(0xFFFFFFFFFFFFull), [w0] "v"(w0[0]), [w1] "v"(w0[1]), [w2] "v"(w0[2]), [w3] "v"(w0[3]), [w4] "v"(w1[0]), [w5] "v"(w1[1]), [w6] "v"(w1[2]), [w7] "v"(w1[3])
        : "v140", "v144", "v145", "v146", "v147", "v148", "v149", "v150", "v151", "v152", "v153", "v154", "v155", "v156", "v157", "v158", "v159", "s74", "s75", "s76", "s77", "s78", "s79", "s80", "s81", "s82", "s83", "s84", "s85", "s86", "s87", "s88", "s89", "s92", "s93", "scc", "memory");
    return o;
}
#define PUMP 0
__device__ __forceinline__ s16x4 trd(LAS unsigned char* L, unsigned off) { return __builtin_amdgcn_ds_read_tr16_b64_v4i16((LAS s16x4*)(L + off)); }
__device__ __forceinline__ bf16x8 cat8(s16x4 a, s16x4 b) { return __builtin_bit_cast(bf16x8, __builtin_shufflevector(a, b, 0, 1, 2, 3, 4, 5, 6, 7)); }
__device__ __forceinline__ f32x4 mfma32(bf16x8 a, bf16x8 b, f32x4 c) { return __builtin_amdgcn_mfma_f32_16x16x32_bf16(a, b, c, 0, 0, 0); }
__device__ __forceinline__ bf16x8 asb(v4u a) { return __builtin_bit_cast(bf16x8, a); }

__device__ __forceinline__ void spin_rd(int c, unsigned n, unsigned a, bf16x8 (&o)[1]) {
    unsigned f, cap = SPIN_LDS;
    const unsigned fa = L_CNT + 4 * c;
    asm volatile(
        "1:\n\t"
        "ds_read_b32 %[f], %[fa]\n\t"
        "ds_read_b128 %[o0], %[a] offset:0\n\t"
        "s_sub_u32 %[cap], %[cap], 1\n\t"
        "s_cbranch_scc1 2f\n\t"
        "s_waitcnt lgkmcnt(1)\n\t"
        "v_readfirstlane_b32 s92, %[f]\n\t"
        "s_cmp_lt_u32 s92, %[n]\n\t"
        "s_cbranch_scc1 1b\n\t"
        "2:\n\t"
        "s_waitcnt lgkmcnt(0)"
        : [f] "=&v"(f), [cap] "+&s"(cap), [o0] "=&v"(o[0]) : [fa] "v"(fa), [a] "v"(a), [n] "s"(n) : "s92", "scc", "memory");
}
__device__ __forceinline__ void spin_rd(int c, unsigned n, unsigned a, bf16x8 (&o)[3]) {
    unsigned f, cap = SPIN_LDS;
    const unsigned fa = L_CNT + 4 * c;
    asm volatile(
        "1:\n\t"
        "ds_read_b32 %[f], %[fa]\n\t"
        "ds_read_b128 %[o0], %[a] offset:0\n\t"
        "ds_read_b128 %[o1], %[a] offset:64\n\t"
        "ds_read_b128 %[o2], %[a] offset:128\n\t"
        "s_sub_u32 %[cap], %[cap], 1\n\t"
        "s_cbranch_scc1 2f\n\t"
        "s_waitcnt lgkmcnt(3)\n\t"
        "v_readfirstlane_b32 s92, %[f]\n\t"
        "s_cmp_lt_u32 s92, %[n]\n\t"
        "s_cbranch_scc1 1b\n\t"
        "2:\n\t"
        "s_waitcnt lgkmcnt(0)"
        : [f] "=&v"(f), [cap] "+&s"(cap), [o0] "=&v"(o[0]), [o1] "=&v"(o[1]), [o2] "=&v"(o[2]) : [fa] "v"(fa), [a] "v"(a), [n] "s"(n) : "s92", "scc", "memory");
}
__device__ __forceinline__ void spin_rd(int c, unsigned n, unsigned a, bf16x8 (&o)[6]) {
    unsigned f, cap = SPIN_LDS;
    const unsigned fa = L_CNT + 4 * c;
    asm volatile(
        "1:\n\t"
        "ds_read_b32 %[f], %[fa]\n\t"
        "ds_read_b128 %[o0], %[a] offset:0\n\t"
        "ds_read_b128 %[o1], %[a] offset:64\n\t"
        "ds_read_b128 %[o2], %[a] offset:128\n\t"
        "ds_read_b128 %[o3], %[a] offset:192\n\t"
        "ds_read_b128 %[o4], %[a] offset:256\n\t"
        "ds_read_b128 %[o5], %[a] offset:320\n\t"
        "s_sub_u32 %[cap], %[cap], 1\n\t"
        "s_cbranch_scc1 2f\n\t"
        "s_waitcnt lgkmcnt(6)\n\t"
        "v_readfirstlane_b32 s92, %[f]\n\t"
        "s_cmp_lt_u32 s92, %[n]\n\t"
        "s_cbranch_scc1 1b\n\t"
        "2:\n\t"
        "s_waitcnt lgkmcnt(0)"
        : [f] "=&v"(f), [cap] "+&s"(cap), [o0] "=&v"(o[0]), [o1] "=&v"(o[1]), [o2] "=&v"(o[2]), [o3] "=&v"(o[3]), [o4] "=&v"(o[4]), [o5] "=&v"(o[5]) : [fa] "v"(fa), [a] "v"(a), [n] "s"(n) : "s92", "scc", "memory");
}
template <int OFF> __device__ __forceinline__ void spin_tr(int c, unsigned n, const unsigned (&a)[3], s16x4 (&o)[6]) {
    unsigned f, cap = SPIN_LDS;
    const unsigned fa = L_CNT + 4 * c;
    asm volatile(
        "1:\n\t"
        "ds_read_b32 %[f], %[fa]\n\t"
        "ds_read_b64_tr_b16 %[o0], %[a0] offset:%c[off0]\n\t"
        "ds_read_b64_tr_b16 %[o1], %[a0] offset:%c[off1]\n\t"
        "ds_read_b64_tr_b16 %[o2], %[a1] offset:%c[off0]\n\t"
        "ds_read_b64_tr_b16 %[o3], %[a1] offset:%c[off1]\n\t"
        "ds_read_b64_tr_b16 %[o4], %[a2] offset:%c[off0]\n\t"
        "ds_read_b64_tr_b16 %[o5], %[a2] offset:%c[off1]\n\t"
        "s_sub_u32 %[cap], %[cap], 1\n\t"
        "s_cbranch_scc1 2f\n\t"
        "s_waitcnt lgkmcnt(6)\n\t"
        "v_readfirstlane_b32 s92, %[f]\n\t"
        "s_cmp_lt_u32 s92, %[n]\n\t"
        "s_cbranch_scc1 1b\n\t"
        "2:\n\t"
        "s_waitcnt lgkmcnt(0)"
        : [f] "=&v"(f), [cap] "+&s"(cap), [o0] "=&v"(o[0]), [o1] "=&v"(o[1]), [o2] "=&v"(o[2]), [o3] "=&v"(o[3]), [o4] "=&v"(o[4]), [o5] "=&v"(o[5])
        : [fa] "v"(fa), [a0] "v"(a[0]), [a1] "v"(a[1]), [a2] "v"(a[2]), [n] "s"(n), [off0] "n"(OFF), [off1] "n"(OFF + 1536) : "s92", "scc", "memory");
}
__device__ __forceinline__ void ln_stats(const unsigned (&xp)[5], float& mb, float& rden) {
    const float c = lo16(__builtin_amdgcn_readfirstlane(xp[0]));
    float s = 0.f, s2 = 0.f;
#pragma unroll
    for (int i = 0; i < 5; ++i) {
        const float a = lo16(xp[i]) - c, b = hi16(xp[i]) - c;
        s += a + b; s2 = fmaf(a, a, s2); s2 = fmaf(b, b, s2);
    }
    s = wave_sum(s); s2 = wave_sum(s2);
    const float dm = s * (1.0f / D);
    const float var = fmaxf(s2 * (1.0f / D) - dm * dm, 0.f);
    mb = rbf(c + dm);
    const float den = rbf(__builtin_amdgcn_sqrtf(rbf(rbf(var) + 1e-5f)));
    rden = __builtin_amdgcn_rcpf(den);
}
__device__ __forceinline__ unsigned ln_pair(unsigned w, float mb, float rden) {
    const unsigned d = pk2(lo16(w) - mb, hi16(w) - mb);
    return pk2(lo16(d) * rden, hi16(d) * rden);
}

template <int N> __device__ __forceinline__ unsigned reduce_cols(rsrc_t rs, unsigned voff, unsigned base, unsigned tag, unsigned res) {
    float r0 = lo16(res), r1 = hi16(res);
    PIN(r0); PIN(r1);
    float s0, s1;
    sum_pairs<N, PUMP>(rs, voff, base, tag, s0, s1);
    auto sw = __builtin_amdgcn_permlane32_swap(__float_as_uint(s0), __float_as_uint(s1), false, false);
    float t = fv(sw[0]) + fv(sw[1]);
    t += dpp<XOR8>(t);
    t = sw16_sum(t);
    auto s2 = __builtin_amdgcn_permlane32_swap(__float_as_uint(t), __float_as_uint(t), false, false);
    const unsigned sp = pk2(fv(s2[0]), fv(s2[1]));
    return pk2(r0 + lo16(sp), r1 + hi16(sp));
}
__device__ __forceinline__ float fold5(const v4u (&p)[5]) {
    const float a0 = ((fv(p[0][0]) + fv(p[1][0])) + (fv(p[2][0]) + fv(p[3][0]))) + fv(p[4][0]);
    const float a1 = ((fv(p[0][2]) + fv(p[1][2])) + (fv(p[2][2]) + fv(p[3][2]))) + fv(p[4][2]);
    auto sw = __builtin_amdgcn_permlane32_swap(__float_as_uint(a0), __float_as_uint(a1), false, false);
    return fv(sw[0]) + fv(sw[1]);
}
__device__ __forceinline__ unsigned lateness(rsrc_t rs, int lane, u64 t_start) {
    const v2u t = __builtin_amdgcn_raw_buffer_load_b64(rs, WS_T0 + (lane & 7) * 128, 0, 16);
    const u64 d = t_start - (((u64)t[1] << 32) | t[0]);
    float l = d < 150ull ? (float)(unsigned)d : 0.f;
    l = fmaxf(l, dpp<XOR1>(l)); l = fmaxf(l, dpp<XOR2>(l)); l = fmaxf(l, dpp<XOR7>(l));
    return __builtin_amdgcn_readfirstlane((unsigned)l);
}
__device__ __forceinline__ void observe_ap(rsrc_t rs, int lane, int cg, unsigned tag) {
    v2u o[1] = {v2u{0u, lane < 32 ? ~tag : tag}};
    gather<1, 1>(rs, lane * 128, WS_AP + cg * 4096, 0, tag, o);
}
#define STAMP(i) do { } while (0)

#define LD_REPORT \
    "s_getreg_b32 s92, hwreg(HW_REG_IB_STS)\n\t" \
    "s_and_b32 s93, s92, 15\n\t" \
    "s_bfe_u32 s92, s92, 0x20016\n\t" \
    "s_lshl4_add_u32 s93, s92, s93\n\t" \
    "s_sub_u32 s92, %[iss], s93\n\t" \
    "v_mov_b32 %[vt], s92\n\t"
#define LD_WRITE \
    "s_mov_b64 exec, 1\n\t" \
    "ds_write_b32 %[va], %[vt]\n\t" \
    "s_mov_b64 exec, -1\n\t"
struct Ld { unsigned issued, tnext, pace, va, sh, w0; };
template <bool FLIP, int POL = 0> __device__ __forceinline__ void ld_stream(Ld& s, rsrc_t rs, unsigned (&o)[6], unsigned soff, unsigned sstep, unsigned dst, int ng, unsigned wrap = ~0u) {
    unsigned vt;
    const unsigned end = __builtin_amdgcn_readfirstlane(s.issued + 6 * ng);
    soff = __builtin_amdgcn_readfirstlane(soff); dst = __builtin_amdgcn_readfirstlane(dst);
    s.issued = __builtin_amdgcn_readfirstlane(s.issued); s.tnext = __builtin_amdgcn_readfirstlane(s.tnext); s.pace = __builtin_amdgcn_readfirstlane(s.pace);
    asm volatile(
        "s_memrealtime s[90:91]\n\t"
        "1:\n\t"
        LD_REPORT
        "s_waitcnt lgkmcnt(0)\n\t"
        "s_lshl_b32 s92, s90, 2\n\t"
        "s_memrealtime s[90:91]\n\t"
        LD_WRITE
        "s_sub_u32 s92, s92, %[tn]\n\t"
        "s_cmp_lt_i32 s92, 0\n\t"
        "s_cbranch_scc1 1b\n\t"
        "s_lshr_b32 s92, s92, %[sh]\n\t"
        "s_add_u32 s92, s92, %[w0]\n\t"
        "s_min_u32 s92, s92, %[wm]\n\t"
        "s_cmp_gt_u32 s93, s92\n\t"
        "s_cbranch_scc1 1b\n\t"
        "s_mov_b32 m0, %[dst]\n\t"
        "s_add_u32 %[tn], %[tn], %[pace]\n\t"
        ".if %c[pol]\n\tbuffer_load_dwordx4 %[o0], %[rs], %[so] offen lds\n\t.else\n\tbuffer_load_dwordx4 %[o0], %[rs], %[so] offen nt lds\n\t.endif\n\t"
        ".if %c[pol]\n\tbuffer_load_dwordx4 %[o1], %[rs], %[so] offen offset:1024 lds\n\t.else\n\tbuffer_load_dwordx4 %[o1], %[rs], %[so] offen offset:1024 nt lds\n\t.endif\n\t"
        ".if %c[pol]\n\tbuffer_load_dwordx4 %[o2], %[rs], %[so] offen offset:2048 lds\n\t.else\n\tbuffer_load_dwordx4 %[o2], %[rs], %[so] offen offset:2048 nt lds\n\t.endif\n\t"
        "s_add_u32 m0, %[dst], 3072\n\t"
        "s_add_u32 %[iss], %[iss], 6\n\t"
        ".if %c[pol]\n\tbuffer_load_dwordx4 %[o3], %[rs], %[so] offen lds\n\t.else\n\tbuffer_load_dwordx4 %[o3], %[rs], %[so] offen nt lds\n\t.endif\n\t"
        ".if %c[pol]\n\tbuffer_load_dwordx4 %[o4], %[rs], %[so] offen offset:1024 lds\n\t.else\n\tbuffer_load_dwordx4 %[o4], %[rs], %[so] offen offset:1024 nt lds\n\t.endif\n\t"
        ".if %c[pol]\n\tbuffer_load_dwordx4 %[o5], %[rs], %[so] offen offset:2048 lds\n\t.else\n\tbuffer_load_dwordx4 %[o5], %[rs], %[so] offen offset:2048 nt lds\n\t.endif\n\t"
        "s_add_u32 %[dst], %[dst], 6144\n\t"
        "s_add_u32 %[so], %[so], %[ss]\n\t"
        "s_cmp_eq_u32 %[so], %[wrap]\n\t"
        "s_cselect_b32 %[so], 0, %[so]\n\t"
        ".if %c[flip]\n\t"
        "v_xor_b32 %[o0], 0x80, %[o0]\n\tv_xor_b32 %[o1], 0x80, %[o1]\n\tv_xor_b32 %[o2], 0x80, %[o2]\n\t"
        "v_xor_b32 %[o3], 0x80, %[o3]\n\tv_xor_b32 %[o4], 0x80, %[o4]\n\tv_xor_b32 %[o5], 0x80, %[o5]\n\t"
        ".endif\n\t"
        "s_cmp_lg_u32 %[iss], %[end]\n\t"
        "s_cbranch_scc1 1b\n\t"
        "s_waitcnt lgkmcnt(0)"
        : [iss] "+&s"(s.issued), [tn] "+&s"(s.tnext), [dst] "+&s"(dst), [so] "+&s"(soff), [vt] "=&v"(vt),
          [o0] "+&v"(o[0]), [o1] "+&v"(o[1]), [o2] "+&v"(o[2]), [o3] "+&v"(o[3]), [o4] "+&v"(o[4]), [o5] "+&v"(o[5])
        : [rs] "s"(rs), [ss] "s"(sstep), [end] "s"(end), [pace] "s"(s.pace), [va] "v"(s.va), [wrap] "s"(wrap), [wm] "n"(WMAX - 6), [sh] "s"(s.sh), [w0] "s"(s.w0), [flip] "n"(FLIP ? 1 : 0), [pol] "n"(POL)
        : "s90", "s91", "s92", "s93", "scc", "memory");
    s.issued = end;
    s.tnext = __builtin_amdgcn_readfirstlane(s.tnext);
}
__device__ __forceinline__ void ld_gate(Ld& s, int c, unsigned n) {
    unsigned vt, vg, cap = SPIN_LDS;
    const unsigned ga = L_CNT + 4 * c;
    asm volatile(
        "1:\n\t"
        "ds_read_b32 %[vg], %[ga]\n\t"
        LD_REPORT
        LD_WRITE
        "s_sub_u32 %[cap], %[cap], 1\n\t"
        "s_cbranch_scc1 2f\n\t"
        "s_waitcnt lgkmcnt(1)\n\t"
        "v_readfirstlane_b32 s92, %[vg]\n\t"
        "s_cmp_lt_u32 s92, %[n]\n\t"
        "s_cbranch_scc1 1b\n\t"
        "2:\n\t"
        "s_waitcnt lgkmcnt(0)"
        : [vt] "=&v"(vt), [vg] "=&v"(vg), [cap] "+&s"(cap)
        : [iss] "s"(s.issued), [va] "v"(s.va), [ga] "v"(ga), [n] "s"(n)
        : "s92", "s93", "scc", "memory");
}
__device__ __forceinline__ void ld_drain(Ld& s) {
    unsigned vt;
    asm volatile(
        "1:\n\t"
        LD_REPORT
        LD_WRITE
        "s_cmp_lg_u32 s93, 0\n\t"
        "s_cbranch_scc1 1b\n\t"
        : [vt] "=&v"(vt) : [iss] "s"(s.issued), [va] "v"(s.va) : "s92", "s93", "scc", "memory");
}

__global__ void __launch_bounds__(640) k_block(const u16* __restrict__ x, const u16* __restrict__ kc, const u16* __restrict__ vc,
                                               const u16* __restrict__ wq, const u16* __restrict__ wo, const u16* __restrict__ wg,
                                               const u16* __restrict__ wu, const u16* __restrict__ wd, u16* __restrict__ out,
                                               unsigned char* ws KNARG) {
    extern __shared__ __attribute__((aligned(16))) unsigned char lds_raw[];
    LAS unsigned char* L = (LAS unsigned char*)lds_raw;
    unsigned ka0, ka1;
    asm volatile("s_load_dword %0, %2, 0x0\n\ts_load_dword %1, %2, 0x40" : "=&s"(ka0), "=&s"(ka1) : "s"(__builtin_amdgcn_kernarg_segment_ptr()));
#define CODEPF 19400
    if (__builtin_expect((unsigned)__builtin_amdgcn_readfirstlane(threadIdx.x >> 6) == 8u, 1)) {
        const rsrc_t rc = mkrs((const void*)(__builtin_amdgcn_s_getpc() & ~255ull), CODEPF);
        asm volatile("s_mov_b32 m0, %0\n\ts_nop 0\n\tbuffer_load_dwordx4 %1, %2, %3 offen lds" :: "s"(L_JUNK), "v"((threadIdx.x & 63) * 16), "s"(rc), "s"((blockIdx.x >> 3) * 1024) : "memory");
    }
    if (threadIdx.x < 20) ((LAS unsigned*)(L + L_CNT))[threadIdx.x] = 0u;
    const int b = blockIdx.x, xcd = b & 7, idx = b >> 3;
    const int h = xcd * 4 + (idx >> 3), j = idx & 7;
    const int tid = threadIdx.x, lane = tid & 63, wave = __builtin_amdgcn_readfirstlane(tid >> 6);
    const int g = lane >> 4, tq = (lane & 15) >> 2, tp = lane & 3, i16 = lane & 15;
    const bool qcu = j >= 2;
    const int qr = (j + 3) >> 3, qc = j - 2 - 3 * qr;
    const int gi = (idx >> 3) * 2 + j;
    const int gcu = (!qcu && gi < 6) ? xcd * 6 + gi : -1;
    const bool pre = qcu || gcu >= 0;
    const unsigned d0 = g * 256 + (i16 ^ (g << 1)) * 16;
    unsigned q0 = qcu ? (lane >> 3) * 12288 + ((lane & 7) ^ (g & 1) * 2) * 16 : d0, q1 = qcu ? (q0 ^ 64) + 98304 - 1024 : d0;
    unsigned q2 = qcu ? q0 + 2 * (98304 - 1024) : d0 ^ 128, q3 = qcu ? q1 + 2 * (98304 - 1024) : d0 ^ 128;
    unsigned qs = qcu ? 393216 : 4096, qb = qcu ? (320 * qr * (H * DH) + h * DH + 64 * qc) * 2 : 32 * gcu * D * 2;
    unsigned qn = (wave == 4 && pre) ? 10 : 0;
    qs = __builtin_amdgcn_readfirstlane(qs); qb = __builtin_amdgcn_readfirstlane(qb); qn = __builtin_amdgcn_readfirstlane(qn);
    asm volatile("" : "+v"(q0), "+v"(q1), "+v"(q2), "+v"(q3));
    asm volatile("" : "+s"(qs), "+s"(qb), "+s"(qn));
    SB();
    asm volatile("s_waitcnt lgkmcnt(0)\n\ts_barrier" :: "s"(ka0), "s"(ka1), "s"(x), "s"(kc), "s"(vc), "s"(wq), "s"(wo), "s"(wg), "s"(wu), "s"(wd), "s"(out), "s"(ws) : "memory");
    const u64 t_start = __builtin_amdgcn_s_memrealtime();
    if (__builtin_expect(qn != 0, 1)) {
        const rsrc_t rq = mkrs(qcu ? wq : wd, qcu ? D * H * DH * 2 : F * D * 2);
#pragma unroll 1
        for (unsigned n = 0; n < 10; ++n, qb += qs, nap(KN(20)))
            asm volatile("s_mov_b32 m0, %0\n\ts_nop 0\n\tbuffer_load_dwordx4 %1, %5, %6 offen nt lds\n\tbuffer_load_dwordx4 %2, %5, %6 offen offset:1024 nt lds\n\t"
                         "buffer_load_dwordx4 %3, %5, %6 offen offset:2048 nt lds\n\tbuffer_load_dwordx4 %4, %5, %6 offen offset:3072 nt lds"
                         :: "s"(L_W + n * 4096), "v"(q0), "v"(q1), "v"(q2), "v"(q3), "s"(rq), "s"(qb) : "memory");
        waitvm<0>();
        lds_signal(C_WQ);
    }
    unsigned tag, xp[5];
    asm volatile("" : "=v"(xp[0]), "=v"(xp[1]), "=v"(xp[2]), "=v"(xp[3]), "=v"(xp[4]), "=v"(tag));
    if (__builtin_expect(wave == 9, 1)) {
        const unsigned xo = lane * 4, zo = 0;
        asm volatile("global_load_dword %0, %6, %7\n\tglobal_load_dword %1, %6, %7 offset:256\n\tglobal_load_dword %2, %6, %7 offset:512\n\t"
                     "global_load_dword %3, %6, %7 offset:768\n\tglobal_load_dword %4, %6, %7 offset:1024\n\tglobal_load_dword %5, %8, %9 sc1"
                     : "+v"(xp[0]), "+v"(xp[1]), "+v"(xp[2]), "+v"(xp[3]), "+v"(xp[4]), "+v"(tag) : "v"(xo), "s"(x), "v"(zo), "s"(ws) : "memory");
    }
    const rsrc_t rws = mkrs(ws, WS_END);
    if (__builtin_expect(wave == 7 && idx == 0, 1)) __builtin_amdgcn_raw_buffer_store_b64(v2u{(unsigned)t_start, (unsigned)(t_start >> 32)}, rws, lane == 0 ? WS_T0 + xcd * 128 : OOB, 0, 16);
    const bool wocu = j < 5;
    const int oi = (idx >> 3) * 3 + (j - 2), oc = (idx >> 3) * 3 + (j - 5);
    const int ocu = (wocu && qcu && oi < 5) ? xcd * 5 + oi : -1;
    const int n1 = __builtin_amdgcn_readfirstlane(!pre ? N1G + 4 : wocu ? N1G : 0);
    const int pv0 = P_K + 6 * n1;
    const f32x4 z4 = {0.f, 0.f, 0.f, 0.f};
    auto sel4 = [&](float a0, float a1, float a2, float a3) {
        const float lo = (lane & 16) ? a1 : a0, hi = (lane & 16) ? a3 : a2;
        return (lane & 32) ? hi : lo;
    };
    auto make_h = [&](int a, unsigned xres) {
        unsigned ha = L_O + (8 * a + (lane & 7)) * 4, ho = (lane < 8 && oc < 8) ? WS_HB + oc * 4096 + (40 * xcd + 8 * a + lane) * 8 : OOB;
        PIN(ha); PIN(ho);
        lds_wait<true>(L, C_OWN, 4);
        delay(D2);
        lds_wait<true>(L, C_PROG, P_WO);
        const unsigned hp = reduce_cols<4>(rws, lane * 16, WS_AP + (5 * xcd + a) * 4096, tag, xres);
        put1(rws, ho, tag, hp);
        *(LAS unsigned*)(L + ha) = hp;
        lds_signal(C_O);
    };

    if (__builtin_expect(wave == 0, 1)) {
        __builtin_amdgcn_s_setprio(3);
        STAMP(14);
        unsigned ko[6], vo[6], po[6];
#pragma unroll
        for (int c = 0; c < 3; ++c) {
            const unsigned P = 64 * c + lane, r = P / 24, ch = P - r * 24;
            ko[c] = r * 384 + (ch ^ (r >> 1)) * 16 - c * 1024; ko[c + 3] = (ko[c] ^ 64) + 3072;
        }
        const size_t kvoff = (KN7 & 32) ? ((size_t)idx * SK + (SK - W) + xcd * 256) * DH : ((size_t)h * SK + (SK - W) + j * 256) * DH;
        Ld s;
        s.issued = 0; s.va = L_CNT + 4 * C_PROG; s.w0 = KN(22); s.sh = KN(23);
        nap(KN(21));
        MEMBAR();
        const unsigned late = (KN7 & 64) ? 0u : lateness(rws, lane, t_start);
        lds_set(C_LATE, late);
        s.pace = ((unsigned)(TE - (pre ? TAQ : TA0) - late) * (!pre ? 262144 / (35 + N1G) : wocu ? 262144 / (31 + N1G) : 262144 / 31)) >> 16;
        s.tnext = ((unsigned)t_start + (pre ? TAQ : TA0)) * 4;
        const unsigned rot = 0;
        ld_stream<false, POLK>(s, mkrs(kc + kvoff, 98304), ko, rot, 6144, L_V, 16, 98304);
        STAMP(4);
#pragma unroll
        for (int c = 0; c < 3; ++c) {
            const unsigned P = 64 * c + lane, r = P / 24, ch = P - r * 24;
            vo[c] = r * 384 + (ch ^ (r & 2u)) * 16 - c * 1024; vo[c + 3] = (vo[c] ^ 64) + 3072;
            const unsigned wb = (i16 ^ (g << 1)) * 16;
            const unsigned rst = wocu ? 1280 : 3072;
            po[c] = (4 * c + g) * rst + (wb ^ ((c >> 1) << 7)) - c * 1024;
            po[c + 3] = (4 * c + 12 + g) * rst + (wb ^ ((((c + 3) >> 1) & 1) << 7)) - c * 1024;
        }
        const rsrc_t rv = mkrs(vc + kvoff, 98304);
        const rsrc_t ro = mkrs(wo + (size_t)(h * DH) * D + j * 128, 191 * 1280 + 256);
        ld_gate(s, C_QP, __builtin_amdgcn_readfirstlane(qcu ? 4 : pre ? 8 : 0));
        if (n1) ld_stream<true, POLW>(s, ro, po, 0, 30720, L_W, n1);
        ld_gate(s, C_KA, 4);
        ld_stream<false, POLV>(s, rv, vo, rot, 6144, L_V, 8, 98304);
        ld_gate(s, C_KB, 4);
        ld_stream<false, POLV>(s, rv, vo, rot >= 49152 ? rot - 49152 : rot + 49152, 6144, L_V + 49152, 8, 98304);
        STAMP(6);
        s.pace = KN(15); s.w0 = KN(17);
        if (wocu) {
            if (n1 < 8) ld_stream<true, POLW>(s, ro, po, n1 * 30720, 30720, L_W + n1 * 6144, 8 - n1);
        } else {
            const rsrc_t r0 = mkrs(wg + (size_t)(80 * xcd) * F + 128 * oc, 79 * 3072 + 256), r1 = mkrs(wu + (size_t)(80 * xcd) * F + 128 * oc, 79 * 3072 + 256);
            s.tnext += FDELAY * 4; s.pace = PACE2; s.w0 = KN(16);
            ld_stream<true, POLW>(s, r0, po, 0, 73728, L_W, 4);
            ld_stream<true, POLW>(s, r1, po, 0, 73728, L_W + 24576, 4);
        }
        ld_drain(s);
        if (!wocu) STAMP(17); else STAMP(9);
        tag = __builtin_amdgcn_readfirstlane(*(LAS unsigned*)(L + L_TAG));
        if (gcu >= 0) {
            unsigned ha = L_HH + (lane & 31) * 2;
            PIN(ha);
            lds_wait<true>(L, C_GO, 1);
            STAMP(18);
            delay(D3);
            float ga0, ga1;
            sum_pairs<4, PUMP>(rws, lane * 16, WS_GP + gcu * 4096, tag, ga0, ga1);
            f32x4 st = *(volatile LAS f32x4*)(L + L_ST);
            float cs = *(volatile LAS float*)(L + L_LN + lane * 4);
            STAMP(12);
            auto sw0 = __builtin_amdgcn_permlane32_swap(__float_as_uint(ga0), __float_as_uint(ga1), false, false);
            float sv = fv(sw0[0]) + fv(sw0[1]);
            PIN(sv);
#pragma unroll 1
            for (unsigned sp = 0; sp < SPIN_LDS && __builtin_amdgcn_readfirstlane(__float_as_uint(st[2]) & __float_as_uint(st[3])) == 0u; ++sp) {
                st = *(volatile LAS f32x4*)(L + L_ST);
                cs = *(volatile LAS float*)(L + L_LN + lane * 4);
            }
            const float v = rbf((sv - st[0] * cs) * st[1]);
            auto sw = __builtin_amdgcn_permlane32_swap(__float_as_uint(v), __float_as_uint(v), false, false);
            const float sg = rbf(__builtin_amdgcn_rcpf(1.0f + __expf(-fv(sw[0]))));
            *(LAS u16*)(L + ha) = (u16)bfbits(sg * fv(sw[1]));
            lds_signal(C_HH);
        } else if (ocu >= 0) {
            const rsrc_t rout = mkrs(out, 1280);
            unsigned oo = lane < 8 ? (8 * ocu + lane) * 4 : OOB;
            PIN(oo);
            lds_wait<true>(L, C_O, 4);
            v2u hres[1] = {v2u{0u, ~tag}};
            delay(DOBS + D3);
            gather<1, 4>(rws, (lane & 7) * 8, WS_HB + xcd * 4096 + 64 * ocu, 0, tag, hres);
            delay(D4);
            STAMP(22);
            const unsigned ov = reduce_cols<6>(rws, lane * 16, WS_DP + ocu * 6144, tag, hres[0][0]);
            __builtin_amdgcn_raw_buffer_store_b32(ov, rout, oo, 0, 0);
            if (ocu == 0 && lane == 0) __hip_atomic_store((gu32*)(ws + WS_EPOCH), tag, RLX_AGENT);
            STAMP(16);
        }
    } else if (wave == 9) {
        asm volatile("s_waitcnt vmcnt(0)" : "+v"(xp[0]), "+v"(xp[1]), "+v"(xp[2]), "+v"(xp[3]), "+v"(xp[4]), "+v"(tag) :: "memory");
        float mb, rden;
        ln_stats(xp, mb, rden);
#pragma unroll
        for (int i = 0; i < 5; ++i) *(LAS unsigned*)(L + L_LN + (lane + 64 * i) * 4) = ln_pair(xp[i], mb, rden);
        tag = __builtin_amdgcn_readfirstlane(tag) + 1u;
        if (lane == 0) *(LAS unsigned*)(L + L_TAG) = tag;
        lds_signal(C_X);
        STAMP(23);
        {
            until(t_start + TQ);
            v4u qp[3];
#pragma unroll
            for (int k = 0; k < 3; ++k) qp[k] = v4u{0u, ~tag, 0u, ~tag};
            if (!(KN7 & 1)) gather<1, 8>(rws, lane * 16, WS_QP + h * 3072, 1024, tag, qp);
#pragma unroll
            for (int k = 0; k < 3; ++k) *(LAS unsigned*)(L + L_Q + k * 128 + (lane & 31) * 4) = pk2(sw32_sum(fv(qp[k][0])), sw32_sum(fv(qp[k][2])));
            lds_signal(C_Q);
            STAMP(3);
        }
        lds_wait<true>(L, C_P, 4);
        const f32x4 mx = *(LAS f32x4*)(L + L_MX), ls = *(LAS f32x4*)(L + L_MX + 16);
        const float m = fmaxf(fmaxf(mx[0], mx[1]), fmaxf(mx[2], mx[3]));
        const float l = (ls[0] + ls[1]) + (ls[2] + ls[3]);
        if (!(KN7 & 8)) put2(rws, lane == 0 ? WS_ST + (h * 8 + j) * 128 : OOB, tag, m, l);
        if (wocu) {
            until(t_start - __builtin_amdgcn_readfirstlane(*(volatile LAS unsigned*)(L + L_CNT + 4 * C_LATE)) + TS);
            const unsigned tt = lane < 8 ? ~tag : tag;
            v4u st[1] = {v4u{0u, tt, 0u, tt}};
            if (!(KN7 & 4)) gather<1, 8>(rws, lane * 128, WS_ST + h * 1024, 0, tag, st);
            const float ms = fv(st[0][0]);
            float M = fmaxf(ms, dpp<XOR1>(ms)); M = fmaxf(M, dpp<XOR2>(M)); M = fmaxf(M, dpp<XOR7>(M));
            const float e = __expf(ms - M);
            float Ls = e * fv(st[0][2]);
            Ls += dpp<XOR1>(Ls); Ls += dpp<XOR2>(Ls); Ls += dpp<XOR7>(Ls);
            if (lane < 8) *(LAS float*)(L + L_WGT + lane * 4) = e * __builtin_amdgcn_rcpf(Ls);
            lds_signal(C_W);
            STAMP(19);
            if (gcu >= 0) {
                lds_wait<true>(L, C_O, 4);
                delay(DOBS);
                if (KN7 & 256) observe_ap(rws, lane, gi * 6, tag);
                lds_signal(C_GO);
                delay(D3 - 22);
                v2u hv[5];
#pragma unroll
                for (int i = 0; i < 5; ++i) hv[i] = v2u{0u, ~tag};
                gather<1, 0>(rws, lane * 8, WS_HB + xcd * 4096, 512, tag, hv);
#pragma unroll
                for (int i = 0; i < 5; ++i) xp[i] = hv[i][0];
                ln_stats(xp, mb, rden);
                if (lane == 0) { *(LAS v2u*)(L + L_ST) = v2u{__float_as_uint(mb), __float_as_uint(rden)}; *(LAS float*)(L + L_ST + 8) = 1.0f; }
                STAMP(15);
            }
        } else {
            make_h(4, ((const unsigned*)x)[40 * xcd + 32 + (lane & 7)]);
        }
    } else {
        bf16x8 dw[5];
        if (gcu >= 0) {
            lds_wait<true>(L, C_WQ, 1);
#pragma unroll
            for (int t = 0; t < 5; ++t) {
                const int tt = 5 * (wave - 1) + t;
                unsigned ad[2];
#pragma unroll
                for (int u = 0; u < 2; ++u) {
                    const int r = 8 * g + 4 * u + tq, B = 5 * r + (tt >> 3);
                    ad[u] = L_W + r * 1280 + (tt ^ ((B & 3) | ((B >> 1) & 4))) * 32 + tp * 8;
                }
                dw[t] = cat8(trd(L, ad[0]), trd(L, ad[1]));
            }
            lds_signal(C_QP);
        }
        if (wave < 5) {
            const int a = wave - 1;
            unsigned xres = 0;
            if (!wocu) xres = ((const unsigned*)x)[40 * xcd + 8 * a + (lane & 7)];
            if ((KN7 & 512) && a == 2) {
#pragma unroll 1
                for (int i = 0; i < KN(18); ++i)
                    asm volatile("s_mov_b32 m0, %0\n\ts_nop 0\n\tbuffer_load_dwordx4 %1, %2, %3 offen lds" :: "s"(L_JUNK), "v"(lane * 16), "s"(rws), "s"(WS_END - (b * KN(18) + i + 1) * 1024) : "memory");
            }
            lds_wait<true>(L, C_X, 1);
            tag = __builtin_amdgcn_readfirstlane(*(LAS unsigned*)(L + L_TAG));
            if (qcu) {
                bf16x8 xa[10];
#pragma unroll
                for (int s = 0; s < 10; ++s) xa[s] = *(LAS bf16x8*)(L + L_LN + (320 * qr + 32 * s + 8 * g) * 2);
                lds_wait<true>(L, C_WQ, 1);
                if (a == 0) STAMP(1);
                const unsigned ca = L_W + (8 * g + tq) * 128 + ((a ^ ((tq >> 1) | ((g & 1) << 1))) * 2 + (tp >> 1)) * 16 + (tp & 1) * 8;
                f32x4 q0 = z4, q1 = z4;
#pragma unroll
                for (int s = 0; s < 10; s += 2) {
                    q0 = mfma32(xa[s], cat8(trd(L, ca + s * 4096), trd(L, ca + s * 4096 + 512)), q0);
                    q1 = mfma32(xa[s + 1], cat8(trd(L, ca + s * 4096 + 4096), trd(L, ca + s * 4096 + 4608)), q1);
                }
                lds_signal(C_QP);
                if (!(KN7 & 2)) put1(rws, lane < 16 ? WS_QP + h * 3072 + qc * 1024 + qr * 512 + (16 * a + lane) * 8 : OOB, tag, __float_as_uint(q0[0] + q1[0]));
                if (a == 0) STAMP(2);
            }
            if (wocu) {
                unsigned oa = L_O + (lane < 48 ? 48 * a + lane : 192 + lane) * 2;
                PIN(oa);
                lds_wait<true>(L, C_W, 1);
                f32x4 w0 = *(LAS f32x4*)(L + L_WGT), w1 = *(LAS f32x4*)(L + L_WGT + 16);
                PIN(w0); PIN(w1);
                lds_wait<true>(L, C_PROG, pv0 + 84);
                unsigned own;
                lds_wait_rd<false>(L, C_OWN, 4, [&] { own = *(volatile LAS unsigned*)(L + L_OWN + (64 * a + lane) * 4); });
                const float o = combine_o<PUMP>(rws, lane * 8, WS_OP + (h * 8 * 192 + 48 * a) * 8, tag, j, own, w0, w1, D1);
                *(LAS u16*)(L + oa) = (u16)bfbits(o);
                lds_signal(C_O);
                if (a == 0) STAMP(8);
                if (gcu >= 0 && a == 0) {
                    lds_wait<true>(L, C_GO, 1);
                    float c0, c1;
                    sum_pairs<4, 0>(rws, lane * 16, WS_CP + gcu * 4096, tag, c0, c1);
                    auto sw = __builtin_amdgcn_permlane32_swap(__float_as_uint(c0), __float_as_uint(c1), false, false);
                    *(LAS float*)(L + L_LN + lane * 4) = fv(sw[0]) + fv(sw[1]);
                    MEMBAR();
                    if (lane == 0) *(LAS float*)(L + L_ST + 12) = 1.0f;
                }
            } else {
                make_h(a, xres);
                if (a == 0) STAMP(11);
            }
        } else {
            const int kw = wave - 5;
            bf16x8 kf[4][6];
            {
                const unsigned kb = L_V + (16 * kw + i16) * 384;
#pragma unroll
                for (int n = 0; n < 4; ++n) {
                    lds_wait<true>(L, C_PROG, 12 * ((kw >> 1) + 2 * n + 1));
#pragma unroll
                    for (int s = 0; s < 6; ++s) kf[n][s] = *(LAS bf16x8*)(L + kb + n * 24576 + ((4 * s + g) ^ (i16 >> 1)) * 16);
                    if (n & 1) lds_signal(n == 1 ? C_KA : C_KB);
                }
            }
            lds_wait<true>(L, C_Q, 1);
            tag = __builtin_amdgcn_readfirstlane(*(LAS unsigned*)(L + L_TAG));
            f32x4 sa[4] = {z4, z4, z4, z4};
#pragma unroll
            for (int s = 0; s < 6; ++s) {
                const bf16x8 qf = *(LAS bf16x8*)(L + L_Q + (32 * s + 8 * g) * 2);
#pragma unroll
                for (int n = 0; n < 4; ++n) sa[n] = mfma32(qf, kf[n][s], sa[n]);
            }
            const float raw = sel4(sa[0][0], sa[1][0], sa[2][0], sa[3][0]);
            const float s = rbf(rbf(raw) * 0.07216878364870322f);
            const float mw = wave_max(s);
            if (lane == 0) *(LAS float*)(L + L_MX + kw * 4) = mw;
            lds_signal(C_MAX);
            lds_wait<false>(L, C_MAX, 4);
            const f32x4 mx = *(LAS f32x4*)(L + L_MX);
            const float m = fmaxf(fmaxf(mx[0], mx[1]), fmaxf(mx[2], mx[3]));
            const float e = __expf(s - m);
            *(LAS u16*)(L + L_P + (16 * (kw + 4 * g) + i16) * 2) = (u16)bfbits(e);
            const float lw = wave_sum(e);
            if (lane == 0) *(LAS float*)(L + L_MX + 16 + kw * 4) = lw;
            lds_signal(C_P);
            lds_wait<false>(L, C_P, 4);
            if (kw == 0) STAMP(5);
            bf16x8 pf[8];
#pragma unroll
            for (int s8 = 0; s8 < 8; ++s8) pf[s8] = *(LAS bf16x8*)(L + L_P + (32 * s8 + 8 * g) * 2);
            const int fvs = (tq >> 1) | ((g & 1) << 1);
            unsigned vb[3], vl[3];
#pragma unroll
            for (int t = 0; t < 3; ++t) { vb[t] = (8 * g + tq) * 384 + (((3 * kw + t) ^ fvs) * 2 + (tp >> 1)) * 16 + (tp & 1) * 8; vl[t] = L_V + 6 * 12288 + vb[t]; PIN(vl[t]); }
            unsigned oo = lane < 48 ? WS_OP + ((h * 8 + j) * 192 + 48 * kw + lane) * 8 : OOB, ow = L_OWN + (64 * kw + lane) * 4;
            PIN(oo); PIN(ow);
            f32x4 oa[3] = {z4, z4, z4};
            lds_wait<true>(L, C_PROG, pv0 + 72);
#pragma unroll
            for (int s8 = 0; s8 < 6; ++s8)
#pragma unroll
                for (int t = 0; t < 3; ++t) oa[t] = mfma32(pf[s8], cat8(trd(L, L_V + vb[t] + s8 * 12288), trd(L, L_V + vb[t] + s8 * 12288 + 1536)), oa[t]);
            {
                s16x4 vf[6];
                spin_tr<0>(C_PROG, pv0 + 84, vl, vf);
#pragma unroll
                for (int t = 0; t < 3; ++t) oa[t] = mfma32(pf[6], cat8(vf[2 * t], vf[2 * t + 1]), oa[t]);
                spin_tr<12288>(C_PROG, pv0 + 96, vl, vf);
                if (kw == 0) STAMP(20);
#pragma unroll
                for (int t = 0; t < 3; ++t) oa[t] = mfma32(pf[7], cat8(vf[2 * t], vf[2 * t + 1]), oa[t]);
            }
            const float op = sel4(oa[0][0], oa[1][0], oa[2][0], oa[2][0]);
            put1(rws, oo, tag, __float_as_uint(op));
            *(LAS float*)(L + ow) = op;
            lds_signal(C_OWN);
            if (kw == 0) STAMP(7);
            if (wocu) {
                unsigned ao = lane < 32 ? WS_AP + (((8 * j + 2 * kw + g) * 32 + h) * 16 + i16) * 8 : OOB, al = L_O + 16 * g;
                PIN(ao); PIN(al);
                lds_wait<false>(L, C_PROG, P_WO);
                bf16x8 bw[2][6], af[6];
                const int f3 = tq | ((g & 1) << 2);
#pragma unroll
                for (int t = 0; t < 2; ++t) {
                    const unsigned cb = L_W + (8 * g + tq) * 256 + (((2 * kw + t) ^ f3) * 2 + (tp >> 1)) * 16 + (tp & 1) * 8;
#pragma unroll
                    for (int s6 = 0; s6 < 6; ++s6) bw[t][s6] = cat8(trd(L, cb + s6 * 8192), trd(L, cb + s6 * 8192 + 1024));
                }
                spin_rd(C_O, 4, al, af);
                f32x4 a0 = z4, a1 = z4;
#pragma unroll
                for (int s6 = 0; s6 < 6; ++s6) { a0 = mfma32(af[s6], bw[0][s6], a0); a1 = mfma32(af[s6], bw[1][s6], a1); }
                put1(rws, ao, tag, __float_as_uint((lane & 16) ? a1[0] : a0[0]));
                if (kw == 0) STAMP(10);
            } else {
                lds_wait<true>(L, C_PROG, P_WO);
                bf16x8 bw[2][2][3], af[3];
                const int f3 = tq | ((g & 1) << 2);
#pragma unroll
                for (int mt = 0; mt < 2; ++mt)
#pragma unroll
                    for (int t = 0; t < 2; ++t) {
                        const unsigned cb = L_W + mt * 24576 + (8 * g + tq) * 256 + ((2 * kw + t) ^ f3) * 32 + tp * 8;
#pragma unroll
                        for (int s3 = 0; s3 < 3; ++s3) bw[mt][t][s3] = cat8(trd(L, cb + s3 * 8192), trd(L, cb + s3 * 8192 + 1024));
                    }
                unsigned go = lane < 32 ? WS_CP + ((4 * oc + kw) * 8 + xcd) * 512 + lane * 16 : OOB, g2 = go + (WS_GP - WS_CP), al = L_O + 16 * g;
                PIN(al); PIN(g2);
                auto tile4 = [&](unsigned off) {
                    f32x4 d[2][2] = {{z4, z4}, {z4, z4}};
#pragma unroll
                    for (int s3 = 0; s3 < 3; ++s3)
#pragma unroll
                        for (int mt = 0; mt < 2; ++mt)
#pragma unroll
                            for (int t = 0; t < 2; ++t) d[mt][t] = mfma32(af[s3], bw[mt][t][s3], d[mt][t]);
                    SB();
                    put2(rws, off, tag, (lane & 16) ? d[0][1][0] : d[0][0][0], (lane & 16) ? d[1][1][0] : d[1][0][0]);
                };
                const v4u ones = {0x3F803F80u, 0x3F803F80u, 0x3F803F80u, 0x3F803F80u};
                af[0] = af[1] = af[2] = asb(ones);
                tile4(go);
                *(LAS unsigned*)(L + L_O + 160 + (lane & 7) * 4) = 0u;
                spin_rd(C_O, 5, al, af);
                tile4(g2);
                if (kw == 0) STAMP(21);
            }
        }
        if (gcu >= 0) {
            const int w8 = wave - 1;
            unsigned d0 = WS_DP + (((5 * w8 + g) * 48 + gcu) * 16 + i16) * 8, d1 = lane < 16 ? WS_DP + (((5 * w8 + 4) * 48 + gcu) * 16 + i16) * 8 : OOB, al = L_HH + 16 * g;
            PIN(d0); PIN(d1); PIN(al);
            bf16x8 af[1];
            lds_wait<true>(L, C_GO, 1);
            spin_rd(C_HH, 1, al, af);
            f32x4 d[5];
#pragma unroll
            for (int t = 0; t < 5; ++t) d[t] = mfma32(af[0], dw[t], z4);
            SB();
            put1(rws, d0, tag, __float_as_uint(sel4(d[0][0], d[1][0], d[2][0], d[3][0])));
            put1(rws, d1, tag, __float_as_uint(d[4][0]));
            if (w8 == 0) STAMP(13);
        }
    }
}

extern "C" void kernel_launch(void* const* d_in, const int* in_sizes, int n_in, void* d_out, int out_size, void* d_ws, size_t ws_size, hipStream_t stream) {
    (void)hipFuncSetAttribute((const void*)k_block, hipFuncAttributeMaxDynamicSharedMemorySize, L_END);
    k_block<<<256, 640, L_END, stream>>>((const u16*)d_in[0], (const u16*)d_in[1], (const u16*)d_in[2], (const u16*)d_in[3], (const u16*)d_in[4],
                                         (const u16*)d_in[6], (const u16*)d_in[7], (const u16*)d_in[8], (u16*)d_out, (unsigned char*)d_ws
        );
}
```

```cpp
#include <hip/hip_runtime.h>
#include <stdint.h>

typedef unsigned short u16;
typedef unsigned long long u64;
typedef unsigned v4u __attribute__((ext_vector_type(4)));
typedef unsigned v2u __attribute__((ext_vector_type(2)));
typedef float f32x4 __attribute__((ext_vector_type(4)));
typedef short s16x4 __attribute__((ext_vector_type(4)));
typedef __bf16 bf16x8 __attribute__((ext_vector_type(8)));
typedef __bf16 bf16x2 __attribute__((ext_vector_type(2)));
#define GAS __attribute__((address_space(1)))
#define LAS __attribute__((address_space(3)))
typedef GAS u64 gu64;
typedef GAS unsigned gu32;
typedef __amdgpu_buffer_rsrc_t rsrc_t;
#define RLX_AGENT __ATOMIC_RELAXED, __HIP_MEMORY_SCOPE_AGENT

constexpr int D = 640, H = 32, SK = 8192, DH = 192, W = 2048, F = 1536;

constexpr unsigned WS_EPOCH = 0;
constexpr unsigned WS_T0 = 1024;
constexpr unsigned WS_QP = 4096;
constexpr unsigned WS_OP = WS_QP + 32 * 3072;
constexpr unsigned WS_ST = WS_OP + 32 * 8 * 192 * 8;
constexpr unsigned WS_AP = WS_ST + 32 * 8 * 128;
constexpr unsigned WS_HB = WS_AP + 40 * 512 * 8;
constexpr unsigned WS_CP = WS_HB + 8 * 4096;
constexpr unsigned WS_GP = WS_CP + 48 * 4096;
constexpr unsigned WS_DP = WS_GP + 48 * 4096;
constexpr unsigned WS_END = WS_DP + 40 * 6144;
constexpr unsigned OOB = 0x7FFFFF00u;

constexpr unsigned L_W = 0;
constexpr unsigned L_V = 49152;
constexpr unsigned L_Q = L_V + 98304;
constexpr unsigned L_P = L_Q + 384;
constexpr unsigned L_O = L_P + 512;
constexpr unsigned L_LN = L_O + 512;
constexpr unsigned L_OWN = L_LN + 1280;
constexpr unsigned L_HH = L_OWN + 1024;
constexpr unsigned L_MX = L_HH + 64;
constexpr unsigned L_WGT = L_MX + 32;
constexpr unsigned L_CNT = L_WGT + 32;
constexpr unsigned L_ST = L_CNT + 64;
constexpr unsigned L_TAG = L_ST + 16;
constexpr unsigned L_STAMP = L_TAG + 16;
constexpr unsigned L_JUNK = L_STAMP + 256;
constexpr unsigned L_END = L_JUNK + 1024;
static_assert(L_END <= 163840, "LDS");
enum { C_X = 0, C_PROG, C_KA, C_KB, C_Q, C_MAX, C_P, C_W, C_OWN, C_O, C_HH, C_WQ, C_QP, C_CS, C_GO, C_LATE };
constexpr int P_K = 96, P_WO = 240, P_WD = 282;

#define KTE 890
#define KNOB_DEFAULTS {2, 40, 100, KTE, 80, 400, -80, 256, 3, 160, 50, 85, 100, 40, 0, 0, 30, 30, 170, 302, 2, 4, 16, 4}
constexpr int kn_c[24] = KNOB_DEFAULTS;
#define KN(i) kn_c[i]
#define KNARG
#define N1G KN(0)
#define TA0 KN(1)
#define TAQ KN(2)
#define TE KN(3)
#define FDELAY KN(4)
#define TQ KN(5)
#define TS (TE + KN(6))
#define KN7 KN(7)
#define D1 KN(8)
#define D2 KN(9)
#define D3 KN(10)
#define D4 KN(11)
#define DOBS KN(12)
#define PACE2 KN(13)
#define RSL KN(14)
#define THR1 KN(16)
#define THR2 KN(17)
#define THR3 KN(18)
#define THR4 KN(19)
#define POLK 0
#define POLV 0
#define POLW 0
#define WMAX 36
#define DEPTH 2

constexpr unsigned SPIN_LDS = 1u << 20;
constexpr unsigned SPIN_GLB = 1u << 15;

__device__ __forceinline__ unsigned pk2(float lo, float hi) {
    const bf16x2 r = {(__bf16)lo, (__bf16)hi};
    return __builtin_bit_cast(unsigned, r);
}
__device__ __forceinline__ unsigned bfbits(float f) { return (unsigned)__builtin_bit_cast(u16, (__bf16)f); }
__device__ __forceinline__ float rbf(float f) { return __uint_as_float(bfbits(f) << 16); }
__device__ __forceinline__ float lo16(unsigned w) { return __uint_as_float(w << 16); }
__device__ __forceinline__ float hi16(unsigned w) { return __uint_as_float(w & 0xFFFF0000u); }
__device__ __forceinline__ float dot2(unsigned a, unsigned b, float c) {
    return __builtin_amdgcn_fdot2_f32_bf16(__builtin_bit_cast(bf16x2, a), __builtin_bit_cast(bf16x2, b), c, false);
}
template <int CTRL> __device__ __forceinline__ float dpp(float x) {
    return __builtin_bit_cast(float, __builtin_amdgcn_mov_dpp(__builtin_bit_cast(int, x), CTRL, 0xf, 0xf, true));
}
constexpr int XOR1 = 0xB1, XOR2 = 0x4E, XOR7 = 0x141, XOR8 = 0x128;
__device__ __forceinline__ float sw16_sum(float x) {
    auto s = __builtin_amdgcn_permlane16_swap(__float_as_uint(x), __float_as_uint(x), false, false);
    return __uint_as_float(s[0]) + __uint_as_float(s[1]);
}
__device__ __forceinline__ float sw32_sum(float x) {
    auto s = __builtin_amdgcn_permlane32_swap(__float_as_uint(x), __float_as_uint(x), false, false);
    return __uint_as_float(s[0]) + __uint_as_float(s[1]);
}
__device__ __forceinline__ float sw16_max(float x) {
    auto s = __builtin_amdgcn_permlane16_swap(__float_as_uint(x), __float_as_uint(x), false, false);
    return fmaxf(__uint_as_float(s[0]), __uint_as_float(s[1]));
}
__device__ __forceinline__ float sw32_max(float x) {
    auto s = __builtin_amdgcn_permlane32_swap(__float_as_uint(x), __float_as_uint(x), false, false);
    return fmaxf(__uint_as_float(s[0]), __uint_as_float(s[1]));
}
__device__ __forceinline__ float wave_sum(float x) {
    x += dpp<XOR1>(x); x += dpp<XOR2>(x); x += dpp<XOR7>(x); x += dpp<XOR8>(x);
    return sw32_sum(sw16_sum(x));
}
__device__ __forceinline__ float wave_max(float x) {
    x = fmaxf(x, dpp<XOR1>(x)); x = fmaxf(x, dpp<XOR2>(x)); x = fmaxf(x, dpp<XOR7>(x)); x = fmaxf(x, dpp<XOR8>(x));
    return sw32_max(sw16_max(x));
}
__device__ __forceinline__ rsrc_t mkrs(const void* p, unsigned bytes) { return __builtin_amdgcn_make_buffer_rsrc((void*)p, 0, bytes, 0x00020000); }

__device__ __forceinline__ void dma16(rsrc_t rs, unsigned voff, unsigned soff, unsigned lds_dst) {
    asm volatile("s_mov_b32 m0, %0\n\ts_nop 0\n\tbuffer_load_dwordx4 %1, %2, %3 offen nt lds"
                 :: "s"(lds_dst), "v"(voff), "s"(rs), "s"(soff) : "memory");
}
template <int N> __device__ __forceinline__ void waitvm() { asm volatile("s_waitcnt vmcnt(%0)" :: "n"(N) : "memory"); }
#define LGKM_DRAIN() asm volatile("s_waitcnt lgkmcnt(0)" ::: "memory")
#define MEMBAR() asm volatile("" ::: "memory")
#define BAR() do { __builtin_amdgcn_sched_barrier(0); asm volatile("s_barrier" ::: "memory"); __builtin_amdgcn_sched_barrier(0); } while (0)

__device__ __forceinline__ void lds_signal(int c) {
    const unsigned addr = L_CNT + 4 * c, one = 1u;
    asm volatile("s_mov_b64 exec, 1\n\tds_add_u32 %0, %1\n\ts_mov_b64 exec, -1" :: "v"(addr), "v"(one) : "memory");
}
__device__ __forceinline__ void lds_set(int c, unsigned val) {
    const unsigned addr = L_CNT + 4 * c;
    asm volatile("s_mov_b64 exec, 1\n\tds_write_b32 %0, %1\n\ts_mov_b64 exec, -1" :: "v"(addr), "v"(val) : "memory");
}
template <bool SLEEP, class F> __device__ __forceinline__ void lds_wait_rd(LAS unsigned char* L, int c, unsigned n, F rd) {
    volatile LAS unsigned* p = (volatile LAS unsigned*)(L + L_CNT) + c;
    unsigned cap = SPIN_LDS;
#pragma unroll 1
    do {
        const unsigned f = *p;
        MEMBAR();
        rd();
        MEMBAR();
        if ((unsigned)__builtin_amdgcn_readfirstlane(f) >= n) break;
        if (SLEEP) __builtin_amdgcn_s_sleep(1);
        MEMBAR();
    } while (--cap != 0);
    MEMBAR();
}
template <bool SLEEP> __device__ __forceinline__ void lds_wait(LAS unsigned char* L, int c, unsigned n) { lds_wait_rd<SLEEP>(L, c, n, [] {}); }
__device__ __forceinline__ void until(u64 t) { while (__builtin_amdgcn_s_memrealtime() < t) __builtin_amdgcn_s_sleep(1); }
__device__ __forceinline__ void delay(int ticks) { until(__builtin_amdgcn_s_memrealtime() + ticks); }
__device__ __forceinline__ void nap(int n) { for (int i = 0; i < n; ++i) __builtin_amdgcn_s_sleep(1); }
#define PIN(x) asm volatile("" : "+v"(x))
#define SB() __builtin_amdgcn_sched_barrier(0)
__device__ __forceinline__ float fv(unsigned u) { return __uint_as_float(u); }
__device__ __forceinline__ void put1(rsrc_t rs, unsigned off, unsigned tag, unsigned val) { __builtin_amdgcn_raw_buffer_store_b64(v2u{val, tag}, rs, off, 0, 16); }
__device__ __forceinline__ void put2(rsrc_t rs, unsigned off, unsigned tag, float a, float b) {
    __builtin_amdgcn_raw_buffer_store_b128(v4u{__float_as_uint(a), tag, __float_as_uint(b), tag}, rs, off, 0, 16);
}
__device__ __forceinline__ u64 badmask(v2u x, unsigned tag) {
    u64 m;
    asm volatile("v_cmp_ne_u32_e64 %0, %1, %2" : "=s"(m) : "s"(tag), "v"(x[1]));
    return m;
}
__device__ __forceinline__ u64 badmask(v4u x, unsigned tag) {
    u64 m;
    const unsigned t = __builtin_amdgcn_bitop3_b32(x[1], x[3], tag, 0x7E);
    asm volatile("v_cmp_ne_u32_e64 %0, 0, %1" : "=s"(m) : "v"(t));
    return m;
}
__device__ __forceinline__ void gload(v2u& x, u64 m, rsrc_t rs, unsigned voff, unsigned soff) {
    asm volatile("s_mov_b64 exec, %2\n\tbuffer_load_dwordx2 %0, %1, %3, %4 offen sc1\n\ts_mov_b64 exec, -1" : "+v"(x) : "v"(voff), "s"(m), "s"(rs), "s"(soff) : "memory");
}
__device__ __forceinline__ void gload(v4u& x, u64 m, rsrc_t rs, unsigned voff, unsigned soff) {
    asm volatile("s_mov_b64 exec, %2\n\tbuffer_load_dwordx4 %0, %1, %3, %4 offen sc1\n\ts_mov_b64 exec, -1" : "+v"(x) : "v"(voff), "s"(m), "s"(rs), "s"(soff) : "memory");
}
template <int DP, int SLP, int N, class T> __device__ __forceinline__ void gather(rsrc_t rs, unsigned voff, unsigned base, unsigned stride, unsigned tag, T (&x)[N]) {
    u64 m[N];
#pragma unroll
    for (int k = 0; k < N; ++k) { PIN(x[k]); m[k] = badmask(x[k], tag); }
    unsigned cap = SPIN_GLB;
#pragma unroll 1
    do {
#pragma unroll
        for (int k = 0; k < N; ++k) gload(x[k], m[k], rs, voff, base + k * stride);
        waitvm<0>();
#pragma unroll
        for (int k = 0; k < N; ++k) PIN(x[k]);
        u64 any = 0;
#pragma unroll
        for (int k = 0; k < N; ++k) { m[k] = badmask(x[k], tag); any |= m[k]; }
        if (!any) break;
        if (SLP) __builtin_amdgcn_s_sleep(SLP);
        MEMBAR();
    } while (--cap != 0);
}
template <int N, int PM> __device__ __forceinline__ void sum_pairs(rsrc_t rs, unsigned voff, unsigned base, unsigned tag, float& a0, float& a1);
template <int PM> __device__ __forceinline__ float combine_o(rsrc_t rs, unsigned voff, unsigned base, unsigned tag, int j, unsigned own, f32x4 w0, f32x4 w1, unsigned napn);
template <> __device__ __forceinline__ void sum_pairs<4, 0>(rsrc_t rs, unsigned voff, unsigned base, unsigned tag, float& a0, float& a1) {
    unsigned cap = SPIN_GLB;
    asm volatile(
        "buffer_load_dwordx4 v[144:147], %[vo], %[rs], %[b0] offen offset:0 sc1\n\t"
        "buffer_load_dwordx4 v[148:151], %[vo], %[rs], %[b0] offen offset:1024 sc1\n\t"
        "buffer_load_dwordx4 v[152:155], %[vo], %[rs], %[b0] offen offset:2048 sc1\n\t"
        "buffer_load_dwordx4 v[156:159], %[vo], %[rs], %[b0] offen offset:3072 sc1\n\t"
        "s_waitcnt vmcnt(0)\n\t"
        "v_bitop3_b32 v140, v145, v147, %[tag] bitop3:0x7e\n\t"
        "v_bitop3_b32 v141, v149, v151, %[tag] bitop3:0x7e\n\t"
        "v_bitop3_b32 v142, v153, v155, %[tag] bitop3:0x7e\n\t"
        "v_bitop3_b32 v143, v157, v159, %[tag] bitop3:0x7e\n\t"
        "v_cmp_ne_u32_e64 s[78:79], 0, v140\n\t"
        "v_cmp_ne_u32_e64 s[80:81], 0, v141\n\t"
        "v_cmp_ne_u32_e64 s[82:83], 0, v142\n\t"
        "v_cmp_ne_u32_e64 s[84:85], 0, v143\n\t"
        "s_or_b64 s[92:93], s[78:79], s[80:81]\n\t"
        "s_or_b64 s[92:93], s[92:93], s[82:83]\n\t"
        "s_or_b64 s[92:93], s[92:93], s[84:85]\n\t"
        "s_cbranch_scc0 3f\n\t"
        "1:\n\t"
        "s_mov_b64 exec, s[78:79]\n\t"
        "buffer_load_dwordx4 v[144:147], %[vo], %[rs], %[b0] offen offset:0 sc1\n\t"
        "s_mov_b64 exec, s[80:81]\n\t"
        "buffer_load_dwordx4 v[148:151], %[vo], %[rs], %[b0] offen offset:1024 sc1\n\t"
        "s_mov_b64 exec, s[82:83]\n\t"
        "buffer_load_dwordx4 v[152:155], %[vo], %[rs], %[b0] offen offset:2048 sc1\n\t"
        "s_mov_b64 exec, s[84:85]\n\t"
        "buffer_load_dwordx4 v[156:159], %[vo], %[rs], %[b0] offen offset:3072 sc1\n\t"
        "s_mov_b64 exec, -1\n\t"
        "s_sub_u32 %[cap], %[cap], 1\n\t"
        "s_cbranch_scc1 3f\n\t"
        "s_waitcnt vmcnt(0)\n\t"
        "v_bitop3_b32 v140, v145, v147, %[tag] bitop3:0x7e\n\t"
        "v_bitop3_b32 v141, v149, v151, %[tag] bitop3:0x7e\n\t"
        "v_bitop3_b32 v142, v153, v155, %[tag] bitop3:0x7e\n\t"
        "v_bitop3_b32 v143, v157, v159, %[tag] bitop3:0x7e\n\t"
        "v_cmp_ne_u32_e64 s[78:79], 0, v140\n\t"
        "v_cmp_ne_u32_e64 s[80:81], 0, v141\n\t"
        "v_cmp_ne_u32_e64 s[82:83], 0, v142\n\t"
        "v_cmp_ne_u32_e64 s[84:85], 0, v143\n\t"
        "s_or_b64 s[92:93], s[78:79], s[80:81]\n\t"
        "s_or_b64 s[92:93], s[92:93], s[82:83]\n\t"
        "s_or_b64 s[92:93], s[92:93], s[84:85]\n\t"
        "s_cbranch_scc1 1b\n\t"
        "3:\n\t"
        "s_waitcnt vmcnt(0)\n\t"
        "v_add_f32 %[a0], v144, v148\n\t"
        "v_add_f32 %[a1], v146, v150\n\t"
        "v_add_f32 v140, v152, v156\n\t"
        "v_add_f32 v141, v154, v158\n\t"
        "v_add_f32 %[a0], %[a0], v140\n\t"
        "v_add_f32 %[a1], %[a1], v141\n\t"
        "s_nop 1"
        : [a0] "=&v"(a0), [a1] "=&v"(a1), [cap] "+&s"(cap) : [vo] "v"(voff), [rs] "s"(rs), [b0] "s"(base), [b1] "s"(base + 4096), [tag] "s"(tag)
        : "v140", "v141", "v142", "v143", "v144", "v145", "v146", "v147", "v148", "v149", "v150", "v151", "v152", "v153", "v154", "v155", "v156", "v157", "v158", "v159", "s78", "s79", "s80", "s81", "s82", "s83", "s84", "s85", "s92", "s93", "scc", "memory");
}
template <> __device__ __forceinline__ void sum_pairs<4, 1>(rsrc_t rs, unsigned voff, unsigned base, unsigned tag, float& a0, float& a1) {
    unsigned cap = SPIN_GLB;
    asm volatile(
        "buffer_load_dwordx4 v[144:147], %[vo], %[rs], %[b0] offen offset:0 sc1\n\t"
        "buffer_load_dwordx4 v[148:151], %[vo], %[rs], %[b0] offen offset:1024 sc1\n\t"
        "buffer_load_dwordx4 v[152:155], %[vo], %[rs], %[b0] offen offset:2048 sc1\n\t"
        "buffer_load_dwordx4 v[156:159], %[vo], %[rs], %[b0] offen offset:3072 sc1\n\t"
        "s_waitcnt vmcnt(0)\n\t"
        "v_bitop3_b32 v140, v145, v147, %[tag] bitop3:0x7e\n\t"
        "v_bitop3_b32 v141, v149, v151, %[tag] bitop3:0x7e\n\t"
        "v_bitop3_b32 v142, v153, v155, %[tag] bitop3:0x7e\n\t"
        "v_bitop3_b32 v143, v157, v159, %[tag] bitop3:0x7e\n\t"
        "v_cmp_ne_u32_e64 s[78:79], 0, v140\n\t"
        "v_cmp_ne_u32_e64 s[80:81], 0, v141\n\t"
        "v_cmp_ne_u32_e64 s[82:83], 0, v142\n\t"
        "v_cmp_ne_u32_e64 s[84:85], 0, v143\n\t"
        "s_or_b64 s[92:93], s[78:79], s[80:81]\n\t"
        "s_or_b64 s[92:93], s[92:93], s[82:83]\n\t"
        "s_or_b64 s[92:93], s[92:93], s[84:85]\n\t"
        "s_cbranch_scc0 3f\n\t"
        "s_mov_b64 exec, s[78:79]\n\t"
        "buffer_load_dwordx4 v[144:147], %[vo], %[rs], %[b0] offen offset:0 sc1\n\t"
        "s_mov_b64 exec, s[80:81]\n\t"
        "buffer_load_dwordx4 v[148:151], %[vo], %[rs], %[b0] offen offset:1024 sc1\n\t"
        "s_mov_b64 exec, s[82:83]\n\t"
        "buffer_load_dwordx4 v[152:155], %[vo], %[rs], %[b0] offen offset:2048 sc1\n\t"
        "s_mov_b64 exec, s[84:85]\n\t"
        "buffer_load_dwordx4 v[156:159], %[vo], %[rs], %[b0] offen offset:3072 sc1\n\t"
        "s_mov_b64 exec, -1\n\t"
        "s_sleep 2\n\t"
        "1:\n\t"
        "s_mov_b64 exec, s[78:79]\n\t"
        "buffer_load_dwordx4 v[144:147], %[vo], %[rs], %[b0] offen offset:0 sc1\n\t"
        "s_mov_b64 exec, s[80:81]\n\t"
        "buffer_load_dwordx4 v[148:151], %[vo], %[rs], %[b0] offen offset:1024 sc1\n\t"
        "s_mov_b64 exec, s[82:83]\n\t"
        "buffer_load_dwordx4 v[152:155], %[vo], %[rs], %[b0] offen offset:2048 sc1\n\t"
        "s_mov_b64 exec, s[84:85]\n\t"
        "buffer_load_dwordx4 v[156:159], %[vo], %[rs], %[b0] offen offset:3072 sc1\n\t"
        "s_mov_b64 exec, -1\n\t"
        "s_sub_u32 %[cap], %[cap], 1\n\t"
        "s_cbranch_scc1 3f\n\t"
        "s_waitcnt vmcnt(4)\n\t"
        "v_bitop3_b32 v140, v145, v147, %[tag] bitop3:0x7e\n\t"
        "v_bitop3_b32 v141, v149, v151, %[tag] bitop3:0x7e\n\t"
        "v_bitop3_b32 v142, v153, v155, %[tag] bitop3:0x7e\n\t"
        "v_bitop3_b32 v143, v157, v159, %[tag] bitop3:0x7e\n\t"
        "v_cmp_ne_u32_e64 s[78:79], 0, v140\n\t"
        "v_cmp_ne_u32_e64 s[80:81], 0, v141\n\t"
        "v_cmp_ne_u32_e64 s[82:83], 0, v142\n\t"
        "v_cmp_ne_u32_e64 s[84:85], 0, v143\n\t"
        "s_or_b64 s[92:93], s[78:79], s[80:81]\n\t"
        "s_or_b64 s[92:93], s[92:93], s[82:83]\n\t"
        "s_or_b64 s[92:93], s[92:93], s[84:85]\n\t"
        "s_cbranch_scc1 1b\n\t"
        "3:\n\t"
        "s_waitcnt vmcnt(0)\n\t"
        "v_add_f32 %[a0], v144, v148\n\t"
        "v_add_f32 %[a1], v146, v150\n\t"
        "v_add_f32 v140, v152, v156\n\t"
        "v_add_f32 v141, v154, v158\n\t"
        "v_add_f32 %[a0], %[a0], v140\n\t"
        "v_add_f32 %[a1], %[a1], v141\n\t"
        "s_nop 1"
        : [a0] "=&v"(a0), [a1] "=&v"(a1), [cap] "+&s"(cap) : [vo] "v"(voff), [rs] "s"(rs), [b0] "s"(base), [b1] "s"(base + 4096), [tag] "s"(tag)
        : "v140", "v141", "v142", "v143", "v144", "v145", "v146", "v147", "v148", "v149", "v150", "v151", "v152", "v153", "v154", "v155", "v156", "v157", "v158", "v159", "s78", "s79", "s80", "s81", "s82", "s83", "s84", "s85", "s92", "s93", "scc", "memory");
}
template <> __device__ __forceinline__ void sum_pairs<5, 0>(rsrc_t rs, unsigned voff, unsigned base, unsigned tag, float& a0, float& a1) {
    unsigned cap = SPIN_GLB;
    asm volatile(
        "buffer_load_dwordx4 v[144:147], %[vo], %[rs], %[b0] offen offset:0 sc1\n\t"
        "buffer_load_dwordx4 v[148:151], %[vo], %[rs], %[b0] offen offset:1024 sc1\n\t"
        "buffer_load_dwordx4 v[152:155], %[vo], %[rs], %[b0] offen offset:2048 sc1\n\t"
        "buffer_load_dwordx4 v[156:159], %[vo], %[rs], %[b0] offen offset:3072 sc1\n\t"
        "buffer_load_dwordx4 v[160:163], %[vo], %[rs], %[b1] offen offset:0 sc1\n\t"
        "s_waitcnt vmcnt(0)\n\t"
        "v_bitop3_b32 v140, v145, v147, %[tag] bitop3:0x7e\n\t"
        "v_bitop3_b32 v141, v149, v151, %[tag] bitop3:0x7e\n\t"
        "v_bitop3_b32 v142, v153, v155, %[tag] bitop3:0x7e\n\t"
        "v_bitop3_b32 v143, v157, v159, %[tag] bitop3:0x7e\n\t"
        "v_cmp_ne_u32_e64 s[78:79], 0, v140\n\t"
        "v_cmp_ne_u32_e64 s[80:81], 0, v141\n\t"
        "v_cmp_ne_u32_e64 s[82:83], 0, v142\n\t"
        "v_cmp_ne_u32_e64 s[84:85], 0, v143\n\t"
        "v_bitop3_b32 v140, v161, v163, %[tag] bitop3:0x7e\n\t"
        "v_cmp_ne_u32_e64 s[86:87], 0, v140\n\t"
        "s_or_b64 s[92:93], s[78:79], s[80:81]\n\t"
        "s_or_b64 s[92:93], s[92:93], s[82:83]\n\t"
        "s_or_b64 s[92:93], s[92:93], s[84:85]\n\t"
        "s_or_b64 s[92:93], s[92:93], s[86:87]\n\t"
        "s_cbranch_scc0 3f\n\t"
        "1:\n\t"
        "s_mov_b64 exec, s[78:79]\n\t"
        "buffer_load_dwordx4 v[144:147], %[vo], %[rs], %[b0] offen offset:0 sc1\n\t"
        "s_mov_b64 exec, s[80:81]\n\t"
        "buffer_load_dwordx4 v[148:151], %[vo], %[rs], %[b0] offen offset:1024 sc1\n\t"
        "s_mov_b64 exec, s[82:83]\n\t"
        "buffer_load_dwordx4 v[152:155], %[vo], %[rs], %[b0] offen offset:2048 sc1\n\t"
        "s_mov_b64 exec, s[84:85]\n\t"
        "buffer_load_dwordx4 v[156:159], %[vo], %[rs], %[b0] offen offset:3072 sc1\n\t"
        "s_mov_b64 exec, s[86:87]\n\t"
        "buffer_load_dwordx4 v[160:163], %[vo], %[rs], %[b1] offen offset:0 sc1\n\t"
        "s_mov_b64 exec, -1\n\t"
        "s_sub_u32 %[cap], %[cap], 1\n\t"
        "s_cbranch_scc1 3f\n\t"
        "s_waitcnt vmcnt(0)\n\t"
        "v_bitop3_b32 v140, v145, v147, %[tag] bitop3:0x7e\n\t"
        "v_bitop3_b32 v141, v149, v151, %[tag] bitop3:0x7e\n\t"
        "v_bitop3_b32 v142, v153, v155, %[tag] bitop3:0x7e\n\t"
        "v_bitop3_b32 v143, v157, v159, %[tag] bitop3:0x7e\n\t"
        "v_cmp_ne_u32_e64 s[78:79], 0, v140\n\t"
        "v_cmp_ne_u32_e64 s[80:81], 0, v141\n\t"
        "v_cmp_ne_u32_e64 s[82:83], 0, v142\n\t"
        "v_cmp_ne_u32_e64 s[84:85], 0, v143\n\t"
        "v_bitop3_b32 v140, v161, v163, %[tag] bitop3:0x7e\n\t"
        "v_cmp_ne_u32_e64 s[86:87], 0, v140\n\t"
        "s_or_b64 s[92:93], s[78:79], s[80:81]\n\t"
        "s_or_b64 s[92:93], s[92:93], s[82:83]\n\t"
        "s_or_b64 s[92:93], s[92:93], s[84:85]\n\t"
        "s_or_b64 s[92:93], s[92:93], s[86:87]\n\t"
        "s_cbranch_scc1 1b\n\t"
        "3:\n\t"
        "s_waitcnt vmcnt(0)\n\t"
        "v_add_f32 %[a0], v144, v148\n\t"
        "v_add_f32 %[a1], v146, v150\n\t"
        "v_add_f32 v140, v152, v156\n\t"
        "v_add_f32 v141, v154, v158\n\t"
        "v_add_f32 %[a0], %[a0], v140\n\t"
        "v_add_f32 %[a1], %[a1], v141\n\t"
        "v_add_f32 %[a0], %[a0], v160\n\t"
        "v_add_f32 %[a1], %[a1], v162\n\t"
        "s_nop 1"
        : [a0] "=&v"(a0), [a1] "=&v"(a1), [cap] "+&s"(cap) : [vo] "v"(voff), [rs] "s"(rs), [b0] "s"(base), [b1] "s"(base + 4096), [tag] "s"(tag)
        : "v140", "v141", "v142", "v143", "v144", "v145", "v146", "v147", "v148", "v149", "v150", "v151", "v152", "v153", "v154", "v155", "v156", "v157", "v158", "v159", "v160", "v161", "v162", "v163", "s78", "s79", "s80", "s81", "s82", "s83", "s84", "s85", "s86", "s87", "s92", "s93", "scc", "memory");
}
template <> __device__ __forceinline__ void sum_pairs<5, 1>(rsrc_t rs, unsigned voff, unsigned base, unsigned tag, float& a0, float& a1) {
    unsigned cap = SPIN_GLB;
    asm volatile(
        "buffer_load_dwordx4 v[144:147], %[vo], %[rs], %[b0] offen offset:0 sc1\n\t"
        "buffer_load_dwordx4 v[148:151], %[vo], %[rs], %[b0] offen offset:1024 sc1\n\t"
        "buffer_load_dwordx4 v[152:155], %[vo], %[rs], %[b0] offen offset:2048 sc1\n\t"
        "buffer_load_dwordx4 v[156:159], %[vo], %[rs], %[b0] offen offset:3072 sc1\n\t"
        "buffer_load_dwordx4 v[160:163], %[vo], %[rs], %[b1] offen offset:0 sc1\n\t"
        "s_waitcnt vmcnt(0)\n\t"
        "v_bitop3_b32 v140, v145, v147, %[tag] bitop3:0x7e\n\t"
        "v_bitop3_b32 v141, v149, v151, %[tag] bitop3:0x7e\n\t"
        "v_bitop3_b32 v142, v153, v155, %[tag] bitop3:0x7e\n\t"
        "v_bitop3_b32 v143, v157, v159, %[tag] bitop3:0x7e\n\t"
        "v_cmp_ne_u32_e64 s[78:79], 0, v140\n\t"
        "v_cmp_ne_u32_e64 s[80:81], 0, v141\n\t"
        "v_cmp_ne_u32_e64 s[82:83], 0, v142\n\t"
        "v_cmp_ne_u32_e64 s[84:85], 0, v143\n\t"
        "v_bitop3_b32 v140, v161, v163, %[tag] bitop3:0x7e\n\t"
        "v_cmp_ne_u32_e64 s[86:87], 0, v140\n\t"
        "s_or_b64 s[92:93], s[78:79], s[80:81]\n\t"
        "s_or_b64 s[92:93], s[92:93], s[82:83]\n\t"
        "s_or_b64 s[92:93], s[92:93], s[84:85]\n\t"
        "s_or_b64 s[92:93], s[92:93], s[86:87]\n\t"
        "s_cbranch_scc0 3f\n\t"
        "s_mov_b64 exec, s[78:79]\n\t"
        "buffer_load_dwordx4 v[144:147], %[vo], %[rs], %[b0] offen offset:0 sc1\n\t"
        "s_mov_b64 exec, s[80:81]\n\t"
        "buffer_load_dwordx4 v[148:151], %[vo], %[rs], %[b0] offen offset:1024 sc1\n\t"
        "s_mov_b64 exec, s[82:83]\n\t"
        "buffer_load_dwordx4 v[152:155], %[vo], %[rs], %[b0] offen offset:2048 sc1\n\t"
        "s_mov_b64 exec, s[84:85]\n\t"
        "buffer_load_dwordx4 v[156:159], %[vo], %[rs], %[b0] offen offset:3072 sc1\n\t"
        "s_mov_b64 exec, s[86:87]\n\t"
        "buffer_load_dwordx4 v[160:163], %[vo], %[rs], %[b1] offen offset:0 sc1\n\t"
        "s_mov_b64 exec, -1\n\t"
        "s_sleep 2\n\t"
        "1:\n\t"
        "s_mov_b64 exec, s[78:79]\n\t"
        "buffer_load_dwordx4 v[144:147], %[vo], %[rs], %[b0] offen offset:0 sc1\n\t"
        "s_mov_b64 exec, s[80:81]\n\t"
        "buffer_load_dwordx4 v[148:151], %[vo], %[rs], %[b0] offen offset:1024 sc1\n\t"
        "s_mov_b64 exec, s[82:83]\n\t"
        "buffer_load_dwordx4 v[152:155], %[vo], %[rs], %[b0] offen offset:2048 sc1\n\t"
        "s_mov_b64 exec, s[84:85]\n\t"
        "buffer_load_dwordx4 v[156:159], %[vo], %[rs], %[b0] offen offset:3072 sc1\n\t"
        "s_mov_b64 exec, s[86:87]\n\t"
        "buffer_load_dwordx4 v[160:163], %[vo], %[rs], %[b1] offen offset:0 sc1\n\t"
        "s_mov_b64 exec, -1\n\t"
        "s_sub_u32 %[cap], %[cap], 1\n\t"
        "s_cbranch_scc1 3f\n\t"
        "s_waitcnt vmcnt(5)\n\t"
        "v_bitop3_b32 v140, v145, v147, %[tag] bitop3:0x7e\n\t"
        "v_bitop3_b32 v141, v149, v151, %[tag] bitop3:0x7e\n\t"
        "v_bitop3_b32 v142, v153, v155, %[tag] bitop3:0x7e\n\t"
        "v_bitop3_b32 v143, v157, v159, %[tag] bitop3:0x7e\n\t"
        "v_cmp_ne_u32_e64 s[78:79], 0, v140\n\t"
        "v_cmp_ne_u32_e64 s[80:81], 0, v141\n\t"
        "v_cmp_ne_u32_e64 s[82:83], 0, v142\n\t"
        "v_cmp_ne_u32_e64 s[84:85], 0, v143\n\t"
        "v_bitop3_b32 v140, v161, v163, %[tag] bitop3:0x7e\n\t"
        "v_cmp_ne_u32_e64 s[86:87], 0, v140\n\t"
        "s_or_b64 s[92:93], s[78:79], s[80:81]\n\t"
        "s_or_b64 s[92:93], s[92:93], s[82:83]\n\t"
        "s_or_b64 s[92:93], s[92:93], s[84:85]\n\t"
        "s_or_b64 s[92:93], s[92:93], s[86:87]\n\t"
        "s_cbranch_scc1 1b\n\t"
        "3:\n\t"
        "s_waitcnt vmcnt(0)\n\t"
        "v_add_f32 %[a0], v144, v148\n\t"
        "v_add_f32 %[a1], v146, v150\n\t"
        "v_add_f32 v140, v152, v156\n\t"
        "v_add_f32 v141, v154, v158\n\t"
        "v_add_f32 %[a0], %[a0], v140\n\t"
        "v_add_f32 %[a1], %[a1], v141\n\t"
        "v_add_f32 %[a0], %[a0], v160\n\t"
        "v_add_f32 %[a1], %[a1], v162\n\t"
        "s_nop 1"
        : [a0] "=&v"(a0), [a1] "=&v"(a1), [cap] "+&s"(cap) : [vo] "v"(voff), [rs] "s"(rs), [b0] "s"(base), [b1] "s"(base + 4096), [tag] "s"(tag)
        : "v140", "v141", "v142", "v143", "v144", "v145", "v146", "v147", "v148", "v149", "v150", "v151", "v152", "v153", "v154", "v155", "v156", "v157", "v158", "v159", "v160", "v161", "v162", "v163", "s78", "s79", "s80", "s81", "s82", "s83", "s84", "s85", "s86", "s87", "s92", "s93", "scc", "memory");
}
template <> __device__ __forceinline__ void sum_pairs<6, 0>(rsrc_t rs, unsigned voff, unsigned base, unsigned tag, float& a0, float& a1) {
    unsigned cap = SPIN_GLB;
    asm volatile(
        "buffer_load_dwordx4 v[144:147], %[vo], %[rs], %[b0] offen offset:0 sc1\n\t"
        "buffer_load_dwordx4 v[148:151], %[vo], %[rs], %[b0] offen offset:1024 sc1\n\t"
        "buffer_load_dwordx4 v[152:155], %[vo], %[rs], %[b0] offen offset:2048 sc1\n\t"
        "buffer_load_dwordx4 v[156:159], %[vo], %[rs], %[b0] offen offset:3072 sc1\n\t"
        "buffer_load_dwordx4 v[160:163], %[vo], %[rs], %[b1] offen offset:0 sc1\n\t"
        "buffer_load_dwordx4 v[164:167], %[vo], %[rs], %[b1] offen offset:1024 sc1\n\t"
        "s_waitcnt vmcnt(0)\n\t"
        "v_bitop3_b32 v140, v145, v147, %[tag] bitop3:0x7e\n\t"
        "v_bitop3_b32 v141, v149, v151, %[tag] bitop3:0x7e\n\t"
        "v_bitop3_b32 v142, v153, v155, %[tag] bitop3:0x7e\n\t"
        "v_bitop3_b32 v143, v157, v159, %[tag] bitop3:0x7e\n\t"
        "v_cmp_ne_u32_e64 s[78:79], 0, v140\n\t"
        "v_cmp_ne_u32_e64 s[80:81], 0, v141\n\t"
        "v_cmp_ne_u32_e64 s[82:83], 0, v142\n\t"
        "v_cmp_ne_u32_e64 s[84:85], 0, v143\n\t"
        "v_bitop3_b32 v140, v161, v163, %[tag] bitop3:0x7e\n\t"
        "v_bitop3_b32 v141, v165, v167, %[tag] bitop3:0x7e\n\t"
        "v_cmp_ne_u32_e64 s[86:87], 0, v140\n\t"
        "v_cmp_ne_u32_e64 s[88:89], 0, v141\n\t"
        "s_or_b64 s[92:93], s[78:79], s[80:81]\n\t"
        "s_or_b64 s[92:93], s[92:93], s[82:83]\n\t"
        "s_or_b64 s[92:93], s[92:93], s[84:85]\n\t"
        "s_or_b64 s[92:93], s[92:93], s[86:87]\n\t"
        "s_or_b64 s[92:93], s[92:93], s[88:89]\n\t"
        "s_cbranch_scc0 3f\n\t"
        "1:\n\t"
        "s_mov_b64 exec, s[78:79]\n\t"
        "buffer_load_dwordx4 v[144:147], %[vo], %[rs], %[b0] offen offset:0 sc1\n\t"
        "s_mov_b64 exec, s[80:81]\n\t"
        "buffer_load_dwordx4 v[148:151], %[vo], %[rs], %[b0] offen offset:1024 sc1\n\t"
        "s_mov_b64 exec, s[82:83]\n\t"
        "buffer_load_dwordx4 v[152:155], %[vo], %[rs], %[b0] offen offset:2048 sc1\n\t"
        "s_mov_b64 exec, s[84:85]\n\t"
        "buffer_load_dwordx4 v[156:159], %[vo], %[rs], %[b0] offen offset:3072 sc1\n\t"
        "s_mov_b64 exec, s[86:87]\n\t"
        "buffer_load_dwordx4 v[160:163], %[vo], %[rs], %[b1] offen offset:0 sc1\n\t"
        "s_mov_b64 exec, s[88:89]\n\t"
        "buffer_load_dwordx4 v[164:167], %[vo], %[rs], %[b1] offen offset:1024 sc1\n\t"
        "s_mov_b64 exec, -1\n\t"
        "s_sub_u32 %[cap], %[cap], 1\n\t"
        "s_cbranch_scc1 3f\n\t"
        "s_waitcnt vmcnt(0)\n\t"
        "v_bitop3_b32 v140, v145, v147, %[tag] bitop3:0x7e\n\t"
        "v_bitop3_b32 v141, v149, v151, %[tag] bitop3:0x7e\n\t"
        "v_bitop3_b32 v142, v153, v155, %[tag] bitop3:0x7e\n\t"
        "v_bitop3_b32 v143, v157, v159, %[tag] bitop3:0x7e\n\t"
        "v_cmp_ne_u32_e64 s[78:79], 0, v140\n\t"
        "v_cmp_ne_u32_e64 s[80:81], 0, v141\n\t"
        "v_cmp_ne_u32_e64 s[82:83], 0, v142\n\t"
        "v_cmp_ne_u32_e64 s[84:85], 0, v143\n\t"
        "v_bitop3_b32 v140, v161, v163, %[tag] bitop3:0x7e\n\t"
        "v_bitop3_b32 v141, v165, v167, %[tag] bitop3:0x7e\n\t"
        "v_cmp_ne_u32_e64 s[86:87], 0, v140\n\t"
        "v_cmp_ne_u32_e64 s[88:89], 0, v141\n\t"
        "s_or_b64 s[92:93], s[78:79], s[80:81]\n\t"
        "s_or_b64 s[92:93], s[92:93], s[82:83]\n\t"
        "s_or_b64 s[92:93], s[92:93], s[84:85]\n\t"
        "s_or_b64 s[92:93], s[92:93], s[86:87]\n\t"
        "s_or_b64 s[92:93], s[92:93], s[88:89]\n\t"
        "s_cbranch_scc1 1b\n\t"
        "3:\n\t"
        "s_waitcnt vmcnt(0)\n\t"
        "v_add_f32 %[a0], v144, v148\n\t"
        "v_add_f32 %[a1], v146, v150\n\t"
        "v_add_f32 v140, v152, v156\n\t"
        "v_add_f32 v141, v154, v158\n\t"
        "v_add_f32 v142, v160, v164\n\t"
        "v_add_f32 v143, v162, v166\n\t"
        "v_add_f32 %[a0], %[a0], v140\n\t"
        "v_add_f32 %[a1], %[a1], v141\n\t"
        "v_add_f32 %[a0], %[a0], v142\n\t"
        "v_add_f32 %[a1], %[a1], v143\n\t"
        "s_nop 1"
        : [a0] "=&v"(a0), [a1] "=&v"(a1), [cap] "+&s"(cap) : [vo] "v"(voff), [rs] "s"(rs), [b0] "s"(base), [b1] "s"(base + 4096), [tag] "s"(tag)
        : "v140", "v141", "v142", "v143", "v144", "v145", "v146", "v147", "v148", "v149", "v150", "v151", "v152", "v153", "v154", "v155", "v156", "v157", "v158", "v159", "v160", "v161", "v162", "v163", "v164", "v165", "v166", "v167", "s78", "s79", "s80", "s81", "s82", "s83", "s84", "s85", "s86", "s87", "s88", "s89", "s92", "s93", "scc", "memory");
}
template <> __device__ __forceinline__ void sum_pairs<6, 1>(rsrc_t rs, unsigned voff, unsigned base, unsigned tag, float& a0, float& a1) {
    unsigned cap = SPIN_GLB;
    asm volatile(
        "buffer_load_dwordx4 v[144:147], %[vo], %[rs], %[b0] offen offset:0 sc1\n\t"
        "buffer_load_dwordx4 v[148:151], %[vo], %[rs], %[b0] offen offset:1024 sc1\n\t"
        "buffer_load_dwordx4 v[152:155], %[vo], %[rs], %[b0] offen offset:2048 sc1\n\t"
        "buffer_load_dwordx4 v[156:159], %[vo], %[rs], %[b0] offen offset:3072 sc1\n\t"
        "buffer_load_dwordx4 v[160:163], %[vo], %[rs], %[b1] offen offset:0 sc1\n\t"
        "buffer_load_dwordx4 v[164:167], %[vo], %[rs], %[b1] offen offset:1024 sc1\n\t"
        "s_waitcnt vmcnt(0)\n\t"
        "v_bitop3_b32 v140, v145, v147, %[tag] bitop3:0x7e\n\t"
        "v_bitop3_b32 v141, v149, v151, %[tag] bitop3:0x7e\n\t"
        "v_bitop3_b32 v142, v153, v155, %[tag] bitop3:0x7e\n\t"
        "v_bitop3_b32 v143, v157, v159, %[tag] bitop3:0x7e\n\t"
        "v_cmp_ne_u32_e64 s[78:79], 0, v140\n\t"
        "v_cmp_ne_u32_e64 s[80:81], 0, v141\n\t"
        "v_cmp_ne_u32_e64 s[82:83], 0, v142\n\t"
        "v_cmp_ne_u32_e64 s[84:85], 0, v143\n\t"
        "v_bitop3_b32 v140, v161, v163, %[tag] bitop3:0x7e\n\t"
        "v_bitop3_b32 v141, v165, v167, %[tag] bitop3:0x7e\n\t"
        "v_cmp_ne_u32_e64 s[86:87], 0, v140\n\t"
        "v_cmp_ne_u32_e64 s[88:89], 0, v141\n\t"
        "s_or_b64 s[92:93], s[78:79], s[80:81]\n\t"
        "s_or_b64 s[92:93], s[92:93], s[82:83]\n\t"
        "s_or_b64 s[92:93], s[92:93], s[84:85]\n\t"
        "s_or_b64 s[92:93], s[92:93], s[86:87]\n\t"
        "s_or_b64 s[92:93], s[92:93], s[88:89]\n\t"
        "s_cbranch_scc0 3f\n\t"
        "s_mov_b64 exec, s[78:79]\n\t"
        "buffer_load_dwordx4 v[144:147], %[vo], %[rs], %[b0] offen offset:0 sc1\n\t"
        "s_mov_b64 exec, s[80:81]\n\t"
        "buffer_load_dwordx4 v[148:151], %[vo], %[rs], %[b0] offen offset:1024 sc1\n\t"
        "s_mov_b64 exec, s[82:83]\n\t"
        "buffer_load_dwordx4 v[152:155], %[vo], %[rs], %[b0] offen offset:2048 sc1\n\t"
        "s_mov_b64 exec, s[84:85]\n\t"
        "buffer_load_dwordx4 v[156:159], %[vo], %[rs], %[b0] offen offset:3072 sc1\n\t"
        "s_mov_b64 exec, s[86:87]\n\t"
        "buffer_load_dwordx4 v[160:163], %[vo], %[rs], %[b1] offen offset:0 sc1\n\t"
        "s_mov_b64 exec, s[88:89]\n\t"
        "buffer_load_dwordx4 v[164:167], %[vo], %[rs], %[b1] offen offset:1024 sc1\n\t"
        "s_mov_b64 exec, -1\n\t"
        "s_sleep 2\n\t"
        "1:\n\t"
        "s_mov_b64 exec, s[78:79]\n\t"
        "buffer_load_dwordx4 v[144:147], %[vo], %[rs], %[b0] offen offset:0 sc1\n\t"
        "s_mov_b64 exec, s[80:81]\n\t"
        "buffer_load_dwordx4 v[148:151], %[vo], %[rs], %[b0] offen offset:1024 sc1\n\t"
        "s_mov_b64 exec, s[82:83]\n\t"
        "buffer_load_dwordx4 v[152:155], %[vo], %[rs], %[b0] offen offset:2048 sc1\n\t"
        "s_mov_b64 exec, s[84:85]\n\t"
        "buffer_load_dwordx4 v[156:159], %[vo], %[rs], %[b0] offen offset:3072 sc1\n\t"
        "s_mov_b64 exec, s[86:87]\n\t"
        "buffer_load_dwordx4 v[160:163], %[vo], %[rs], %[b1] offen offset:0 sc1\n\t"
        "s_mov_b64 exec, s[88:89]\n\t"
        "buffer_load_dwordx4 v[164:167], %[vo], %[rs], %[b1] offen offset:1024 sc1\n\t"
        "s_mov_b64 exec, -1\n\t"
        "s_sub_u32 %[cap], %[cap], 1\n\t"
        "s_cbranch_scc1 3f\n\t"
        "s_waitcnt vmcnt(6)\n\t"
        "v_bitop3_b32 v140, v145, v147, %[tag] bitop3:0x7e\n\t"
        "v_bitop3_b32 v141, v149, v151, %[tag] bitop3:0x7e\n\t"
        "v_bitop3_b32 v142, v153, v155, %[tag] bitop3:0x7e\n\t"
        "v_bitop3_b32 v143, v157, v159, %[tag] bitop3:0x7e\n\t"
        "v_cmp_ne_u32_e64 s[78:79], 0, v140\n\t"
        "v_cmp_ne_u32_e64 s[80:81], 0, v141\n\t"
        "v_cmp_ne_u32_e64 s[82:83], 0, v142\n\t"
        "v_cmp_ne_u32_e64 s[84:85], 0, v143\n\t"
        "v_bitop3_b32 v140, v161, v163, %[tag] bitop3:0x7e\n\t"
        "v_bitop3_b32 v141, v165, v167, %[tag] bitop3:0x7e\n\t"
        "v_cmp_ne_u32_e64 s[86:87], 0, v140\n\t"
        "v_cmp_ne_u32_e64 s[88:89], 0, v141\n\t"
        "s_or_b64 s[92:93], s[78:79], s[80:81]\n\t"
        "s_or_b64 s[92:93], s[92:93], s[82:83]\n\t"
        "s_or_b64 s[92:93], s[92:93], s[84:85]\n\t"
        "s_or_b64 s[92:93], s[92:93], s[86:87]\n\t"
        "s_or_b64 s[92:93], s[92:93], s[88:89]\n\t"
        "s_cbranch_scc1 1b\n\t"
        "3:\n\t"
        "s_waitcnt vmcnt(0)\n\t"
        "v_add_f32 %[a0], v144, v148\n\t"
        "v_add_f32 %[a1], v146, v150\n\t"
        "v_add_f32 v140, v152, v156\n\t"
        "v_add_f32 v141, v154, v158\n\t"
        "v_add_f32 v142, v160, v164\n\t"
        "v_add_f32 v143, v162, v166\n\t"
        "v_add_f32 %[a0], %[a0], v140\n\t"
        "v_add_f32 %[a1], %[a1], v141\n\t"
        "v_add_f32 %[a0], %[a0], v142\n\t"
        "v_add_f32 %[a1], %[a1], v143\n\t"
        "s_nop 1"
        : [a0] "=&v"(a0), [a1] "=&v"(a1), [cap] "+&s"(cap) : [vo] "v"(voff), [rs] "s"(rs), [b0] "s"(base), [b1] "s"(base + 4096), [tag] "s"(tag)
        : "v140", "v141", "v142", "v143", "v144", "v145", "v146", "v147", "v148", "v149", "v150", "v151", "v152", "v153", "v154", "v155", "v156", "v157", "v158", "v159", "v160", "v161", "v162", "v163", "v164", "v165", "v166", "v167", "s78", "s79", "s80", "s81", "s82", "s83", "s84", "s85", "s86", "s87", "s88", "s89", "s92", "s93", "scc", "memory");
}
template <> __device__ __forceinline__ float combine_o<0>(rsrc_t rs, unsigned voff, unsigned base, unsigned tag, int j, unsigned own, f32x4 w0, f32x4 w1, unsigned napn) {
    unsigned cap = SPIN_GLB;
    float o;
    asm volatile(
        "s_cmp_eq_u32 %[j], 0\n\t"
        "s_cselect_b64 s[74:75], 0, %[lm]\n\t"
        "v_mov_b32 v144, %[own]\n\t"
        "v_mov_b32 v145, %[tag]\n\t"
        "s_cmp_eq_u32 %[j], 1\n\t"
        "s_cselect_b64 s[76:77], 0, %[lm]\n\t"
        "v_mov_b32 v146, %[own]\n\t"
        "v_mov_b32 v147, %[tag]\n\t"
        "s_cmp_eq_u32 %[j], 2\n\t"
        "s_cselect_b64 s[78:79], 0, %[lm]\n\t"
        "v_mov_b32 v148, %[own]\n\t"
        "v_mov_b32 v149, %[tag]\n\t"
        "s_cmp_eq_u32 %[j], 3\n\t"
        "s_cselect_b64 s[80:81], 0, %[lm]\n\t"
        "v_mov_b32 v150, %[own]\n\t"
        "v_mov_b32 v151, %[tag]\n\t"
        "s_cmp_eq_u32 %[j], 4\n\t"
        "s_cselect_b64 s[82:83], 0, %[lm]\n\t"
        "v_mov_b32 v152, %[own]\n\t"
        "v_mov_b32 v153, %[tag]\n\t"
        "s_cmp_eq_u32 %[j], 5\n\t"
        "s_cselect_b64 s[84:85], 0, %[lm]\n\t"
        "v_mov_b32 v154, %[own]\n\t"
        "v_mov_b32 v155, %[tag]\n\t"
        "s_cmp_eq_u32 %[j], 6\n\t"
        "s_cselect_b64 s[86:87], 0, %[lm]\n\t"
        "v_mov_b32 v156, %[own]\n\t"
        "v_mov_b32 v157, %[tag]\n\t"
        "s_cmp_eq_u32 %[j], 7\n\t"
        "s_cselect_b64 s[88:89], 0, %[lm]\n\t"
        "v_mov_b32 v158, %[own]\n\t"
        "v_mov_b32 v159, %[tag]\n\t"
        "s_cmp_eq_u32 %[nap], 0\n\t"
        "s_cbranch_scc1 5f\n\t"
        "4:\n\t"
        "s_sleep 1\n\t"
        "s_sub_u32 %[nap], %[nap], 1\n\t"
        "s_cmp_lg_u32 %[nap], 0\n\t"
        "s_cbranch_scc1 4b\n\t"
        "5:\n\t"
        "s_mov_b64 exec, s[74:75]\n\t"
        "buffer_load_dwordx2 v[144:145], %[vo], %[rs], %[b0] offen offset:0 sc1\n\t"
        "s_mov_b64 exec, s[76:77]\n\t"
        "buffer_load_dwordx2 v[146:147], %[vo], %[rs], %[b0] offen offset:1536 sc1\n\t"
        "s_mov_b64 exec, s[78:79]\n\t"
        "buffer_load_dwordx2 v[148:149], %[vo], %[rs], %[b0] offen offset:3072 sc1\n\t"
        "s_mov_b64 exec, s[80:81]\n\t"
        "buffer_load_dwordx2 v[150:151], %[vo], %[rs], %[b1] offen offset:0 sc1\n\t"
        "s_mov_b64 exec, s[82:83]\n\t"
        "buffer_load_dwordx2 v[152:153], %[vo], %[rs], %[b1] offen offset:1536 sc1\n\t"
        "s_mov_b64 exec, s[84:85]\n\t"
        "buffer_load_dwordx2 v[154:155], %[vo], %[rs], %[b1] offen offset:3072 sc1\n\t"
        "s_mov_b64 exec, s[86:87]\n\t"
        "buffer_load_dwordx2 v[156:157], %[vo], %[rs], %[b2] offen offset:0 sc1\n\t"
        "s_mov_b64 exec, s[88:89]\n\t"
        "buffer_load_dwordx2 v[158:159], %[vo], %[rs], %[b2] offen offset:1536 sc1\n\t"
        "s_mov_b64 exec, -1\n\t"
        "s_waitcnt vmcnt(0)\n\t"
        "v_cmp_ne_u32_e64 s[74:75], %[tag], v145\n\t"
        "v_cmp_ne_u32_e64 s[76:77], %[tag], v147\n\t"
        "v_cmp_ne_u32_e64 s[78:79], %[tag], v149\n\t"
        "v_cmp_ne_u32_e64 s[80:81], %[tag], v151\n\t"
        "v_cmp_ne_u32_e64 s[82:83], %[tag], v153\n\t"
        "v_cmp_ne_u32_e64 s[84:85], %[tag], v155\n\t"
        "v_cmp_ne_u32_e64 s[86:87], %[tag], v157\n\t"
        "v_cmp_ne_u32_e64 s[88:89], %[tag], v159\n\t"
        "s_or_b64 s[92:93], s[74:75], s[76:77]\n\t"
        "s_or_b64 s[92:93], s[92:93], s[78:79]\n\t"
        "s_or_b64 s[92:93], s[92:93], s[80:81]\n\t"
        "s_or_b64 s[92:93], s[92:93], s[82:83]\n\t"
        "s_or_b64 s[92:93], s[92:93], s[84:85]\n\t"
        "s_or_b64 s[92:93], s[92:93], s[86:87]\n\t"
        "s_or_b64 s[92:93], s[92:93], s[88:89]\n\t"
        "s_cbranch_scc0 3f\n\t"
        "1:\n\t"
        "s_mov_b64 exec, s[74:75]\n\t"
        "buffer_load_dwordx2 v[144:145], %[vo], %[rs], %[b0] offen offset:0 sc1\n\t"
        "s_mov_b64 exec, s[76:77]\n\t"
        "buffer_load_dwordx2 v[146:147], %[vo], %[rs], %[b0] offen offset:1536 sc1\n\t"
        "s_mov_b64 exec, s[78:79]\n\t"
        "buffer_load_dwordx2 v[148:149], %[vo], %[rs], %[b0] offen offset:3072 sc1\n\t"
        "s_mov_b64 exec, s[80:81]\n\t"
        "buffer_load_dwordx2 v[150:151], %[vo], %[rs], %[b1] offen offset:0 sc1\n\t"
        "s_mov_b64 exec, s[82:83]\n\t"
        "buffer_load_dwordx2 v[152:153], %[vo], %[rs], %[b1] offen offset:1536 sc1\n\t"
        "s_mov_b64 exec, s[84:85]\n\t"
        "buffer_load_dwordx2 v[154:155], %[vo], %[rs], %[b1] offen offset:3072 sc1\n\t"
        "s_mov_b64 exec, s[86:87]\n\t"
        "buffer_load_dwordx2 v[156:157], %[vo], %[rs], %[b2] offen offset:0 sc1\n\t"
        "s_mov_b64 exec, s[88:89]\n\t"
        "buffer_load_dwordx2 v[158:159], %[vo], %[rs], %[b2] offen offset:1536 sc1\n\t"
        "s_mov_b64 exec, -1\n\t"
        "s_sub_u32 %[cap], %[cap], 1\n\t"
        "s_cbranch_scc1 3f\n\t"
        "s_waitcnt vmcnt(0)\n\t"
        "v_cmp_ne_u32_e64 s[74:75], %[tag], v145\n\t"
        "v_cmp_ne_u32_e64 s[76:77], %[tag], v147\n\t"
        "v_cmp_ne_u32_e64 s[78:79], %[tag], v149\n\t"
        "v_cmp_ne_u32_e64 s[80:81], %[tag], v151\n\t"
        "v_cmp_ne_u32_e64 s[82:83], %[tag], v153\n\t"
        "v_cmp_ne_u32_e64 s[84:85], %[tag], v155\n\t"
        "v_cmp_ne_u32_e64 s[86:87], %[tag], v157\n\t"
        "v_cmp_ne_u32_e64 s[88:89], %[tag], v159\n\t"
        "s_or_b64 s[92:93], s[74:75], s[76:77]\n\t"
        "s_or_b64 s[92:93], s[92:93], s[78:79]\n\t"
        "s_or_b64 s[92:93], s[92:93], s[80:81]\n\t"
        "s_or_b64 s[92:93], s[92:93], s[82:83]\n\t"
        "s_or_b64 s[92:93], s[92:93], s[84:85]\n\t"
        "s_or_b64 s[92:93], s[92:93], s[86:87]\n\t"
        "s_or_b64 s[92:93], s[92:93], s[88:89]\n\t"
        "s_cbranch_scc1 1b\n\t"
        "3:\n\t"
        "s_waitcnt vmcnt(0)\n\t"
        "v_mul_f32 %[o], %[w0], v144\n\t"
        "v_mul_f32 v140, %[w4], v152\n\t"
        "v_fmac_f32 %[o], %[w1], v146\n\t"
        "v_fmac_f32 v140, %[w5], v154\n\t"
        "v_fmac_f32 %[o], %[w2], v148\n\t"
        "v_fmac_f32 v140, %[w6], v156\n\t"
        "v_fmac_f32 %[o], %[w3], v150\n\t"
        "v_fmac_f32 v140, %[w7], v158\n\t"
        "v_add_f32 %[o], %[o], v140\n\t"
        : [o] "=&v"(o), [cap] "+&s"(cap), [nap] "+&s"(napn)
        : [vo] "v"(voff), [rs] "s"(rs), [b0] "s"(base), [b1] "s"(base + 4608), [b2] "s"(base + 9216), [tag] "s"(tag), [j] "s"(j), [own] "v"(own),
          [lm] "s"(0xFFFFFFFFFFFFull), [w0] "v"(w0[0]), [w1] "v"(w0[1]), [w2] "v"(w0[2]), [w3] "v"(w0[3]), [w4] "v"(w1[0]), [w5] "v"(w1[1]), [w6] "v"(w1[2]), [w7] "v"(w1[3])
        : "v140", "v144", "v145", "v146", "v147", "v148", "v149", "v150", "v151", "v152", "v153", "v154", "v155", "v156", "v157", "v158", "v159", "s74", "s75", "s76", "s77", "s78", "s79", "s80", "s81", "s82", "s83", "s84", "s85", "s86", "s87", "s88", "s89", "s92", "s93", "scc", "memory");
    return o;
}
template <> __device__ __forceinline__ float combine_o<1>(rsrc_t rs, unsigned voff, unsigned base, unsigned tag, int j, unsigned own, f32x4 w0, f32x4 w1, unsigned napn) {
    unsigned cap = SPIN_GLB;
    float o;
    asm volatile(
        "s_cmp_eq_u32 %[j], 0\n\t"
        "s_cselect_b64 s[74:75], 0, %[lm]\n\t"
        "v_mov_b32 v144, %[own]\n\t"
        "v_mov_b32 v145, %[tag]\n\t"
        "s_cmp_eq_u32 %[j], 1\n\t"
        "s_cselect_b64 s[76:77], 0, %[lm]\n\t"
        "v_mov_b32 v146, %[own]\n\t"
        "v_mov_b32 v147, %[tag]\n\t"
        "s_cmp_eq_u32 %[j], 2\n\t"
        "s_cselect_b64 s[78:79], 0, %[lm]\n\t"
        "v_mov_b32 v148, %[own]\n\t"
        "v_mov_b32 v149, %[tag]\n\t"
        "s_cmp_eq_u32 %[j], 3\n\t"
        "s_cselect_b64 s[80:81], 0, %[lm]\n\t"
        "v_mov_b32 v150, %[own]\n\t"
        "v_mov_b32 v151, %[tag]\n\t"
        "s_cmp_eq_u32 %[j], 4\n\t"
        "s_cselect_b64 s[82:83], 0, %[lm]\n\t"
        "v_mov_b32 v152, %[own]\n\t"
        "v_mov_b32 v153, %[tag]\n\t"
        "s_cmp_eq_u32 %[j], 5\n\t"
        "s_cselect_b64 s[84:85], 0, %[lm]\n\t"
        "v_mov_b32 v154, %[own]\n\t"
        "v_mov_b32 v155, %[tag]\n\t"
        "s_cmp_eq_u32 %[j], 6\n\t"
        "s_cselect_b64 s[86:87], 0, %[lm]\n\t"
        "v_mov_b32 v156, %[own]\n\t"
        "v_mov_b32 v157, %[tag]\n\t"
        "s_cmp_eq_u32 %[j], 7\n\t"
        "s_cselect_b64 s[88:89], 0, %[lm]\n\t"
        "v_mov_b32 v158, %[own]\n\t"
        "v_mov_b32 v159, %[tag]\n\t"
        "s_cmp_eq_u32 %[nap], 0\n\t"
        "s_cbranch_scc1 5f\n\t"
        "4:\n\t"
        "s_sleep 1\n\t"
        "s_sub_u32 %[nap], %[nap], 1\n\t"
        "s_cmp_lg_u32 %[nap], 0\n\t"
        "s_cbranch_scc1 4b\n\t"
        "5:\n\t"
        "s_mov_b64 exec, s[74:75]\n\t"
        "buffer_load_dwordx2 v[144:145], %[vo], %[rs], %[b0] offen offset:0 sc1\n\t"
        "s_mov_b64 exec, s[76:77]\n\t"
        "buffer_load_dwordx2 v[146:147], %[vo], %[rs], %[b0] offen offset:1536 sc1\n\t"
        "s_mov_b64 exec, s[78:79]\n\t"
        "buffer_load_dwordx2 v[148:149], %[vo], %[rs], %[b0] offen offset:3072 sc1\n\t"
        "s_mov_b64 exec, s[80:81]\n\t"
        "buffer_load_dwordx2 v[150:151], %[vo], %[rs], %[b1] offen offset:0 sc1\n\t"
        "s_mov_b64 exec, s[82:83]\n\t"
        "buffer_load_dwordx2 v[152:153], %[vo], %[rs], %[b1] offen offset:1536 sc1\n\t"
        "s_mov_b64 exec, s[84:85]\n\t"
        "buffer_load_dwordx2 v[154:155], %[vo], %[rs], %[b1] offen offset:3072 sc1\n\t"
        "s_mov_b64 exec, s[86:87]\n\t"
        "buffer_load_dwordx2 v[156:157], %[vo], %[rs], %[b2] offen offset:0 sc1\n\t"
        "s_mov_b64 exec, s[88:89]\n\t"
        "buffer_load_dwordx2 v[158:159], %[vo], %[rs], %[b2] offen offset:1536 sc1\n\t"
        "s_mov_b64 exec, -1\n\t"
        "s_waitcnt vmcnt(0)\n\t"
        "v_cmp_ne_u32_e64 s[74:75], %[tag], v145\n\t"
        "v_cmp_ne_u32_e64 s[76:77], %[tag], v147\n\t"
        "v_cmp_ne_u32_e64 s[78:79], %[tag], v149\n\t"
        "v_cmp_ne_u32_e64 s[80:81], %[tag], v151\n\t"
        "v_cmp_ne_u32_e64 s[82:83], %[tag], v153\n\t"
        "v_cmp_ne_u32_e64 s[84:85], %[tag], v155\n\t"
        "v_cmp_ne_u32_e64 s[86:87], %[tag], v157\n\t"
        "v_cmp_ne_u32_e64 s[88:89], %[tag], v159\n\t"
        "s_or_b64 s[92:93], s[74:75], s[76:77]\n\t"
        "s_or_b64 s[92:93], s[92:93], s[78:79]\n\t"
        "s_or_b64 s[92:93], s[92:93], s[80:81]\n\t"
        "s_or_b64 s[92:93], s[92:93], s[82:83]\n\t"
        "s_or_b64 s[92:93], s[92:93], s[84:85]\n\t"
        "s_or_b64 s[92:93], s[92:93], s[86:87]\n\t"
        "s_or_b64 s[92:93], s[92:93], s[88:89]\n\t"
        "s_cbranch_scc0 3f\n\t"
        "s_mov_b64 exec, s[74:75]\n\t"
        "buffer_load_dwordx2 v[144:145], %[vo], %[rs], %[b0] offen offset:0 sc1\n\t"
        "s_mov_b64 exec, s[76:77]\n\t"
        "buffer_load_dwordx2 v[146:147], %[vo], %[rs], %[b0] offen offset:1536 sc1\n\t"
        "s_mov_b64 exec, s[78:79]\n\t"
        "buffer_load_dwordx2 v[148:149], %[vo], %[rs], %[b0] offen offset:3072 sc1\n\t"
        "s_mov_b64 exec, s[80:81]\n\t"
        "buffer_load_dwordx2 v[150:151], %[vo], %[rs], %[b1] offen offset:0 sc1\n\t"
        "s_mov_b64 exec, s[82:83]\n\t"
        "buffer_load_dwordx2 v[152:153], %[vo], %[rs], %[b1] offen offset:1536 sc1\n\t"
        "s_mov_b64 exec, s[84:85]\n\t"
        "buffer_load_dwordx2 v[154:155], %[vo], %[rs], %[b1] offen offset:3072 sc1\n\t"
        "s_mov_b64 exec, s[86:87]\n\t"
        "buffer_load_dwordx2 v[156:157], %[vo], %[rs], %[b2] offen offset:0 sc1\n\t"
        "s_mov_b64 exec, s[88:89]\n\t"
        "buffer_load_dwordx2 v[158:159], %[vo], %[rs], %[b2] offen offset:1536 sc1\n\t"
        "s_mov_b64 exec, -1\n\t"
        "s_sleep 2\n\t"
        "1:\n\t"
        "s_mov_b64 exec, s[74:75]\n\t"
        "buffer_load_dwordx2 v[144:145], %[vo], %[rs], %[b0] offen offset:0 sc1\n\t"
        "s_mov_b64 exec, s[76:77]\n\t"
        "buffer_load_dwordx2 v[146:147], %[vo], %[rs], %[b0] offen offset:1536 sc1\n\t"
        "s_mov_b64 exec, s[78:79]\n\t"
        "buffer_load_dwordx2 v[148:149], %[vo], %[rs], %[b0] offen offset:3072 sc1\n\t"
        "s_mov_b64 exec, s[80:81]\n\t"
        "buffer_load_dwordx2 v[150:151], %[vo], %[rs], %[b1] offen offset:0 sc1\n\t"
        "s_mov_b64 exec, s[82:83]\n\t"
        "buffer_load_dwordx2 v[152:153], %[vo], %[rs], %[b1] offen offset:1536 sc1\n\t"
        "s_mov_b64 exec, s[84:85]\n\t"
        "buffer_load_dwordx2 v[154:155], %[vo], %[rs], %[b1] offen offset:3072 sc1\n\t"
        "s_mov_b64 exec, s[86:87]\n\t"
        "buffer_load_dwordx2 v[156:157], %[vo], %[rs], %[b2] offen offset:0 sc1\n\t"
        "s_mov_b64 exec, s[88:89]\n\t"
        "buffer_load_dwordx2 v[158:159], %[vo], %[rs], %[b2] offen offset:1536 sc1\n\t"
        "s_mov_b64 exec, -1\n\t"
        "s_sub_u32 %[cap], %[cap], 1\n\t"
        "s_cbranch_scc1 3f\n\t"
        "s_waitcnt vmcnt(8)\n\t"
        "v_cmp_ne_u32_e64 s[74:75], %[tag], v145\n\t"
        "v_cmp_ne_u32_e64 s[76:77], %[tag], v147\n\t"
        "v_cmp_ne_u32_e64 s[78:79], %[tag], v149\n\t"
        "v_cmp_ne_u32_e64 s[80:81], %[tag], v151\n\t"
        "v_cmp_ne_u32_e64 s[82:83], %[tag], v153\n\t"
        "v_cmp_ne_u32_e64 s[84:85], %[tag], v155\n\t"
        "v_cmp_ne_u32_e64 s[86:87], %[tag], v157\n\t"
        "v_cmp_ne_u32_e64 s[88:89], %[tag], v159\n\t"
        "s_or_b64 s[92:93], s[74:75], s[76:77]\n\t"
        "s_or_b64 s[92:93], s[92:93], s[78:79]\n\t"
        "s_or_b64 s[92:93], s[92:93], s[80:81]\n\t"
        "s_or_b64 s[92:93], s[92:93], s[82:83]\n\t"
        "s_or_b64 s[92:93], s[92:93], s[84:85]\n\t"
        "s_or_b64 s[92:93], s[92:93], s[86:87]\n\t"
        "s_or_b64 s[92:93], s[92:93], s[88:89]\n\t"
        "s_cbranch_scc1 1b\n\t"
        "3:\n\t"
        "s_waitcnt vmcnt(0)\n\t"
        "v_mul_f32 %[o], %[w0], v144\n\t"
        "v_mul_f32 v140, %[w4], v152\n\t"
        "v_fmac_f32 %[o], %[w1], v146\n\t"
        "v_fmac_f32 v140, %[w5], v154\n\t"
        "v_fmac_f32 %[o], %[w2], v148\n\t"
        "v_fmac_f32 v140, %[w6], v156\n\t"
        "v_fmac_f32 %[o], %[w3], v150\n\t"
        "v_fmac_f32 v140, %[w7], v158\n\t"
        "v_add_f32 %[o], %[o], v140\n\t"
        : [o] "=&v"(o), [cap] "+&s"(cap), [nap] "+&s"(napn)
        : [vo] "v"(voff), [rs] "s"(rs), [b0] "s"(base), [b1] "s"(base + 4608), [b2] "s"(base + 9216), [tag] "s"(tag), [j] "s"(j), [own] "v"(own),
          [lm] "s"(0xFFFFFFFFFFFFull), [w0] "v"(w0[0]), [w1] "v"(w0[1]), [w2] "v"(w0[2]), [w3] "v"(w0[3]), [w4] "v"(w1[0]), [w5] "v"(w1[1]), [w6] "v"(w1[2]), [w7] "v"(w1[3])
        : "v140", "v144", "v145", "v146", "v147", "v148", "v149", "v150", "v151", "v152", "v153", "v154", "v155", "v156", "v157", "v158", "v159", "s74", "s75", "s76", "s77", "s78", "s79", "s80", "s81", "s82", "s83", "s84", "s85", "s86", "s87", "s88", "s89", "s92", "s93", "scc", "memory");
    return o;
}
#define PUMP 0
__device__ __forceinline__ s16x4 trd(LAS unsigned char* L, unsigned off) { return __builtin_amdgcn_ds_read_tr16_b64_v4i16((LAS s16x4*)(L + off)); }
__device__ __forceinline__ bf16x8 cat8(s16x4 a, s16x4 b) { return __builtin_bit_cast(bf16x8, __builtin_shufflevector(a, b, 0, 1, 2, 3, 4, 5, 6, 7)); }
__device__ __forceinline__ f32x4 mfma32(bf16x8 a, bf16x8 b, f32x4 c) { return __builtin_amdgcn_mfma_f32_16x16x32_bf16(a, b, c, 0, 0, 0); }
__device__ __forceinline__ bf16x8 asb(v4u a) { return __builtin_bit_cast(bf16x8, a); }

__device__ __forceinline__ void spin_rd(int c, unsigned n, unsigned a, bf16x8 (&o)[1]) {
    unsigned f, cap = SPIN_LDS;
    const unsigned fa = L_CNT + 4 * c;
    asm volatile(
        "1:\n\t"
        "ds_read_b32 %[f], %[fa]\n\t"
        "ds_read_b128 %[o0], %[a] offset:0\n\t"
        "s_sub_u32 %[cap], %[cap], 1\n\t"
        "s_cbranch_scc1 2f\n\t"
        "s_waitcnt lgkmcnt(1)\n\t"
        "v_readfirstlane_b32 s92, %[f]\n\t"
        "s_cmp_lt_u32 s92, %[n]\n\t"
        "s_cbranch_scc1 1b\n\t"
        "2:\n\t"
        "s_waitcnt lgkmcnt(0)"
        : [f] "=&v"(f), [cap] "+&s"(cap), [o0] "=&v"(o[0]) : [fa] "v"(fa), [a] "v"(a), [n] "s"(n) : "s92", "scc", "memory");
}
__device__ __forceinline__ void spin_rd(int c, unsigned n, unsigned a, bf16x8 (&o)[3]) {
    unsigned f, cap = SPIN_LDS;
    const unsigned fa = L_CNT + 4 * c;
    asm volatile(
        "1:\n\t"
        "ds_read_b32 %[f], %[fa]\n\t"
        "ds_read_b128 %[o0], %[a] offset:0\n\t"
        "ds_read_b128 %[o1], %[a] offset:64\n\t"
        "ds_read_b128 %[o2], %[a] offset:128\n\t"
        "s_sub_u32 %[cap], %[cap], 1\n\t"
        "s_cbranch_scc1 2f\n\t"
        "s_waitcnt lgkmcnt(3)\n\t"
        "v_readfirstlane_b32 s92, %[f]\n\t"
        "s_cmp_lt_u32 s92, %[n]\n\t"
        "s_cbranch_scc1 1b\n\t"
        "2:\n\t"
        "s_waitcnt lgkmcnt(0)"
        : [f] "=&v"(f), [cap] "+&s"(cap), [o0] "=&v"(o[0]), [o1] "=&v"(o[1]), [o2] "=&v"(o[2]) : [fa] "v"(fa), [a] "v"(a), [n] "s"(n) : "s92", "scc", "memory");
}
__device__ __forceinline__ void spin_rd(int c, unsigned n, unsigned a, bf16x8 (&o)[6]) {
    unsigned f, cap = SPIN_LDS;
    const unsigned fa = L_CNT + 4 * c;
    asm volatile(
        "1:\n\t"
        "ds_read_b32 %[f], %[fa]\n\t"
        "ds_read_b128 %[o0], %[a] offset:0\n\t"
        "ds_read_b128 %[o1], %[a] offset:64\n\t"
        "ds_read_b128 %[o2], %[a] offset:128\n\t"
        "ds_read_b128 %[o3], %[a] offset:192\n\t"
        "ds_read_b128 %[o4], %[a] offset:256\n\t"
        "ds_read_b128 %[o5], %[a] offset:320\n\t"
        "s_sub_u32 %[cap], %[cap], 1\n\t"
        "s_cbranch_scc1 2f\n\t"
        "s_waitcnt lgkmcnt(6)\n\t"
        "v_readfirstlane_b32 s92, %[f]\n\t"
        "s_cmp_lt_u32 s92, %[n]\n\t"
        "s_cbranch_scc1 1b\n\t"
        "2:\n\t"
        "s_waitcnt lgkmcnt(0)"
        : [f] "=&v"(f), [cap] "+&s"(cap), [o0] "=&v"(o[0]), [o1] "=&v"(o[1]), [o2] "=&v"(o[2]), [o3] "=&v"(o[3]), [o4] "=&v"(o[4]), [o5] "=&v"(o[5]) : [fa] "v"(fa), [a] "v"(a), [n] "s"(n) : "s92", "scc", "memory");
}
template <int OFF> __device__ __forceinline__ void spin_tr(int c, unsigned n, const unsigned (&a)[3], s16x4 (&o)[6]) {
    unsigned f, cap = SPIN_LDS;
    const unsigned fa = L_CNT + 4 * c;
    asm volatile(
        "1:\n\t"
        "ds_read_b32 %[f], %[fa]\n\t"
        "ds_read_b64_tr_b16 %[o0], %[a0] offset:%c[off0]\n\t"
        "ds_read_b64_tr_b16 %[o1], %[a0] offset:%c[off1]\n\t"
        "ds_read_b64_tr_b16 %[o2], %[a1] offset:%c[off0]\n\t"
        "ds_read_b64_tr_b16 %[o3], %[a1] offset:%c[off1]\n\t"
        "ds_read_b64_tr_b16 %[o4], %[a2] offset:%c[off0]\n\t"
        "ds_read_b64_tr_b16 %[o5], %[a2] offset:%c[off1]\n\t"
        "s_sub_u32 %[cap], %[cap], 1\n\t"
        "s_cbranch_scc1 2f\n\t"
        "s_waitcnt lgkmcnt(6)\n\t"
        "v_readfirstlane_b32 s92, %[f]\n\t"
        "s_cmp_lt_u32 s92, %[n]\n\t"
        "s_cbranch_scc1 1b\n\t"
        "2:\n\t"
        "s_waitcnt lgkmcnt(0)"
        : [f] "=&v"(f), [cap] "+&s"(cap), [o0] "=&v"(o[0]), [o1] "=&v"(o[1]), [o2] "=&v"(o[2]), [o3] "=&v"(o[3]), [o4] "=&v"(o[4]), [o5] "=&v"(o[5])
        : [fa] "v"(fa), [a0] "v"(a[0]), [a1] "v"(a[1]), [a2] "v"(a[2]), [n] "s"(n), [off0] "n"(OFF), [off1] "n"(OFF + 1536) : "s92", "scc", "memory");
}
__device__ __forceinline__ void ln_stats(const unsigned (&xp)[5], float& mb, float& rden) {
    const float c = lo16(__builtin_amdgcn_readfirstlane(xp[0]));
    float s = 0.f, s2 = 0.f;
#pragma unroll
    for (int i = 0; i < 5; ++i) {
        const float a = lo16(xp[i]) - c, b = hi16(xp[i]) - c;
        s += a + b; s2 = fmaf(a, a, s2); s2 = fmaf(b, b, s2);
    }
    s = wave_sum(s); s2 = wave_sum(s2);
    const float dm = s * (1.0f / D);
    const float var = fmaxf(s2 * (1.0f / D) - dm * dm, 0.f);
    mb = rbf(c + dm);
    const float den = rbf(__builtin_amdgcn_sqrtf(rbf(rbf(var) + 1e-5f)));
    rden = __builtin_amdgcn_rcpf(den);
}
__device__ __forceinline__ void ln_stats_fast(const unsigned (&xp)[5], float& mb, float& rden) {
    float s = dot2(xp[0], 0x3F803F80u, 0.f), s2 = dot2(xp[0], xp[0], 0.f);
#pragma unroll
    for (int i = 1; i < 5; ++i) { s = dot2(xp[i], 0x3F803F80u, s); s2 = dot2(xp[i], xp[i], s2); }
    auto w = __builtin_amdgcn_permlane32_swap(__float_as_uint(s), __float_as_uint(s2), false, false);
    float t = fv(w[0]) + fv(w[1]);
    t += dpp<XOR1>(t); t += dpp<XOR2>(t); t += dpp<XOR7>(t); t += dpp<XOR8>(t);
    t = sw16_sum(t) * (1.0f / D);
    auto u = __builtin_amdgcn_permlane32_swap(__float_as_uint(t), __float_as_uint(t), false, false);
    const float mean = fv(u[0]);
    const float var = fmaxf(fmaf(-mean, mean, fv(u[1])), 0.f);
    mb = rbf(mean);
    rden = __builtin_amdgcn_rcpf(rbf(__builtin_amdgcn_sqrtf(rbf(rbf(var) + 1e-5f))));
}
__device__ __forceinline__ unsigned ln_pair(unsigned w, float mb, float rden) {
    const unsigned d = pk2(lo16(w) - mb, hi16(w) - mb);
    return pk2(lo16(d) * rden, hi16(d) * rden);
}

template <int N> __device__ __forceinline__ unsigned reduce_cols(rsrc_t rs, unsigned voff, unsigned base, unsigned tag, unsigned res) {
    float r0 = lo16(res), r1 = hi16(res);
    PIN(r0); PIN(r1);
    float s0, s1;
    sum_pairs<N, PUMP>(rs, voff, base, tag, s0, s1);
    auto sw = __builtin_amdgcn_permlane32_swap(__float_as_uint(s0), __float_as_uint(s1), false, false);
    float t = fv(sw[0]) + fv(sw[1]);
    t += dpp<XOR8>(t);
    t = sw16_sum(t);
    auto s2 = __builtin_amdgcn_permlane32_swap(__float_as_uint(t), __float_as_uint(t), false, false);
    const unsigned sp = pk2(fv(s2[0]), fv(s2[1]));
    return pk2(r0 + lo16(sp), r1 + hi16(sp));
}
__device__ __forceinline__ float fold5(const v4u (&p)[5]) {
    const float a0 = ((fv(p[0][0]) + fv(p[1][0])) + (fv(p[2][0]) + fv(p[3][0]))) + fv(p[4][0]);
    const float a1 = ((fv(p[0][2]) + fv(p[1][2])) + (fv(p[2][2]) + fv(p[3][2]))) + fv(p[4][2]);
    auto sw = __builtin_amdgcn_permlane32_swap(__float_as_uint(a0), __float_as_uint(a1), false, false);
    return fv(sw[0]) + fv(sw[1]);
}
__device__ __forceinline__ unsigned lateness(rsrc_t rs, int lane, u64 t_start) {
    const v2u t = __builtin_amdgcn_raw_buffer_load_b64(rs, WS_T0 + (lane & 7) * 128, 0, 16);
    const u64 d = t_start - (((u64)t[1] << 32) | t[0]);
    float l = d < 150ull ? (float)(unsigned)d : 0.f;
    l = fmaxf(l, dpp<XOR1>(l)); l = fmaxf(l, dpp<XOR2>(l)); l = fmaxf(l, dpp<XOR7>(l));
    return __builtin_amdgcn_readfirstlane((unsigned)l);
}
__device__ __forceinline__ void observe_ap(rsrc_t rs, int lane, int cg, unsigned tag) {
    v2u o[1] = {v2u{0u, lane < 32 ? ~tag : tag}};
    gather<1, 2>(rs, lane * 128, WS_AP + cg * 4096, 0, tag, o);
}
#define STAMP(i) do { } while (0)

#define LD_REPORT \
    "s_getreg_b32 s92, hwreg(HW_REG_IB_STS)\n\t" \
    "s_and_b32 s93, s92, 15\n\t" \
    "s_bfe_u32 s92, s92, 0x20016\n\t" \
    "s_lshl4_add_u32 s93, s92, s93\n\t" \
    "s_sub_u32 s92, %[iss], s93\n\t" \
    "v_mov_b32 %[vt], s92\n\t"
#define LD_WRITE \
    "s_mov_b64 exec, 1\n\t" \
    "ds_write_b32 %[va], %[vt]\n\t" \
    "s_mov_b64 exec, -1\n\t"
struct Ld { unsigned issued, tnext, pace, va, sh, w0; };
template <bool FLIP, int POL = 0> __device__ __forceinline__ void ld_stream(Ld& s, rsrc_t rs, unsigned (&o)[6], unsigned soff, unsigned sstep, unsigned dst, int ng, unsigned wrap = ~0u) {
    unsigned vt;
    const unsigned end = __builtin_amdgcn_readfirstlane(s.issued + 6 * ng);
    soff = __builtin_amdgcn_readfirstlane(soff); dst = __builtin_amdgcn_readfirstlane(dst);
    s.issued = __builtin_amdgcn_readfirstlane(s.issued); s.tnext = __builtin_amdgcn_readfirstlane(s.tnext); s.pace = __builtin_amdgcn_readfirstlane(s.pace);
    asm volatile(
        "s_memrealtime s[90:91]\n\t"
        "1:\n\t"
        LD_REPORT
        "s_waitcnt lgkmcnt(0)\n\t"
        "s_lshl_b32 s92, s90, 2\n\t"
        "s_memrealtime s[90:91]\n\t"
        LD_WRITE
        "s_sub_u32 s92, s92, %[tn]\n\t"
        "s_cmp_lt_i32 s92, 0\n\t"
        "s_cbranch_scc1 1b\n\t"
        "s_lshr_b32 s92, s92, %[sh]\n\t"
        "s_add_u32 s92, s92, %[w0]\n\t"
        "s_min_u32 s92, s92, %[wm]\n\t"
        "s_cmp_gt_u32 s93, s92\n\t"
        "s_cbranch_scc1 1b\n\t"
        "s_mov_b32 m0, %[dst]\n\t"
        "s_add_u32 %[tn], %[tn], %[pace]\n\t"
        ".if %c[pol]\n\tbuffer_load_dwordx4 %[o0], %[rs], %[so] offen lds\n\t.else\n\tbuffer_load_dwordx4 %[o0], %[rs], %[so] offen nt lds\n\t.endif\n\t"
        ".if %c[pol]\n\tbuffer_load_dwordx4 %[o1], %[rs], %[so] offen offset:1024 lds\n\t.else\n\tbuffer_load_dwordx4 %[o1], %[rs], %[so] offen offset:1024 nt lds\n\t.endif\n\t"
        ".if %c[pol]\n\tbuffer_load_dwordx4 %[o2], %[rs], %[so] offen offset:2048 lds\n\t.else\n\tbuffer_load_dwordx4 %[o2], %[rs], %[so] offen offset:2048 nt lds\n\t.endif\n\t"
        "s_add_u32 m0, %[dst], 3072\n\t"
        "s_add_u32 %[iss], %[iss], 6\n\t"
        ".if %c[pol]\n\tbuffer_load_dwordx4 %[o3], %[rs], %[so] offen lds\n\t.else\n\tbuffer_load_dwordx4 %[o3], %[rs], %[so] offen nt lds\n\t.endif\n\t"
        ".if %c[pol]\n\tbuffer_load_dwordx4 %[o4], %[rs], %[so] offen offset:1024 lds\n\t.else\n\tbuffer_load_dwordx4 %[o4], %[rs], %[so] offen offset:1024 nt lds\n\t.endif\n\t"
        ".if %c[pol]\n\tbuffer_load_dwordx4 %[o5], %[rs], %[so] offen offset:2048 lds\n\t.else\n\tbuffer_load_dwordx4 %[o5], %[rs], %[so] offen offset:2048 nt lds\n\t.endif\n\t"
        "s_add_u32 %[dst], %[dst], 6144\n\t"
        "s_add_u32 %[so], %[so], %[ss]\n\t"
        "s_cmp_eq_u32 %[so], %[wrap]\n\t"
        "s_cselect_b32 %[so], 0, %[so]\n\t"
        ".if %c[flip]\n\t"
        "v_xor_b32 %[o0], 0x80, %[o0]\n\tv_xor_b32 %[o1], 0x80, %[o1]\n\tv_xor_b32 %[o2], 0x80, %[o2]\n\t"
        "v_xor_b32 %[o3], 0x80, %[o3]\n\tv_xor_b32 %[o4], 0x80, %[o4]\n\tv_xor_b32 %[o5], 0x80, %[o5]\n\t"
        ".endif\n\t"
        "s_cmp_lg_u32 %[iss], %[end]\n\t"
        "s_cbranch_scc1 1b\n\t"
        "s_waitcnt lgkmcnt(0)"
        : [iss] "+&s"(s.issued), [tn] "+&s"(s.tnext), [dst] "+&s"(dst), [so] "+&s"(soff), [vt] "=&v"(vt),
          [o0] "+&v"(o[0]), [o1] "+&v"(o[1]), [o2] "+&v"(o[2]), [o3] "+&v"(o[3]), [o4] "+&v"(o[4]), [o5] "+&v"(o[5])
        : [rs] "s"(rs), [ss] "s"(sstep), [end] "s"(end), [pace] "s"(s.pace), [va] "v"(s.va), [wrap] "s"(wrap), [wm] "n"(WMAX - 6), [sh] "s"(s.sh), [w0] "s"(s.w0), [flip] "n"(FLIP ? 1 : 0), [pol] "n"(POL)
        : "s90", "s91", "s92", "s93", "scc", "memory");
    s.issued = end;
    s.tnext = __builtin_amdgcn_readfirstlane(s.tnext);
}
__device__ __forceinline__ void ld_gate(Ld& s, int c, unsigned n) {
    unsigned vt, vg, cap = SPIN_LDS;
    const unsigned ga = L_CNT + 4 * c;
    asm volatile(
        "1:\n\t"
        "ds_read_b32 %[vg], %[ga]\n\t"
        LD_REPORT
        LD_WRITE
        "s_sub_u32 %[cap], %[cap], 1\n\t"
        "s_cbranch_scc1 2f\n\t"
        "s_waitcnt lgkmcnt(1)\n\t"
        "v_readfirstlane_b32 s92, %[vg]\n\t"
        "s_cmp_lt_u32 s92, %[n]\n\t"
        "s_cbranch_scc1 1b\n\t"
        "2:\n\t"
        "s_waitcnt lgkmcnt(0)"
        : [vt] "=&v"(vt), [vg] "=&v"(vg), [cap] "+&s"(cap)
        : [iss] "s"(s.issued), [va] "v"(s.va), [ga] "v"(ga), [n] "s"(n)
        : "s92", "s93", "scc", "memory");
}
__device__ __forceinline__ void ld_drain(Ld& s) {
    unsigned vt;
    asm volatile(
        "1:\n\t"
        LD_REPORT
        LD_WRITE
        "s_cmp_lg_u32 s93, 0\n\t"
        "s_cbranch_scc1 1b\n\t"
        : [vt] "=&v"(vt) : [iss] "s"(s.issued), [va] "v"(s.va) : "s92", "s93", "scc", "memory");
}

__global__ void __launch_bounds__(640) k_block(const u16* __restrict__ x, const u16* __restrict__ kc, const u16* __restrict__ vc,
                                               const u16* __restrict__ wq, const u16* __restrict__ wo, const u16* __restrict__ wg,
                                               const u16* __restrict__ wu, const u16* __restrict__ wd, u16* __restrict__ out,
                                               unsigned char* ws KNARG) {
    extern __shared__ __attribute__((aligned(16))) unsigned char lds_raw[];
    LAS unsigned char* L = (LAS unsigned char*)lds_raw;
    unsigned ka0, ka1;
    asm volatile("s_load_dword %0, %2, 0x0\n\ts_load_dword %1, %2, 0x40" : "=&s"(ka0), "=&s"(ka1) : "s"(__builtin_amdgcn_kernarg_segment_ptr()));
#define CODEPF 19400
    if (__builtin_expect((unsigned)__builtin_amdgcn_readfirstlane(threadIdx.x >> 6) == 8u, 1)) {
        const rsrc_t rc = mkrs((const void*)(__builtin_amdgcn_s_getpc() & ~255ull), CODEPF);
        asm volatile("s_mov_b32 m0, %0\n\ts_nop 0\n\tbuffer_load_dwordx4 %1, %2, %3 offen lds" :: "s"(L_JUNK), "v"((threadIdx.x & 63) * 16), "s"(rc), "s"((blockIdx.x >> 3) * 1024) : "memory");
    }
    if (threadIdx.x < 20) ((LAS unsigned*)(L + L_CNT))[threadIdx.x] = 0u;
    const int b = blockIdx.x, xcd = b & 7, idx = b >> 3;
    const int h = xcd * 4 + (idx >> 3), j = idx & 7;
    const int tid = threadIdx.x, lane = tid & 63, wave = __builtin_amdgcn_readfirstlane(tid >> 6);
    const int g = lane >> 4, tq = (lane & 15) >> 2, tp = lane & 3, i16 = lane & 15;
    const bool qcu = j >= 2;
    const int qr = (j + 3) >> 3, qc = j - 2 - 3 * qr;
    const int gi = (idx >> 3) * 2 + j;
    const int gcu = (!qcu && gi < 6) ? xcd * 6 + gi : -1;
    const bool pre = qcu || gcu >= 0;
    const unsigned d0 = g * 256 + (i16 ^ (g << 1)) * 16;
    unsigned q0 = qcu ? (lane >> 3) * 12288 + ((lane & 7) ^ (g & 1) * 2) * 16 : d0, q1 = qcu ? (q0 ^ 64) + 98304 - 1024 : d0;
    unsigned q2 = qcu ? q0 + 2 * (98304 - 1024) : d0 ^ 128, q3 = qcu ? q1 + 2 * (98304 - 1024) : d0 ^ 128;
    unsigned qs = qcu ? 393216 : 4096, qb = qcu ? (320 * qr * (H * DH) + h * DH + 64 * qc) * 2 : 32 * gcu * D * 2;
    unsigned qn = (wave == 4 && pre) ? 10 : 0;
    qs = __builtin_amdgcn_readfirstlane(qs); qb = __builtin_amdgcn_readfirstlane(qb); qn = __builtin_amdgcn_readfirstlane(qn);
    asm volatile("" : "+v"(q0), "+v"(q1), "+v"(q2), "+v"(q3));
    asm volatile("" : "+s"(qs), "+s"(qb), "+s"(qn));
    SB();
    asm volatile("s_waitcnt lgkmcnt(0)\n\ts_barrier" :: "s"(ka0), "s"(ka1), "s"(x), "s"(kc), "s"(vc), "s"(wq), "s"(wo), "s"(wg), "s"(wu), "s"(wd), "s"(out), "s"(ws) : "memory");
    const u64 t_start = __builtin_amdgcn_s_memrealtime();
    if (__builtin_expect(qn != 0, 1)) {
        const rsrc_t rq = mkrs(qcu ? wq : wd, qcu ? D * H * DH * 2 : F * D * 2);
#pragma unroll 1
        for (unsigned n = 0; n < 10; ++n, qb += qs, nap(KN(20)))
            asm volatile("s_mov_b32 m0, %0\n\ts_nop 0\n\tbuffer_load_dwordx4 %1, %5, %6 offen nt lds\n\tbuffer_load_dwordx4 %2, %5, %6 offen offset:1024 nt lds\n\t"
                         "buffer_load_dwordx4 %3, %5, %6 offen offset:2048 nt lds\n\tbuffer_load_dwordx4 %4, %5, %6 offen offset:3072 nt lds"
                         :: "s"(L_W + n * 4096), "v"(q0), "v"(q1), "v"(q2), "v"(q3), "s"(rq), "s"(qb) : "memory");
        waitvm<0>();
        lds_signal(C_WQ);
    }
    unsigned tag, xp[5];
    asm volatile("" : "=v"(xp[0]), "=v"(xp[1]), "=v"(xp[2]), "=v"(xp[3]), "=v"(xp[4]), "=v"(tag));
    if (__builtin_expect(wave == 9, 1)) {
        const unsigned xo = lane * 4, zo = 0;
        asm volatile("global_load_dword %0, %6, %7\n\tglobal_load_dword %1, %6, %7 offset:256\n\tglobal_load_dword %2, %6, %7 offset:512\n\t"
                     "global_load_dword %3, %6, %7 offset:768\n\tglobal_load_dword %4, %6, %7 offset:1024\n\tglobal_load_dword %5, %8, %9 sc1"
                     : "+v"(xp[0]), "+v"(xp[1]), "+v"(xp[2]), "+v"(xp[3]), "+v"(xp[4]), "+v"(tag) : "v"(xo), "s"(x), "v"(zo), "s"(ws) : "memory");
    }
    const rsrc_t rws = mkrs(ws, WS_END);
    if (__builtin_expect(wave == 7 && idx == 0, 1)) __builtin_amdgcn_raw_buffer_store_b64(v2u{(unsigned)t_start, (unsigned)(t_start >> 32)}, rws, lane == 0 ? WS_T0 + xcd * 128 : OOB, 0, 16);
    const bool wocu = j < 5;
    const int oi = (idx >> 3) * 3 + (j - 2), oc = (idx >> 3) * 3 + (j - 5);
    const int ocu = (wocu && qcu && oi < 5) ? xcd * 5 + oi : -1;
    const int n1 = __builtin_amdgcn_readfirstlane(!pre ? N1G + 4 : wocu ? N1G : 0);
    const int pv0 = P_K + 6 * n1;
    const f32x4 z4 = {0.f, 0.f, 0.f, 0.f};
    auto sel4 = [&](float a0, float a1, float a2, float a3) {
        const float lo = (lane & 16) ? a1 : a0, hi = (lane & 16) ? a3 : a2;
        return (lane & 32) ? hi : lo;
    };
    auto t_end = [&]() { return t_start - __builtin_amdgcn_readfirstlane(*(volatile LAS unsigned*)(L + L_CNT + 4 * C_LATE)) + TE; };
    auto make_h = [&](int a, unsigned xres) {
        unsigned ha = L_O + (8 * a + (lane & 7)) * 4, ho = (lane < 8 && oc < 8) ? WS_HB + oc * 4096 + (40 * xcd + 8 * a + lane) * 8 : OOB;
        PIN(ha); PIN(ho);
        lds_wait<true>(L, C_OWN, 4);
        delay(D2);
        until(t_end() + KN(19));
        lds_wait<true>(L, C_PROG, P_WO);
        const unsigned hp = reduce_cols<4>(rws, lane * 16, WS_AP + (5 * xcd + a) * 4096, tag, xres);
        put1(rws, ho, tag, hp);
        *(LAS unsigned*)(L + ha) = hp;
        lds_signal(C_O);
    };

    if (__builtin_expect(wave == 0, 1)) {
        __builtin_amdgcn_s_setprio(3);
        STAMP(14);
        unsigned ko[6], vo[6], po[6];
#pragma unroll
        for (int c = 0; c < 3; ++c) {
            const unsigned P = 64 * c + lane, r = P / 24, ch = P - r * 24;
            ko[c] = r * 384 + (ch ^ (r >> 1)) * 16 - c * 1024; ko[c + 3] = (ko[c] ^ 64) + 3072;
        }
        const size_t kvoff = (KN7 & 32) ? ((size_t)idx * SK + (SK - W) + xcd * 256) * DH : ((size_t)h * SK + (SK - W) + j * 256) * DH;
        Ld s;
        s.issued = 0; s.va = L_CNT + 4 * C_PROG; s.w0 = KN(22); s.sh = KN(23);
        nap(KN(21));
        MEMBAR();
        const unsigned late = (KN7 & 64) ? 0u : lateness(rws, lane, t_start);
        lds_set(C_LATE, late);
        s.pace = ((unsigned)(TE - (pre ? TAQ : TA0) - late) * (!pre ? 262144 / (35 + N1G) : wocu ? 262144 / (31 + N1G) : 262144 / 31)) >> 16;
        s.tnext = ((unsigned)t_start + (pre ? TAQ : TA0)) * 4;
        const unsigned rot = 0;
        ld_stream<false, POLK>(s, mkrs(kc + kvoff, 98304), ko, rot, 6144, L_V, 16, 98304);
        STAMP(4);
#pragma unroll
        for (int c = 0; c < 3; ++c) {
            const unsigned P = 64 * c + lane, r = P / 24, ch = P - r * 24;
            vo[c] = r * 384 + (ch ^ (r & 2u)) * 16 - c * 1024; vo[c + 3] = (vo[c] ^ 64) + 3072;
            const unsigned wb = (i16 ^ (g << 1)) * 16;
            const unsigned rst = wocu ? 1280 : 3072;
            po[c] = (4 * c + g) * rst + (wb ^ ((c >> 1) << 7)) - c * 1024;
            po[c + 3] = (4 * c + 12 + g) * rst + (wb ^ ((((c + 3) >> 1) & 1) << 7)) - c * 1024;
        }
        const rsrc_t rv = mkrs(vc + kvoff, 98304);
        const rsrc_t ro = mkrs(wo + (size_t)(h * DH) * D + j * 128, 191 * 1280 + 256);
        ld_gate(s, C_QP, __builtin_amdgcn_readfirstlane(qcu ? 4 : pre ? 8 : 0));
        if (n1) ld_stream<true, POLW>(s, ro, po, 0, 30720, L_W, n1);
        ld_gate(s, C_KA, 4);
        ld_stream<false, POLV>(s, rv, vo, rot, 6144, L_V, 8, 98304);
        ld_gate(s, C_KB, 4);
        ld_stream<false, POLV>(s, rv, vo, rot >= 49152 ? rot - 49152 : rot + 49152, 6144, L_V + 49152, 8, 98304);
        STAMP(6);
        s.pace = KN(15); s.w0 = KN(17);
        if (wocu) {
            if (n1 < 8) ld_stream<true, POLW>(s, ro, po, n1 * 30720, 30720, L_W + n1 * 6144, 8 - n1);
        } else {
            const rsrc_t r0 = mkrs(wg + (size_t)(80 * xcd) * F + 128 * oc, 79 * 3072 + 256), r1 = mkrs(wu + (size_t)(80 * xcd) * F + 128 * oc, 79 * 3072 + 256);
            s.tnext += FDELAY * 4; s.w0 = KN(16);
            ld_stream<true, POLW>(s, r0, po, 0, 73728, L_W, 4);
            ld_stream<true, POLW>(s, r1, po, 0, 73728, L_W + 24576, 4);
        }
        ld_drain(s);
        if (!wocu) STAMP(17); else STAMP(9);
        tag = __builtin_amdgcn_readfirstlane(*(LAS unsigned*)(L + L_TAG));
        if (gcu >= 0) {
            unsigned ha = L_HH + (lane & 31) * 2;
            PIN(ha);
            lds_wait<true>(L, C_GO, 1);
            STAMP(18);
            delay(D3);
            float ga0, ga1;
            sum_pairs<4, PUMP>(rws, lane * 16, WS_GP + gcu * 4096, tag, ga0, ga1);
            f32x4 st = *(volatile LAS f32x4*)(L + L_ST);
            float cs = *(volatile LAS float*)(L + L_LN + lane * 4);
            STAMP(12);
            auto sw0 = __builtin_amdgcn_permlane32_swap(__float_as_uint(ga0), __float_as_uint(ga1), false, false);
            float sv = fv(sw0[0]) + fv(sw0[1]);
            PIN(sv);
#pragma unroll 1
            for (unsigned sp = 0; sp < SPIN_LDS && __builtin_amdgcn_readfirstlane(__float_as_uint(st[2]) & __float_as_uint(st[3])) == 0u; ++sp) {
                st = *(volatile LAS f32x4*)(L + L_ST);
                cs = *(volatile LAS float*)(L + L_LN + lane * 4);
            }
            const float v = rbf((sv - st[0] * cs) * st[1]);
            auto sw = __builtin_amdgcn_permlane32_swap(__float_as_uint(v), __float_as_uint(v), false, false);
            const float sg = rbf(__builtin_amdgcn_rcpf(1.0f + __expf(-fv(sw[0]))));
            *(LAS u16*)(L + ha) = (u16)bfbits(sg * fv(sw[1]));
            lds_signal(C_HH);
        } else if (ocu >= 0) {
            const rsrc_t rout = mkrs(out, 1280);
            unsigned oo = lane < 8 ? (8 * ocu + lane) * 4 : OOB;
            PIN(oo);
            lds_wait<true>(L, C_O, 4);
            v2u hres[1] = {v2u{0u, ~tag}};
            delay(DOBS + D3);
            gather<1, 4>(rws, (lane & 7) * 8, WS_HB + xcd * 4096 + 64 * ocu, 0, tag, hres);
            delay(D4);
            STAMP(22);
            const unsigned ov = reduce_cols<6>(rws, lane * 16, WS_DP + ocu * 6144, tag, hres[0][0]);
            __builtin_amdgcn_raw_buffer_store_b32(ov, rout, oo, 0, 0);
            if (ocu == 0 && lane == 0) __hip_atomic_store((gu32*)(ws + WS_EPOCH), tag, RLX_AGENT);
            STAMP(16);
        }
    } else if (wave == 9) {
        asm volatile("s_waitcnt vmcnt(0)" : "+v"(xp[0]), "+v"(xp[1]), "+v"(xp[2]), "+v"(xp[3]), "+v"(xp[4]), "+v"(tag) :: "memory");
        float mb, rden;
        ln_stats(xp, mb, rden);
#pragma unroll
        for (int i = 0; i < 5; ++i) *(LAS unsigned*)(L + L_LN + (lane + 64 * i) * 4) = ln_pair(xp[i], mb, rden);
        tag = __builtin_amdgcn_readfirstlane(tag) + 1u;
        if (lane == 0) *(LAS unsigned*)(L + L_TAG) = tag;
        lds_signal(C_X);
        STAMP(23);
        {
            until(t_start + TQ);
            v4u qp[3];
#pragma unroll
            for (int k = 0; k < 3; ++k) qp[k] = v4u{0u, ~tag, 0u, ~tag};
            if (!(KN7 & 1)) gather<1, 8>(rws, lane * 16, WS_QP + h * 3072, 1024, tag, qp);
#pragma unroll
            for (int k = 0; k < 3; ++k) *(LAS unsigned*)(L + L_Q + k * 128 + (lane & 31) * 4) = pk2(sw32_sum(fv(qp[k][0])), sw32_sum(fv(qp[k][2])));
            lds_signal(C_Q);
            STAMP(3);
        }
        lds_wait<true>(L, C_P, 4);
        const f32x4 mx = *(LAS f32x4*)(L + L_MX), ls = *(LAS f32x4*)(L + L_MX + 16);
        const float m = fmaxf(fmaxf(mx[0], mx[1]), fmaxf(mx[2], mx[3]));
        const float l = (ls[0] + ls[1]) + (ls[2] + ls[3]);
        if (!(KN7 & 8)) put2(rws, lane == 0 ? WS_ST + (h * 8 + j) * 128 : OOB, tag, m, l);
        if (wocu) {
            until(t_start - __builtin_amdgcn_readfirstlane(*(volatile LAS unsigned*)(L + L_CNT + 4 * C_LATE)) + TS);
            const unsigned tt = lane < 8 ? ~tag : tag;
            v4u st[1] = {v4u{0u, tt, 0u, tt}};
            if (!(KN7 & 4)) gather<1, 8>(rws, lane * 128, WS_ST + h * 1024, 0, tag, st);
            const float ms = fv(st[0][0]);
            float M = fmaxf(ms, dpp<XOR1>(ms)); M = fmaxf(M, dpp<XOR2>(M)); M = fmaxf(M, dpp<XOR7>(M));
            const float e = __expf(ms - M);
            float Ls = e * fv(st[0][2]);
            Ls += dpp<XOR1>(Ls); Ls += dpp<XOR2>(Ls); Ls += dpp<XOR7>(Ls);
            if (lane < 8) *(LAS float*)(L + L_WGT + lane * 4) = e * __builtin_amdgcn_rcpf(Ls);
            lds_signal(C_W);
            STAMP(19);
            if (gcu >= 0) {
                lds_wait<true>(L, C_O, 4);
                if (KN7 & 256) { delay(KN(13)); until(t_end() + KN(19)); observe_ap(rws, lane, 5 * xcd + (gi & 3), tag); } else delay(DOBS);
                lds_signal(C_GO);
                delay(D3 - 22);
                v2u hv[5];
#pragma unroll
                for (int i = 0; i < 5; ++i) hv[i] = v2u{0u, ~tag};
                gather<1, 0>(rws, lane * 8, WS_HB + xcd * 4096, 512, tag, hv);
#pragma unroll
                for (int i = 0; i < 5; ++i) xp[i] = hv[i][0];
                ln_stats_fast(xp, mb, rden);
                if (lane == 0) { *(LAS v2u*)(L + L_ST) = v2u{__float_as_uint(mb), __float_as_uint(rden)}; *(LAS float*)(L + L_ST + 8) = 1.0f; }
                STAMP(15);
            }
        } else {
            make_h(4, ((const unsigned*)x)[40 * xcd + 32 + (lane & 7)]);
        }
    } else {
        bf16x8 dw[5];
        if (gcu >= 0) {
            lds_wait<true>(L, C_WQ, 1);
#pragma unroll
            for (int t = 0; t < 5; ++t) {
                const int tt = 5 * (wave - 1) + t;
                unsigned ad[2];
#pragma unroll
                for (int u = 0; u < 2; ++u) {
                    const int r = 8 * g + 4 * u + tq, B = 5 * r + (tt >> 3);
                    ad[u] = L_W + r * 1280 + (tt ^ ((B & 3) | ((B >> 1) & 4))) * 32 + tp * 8;
                }
                dw[t] = cat8(trd(L, ad[0]), trd(L, ad[1]));
            }
            lds_signal(C_QP);
        }
        if (wave < 5) {
            const int a = wave - 1;
            unsigned xres = 0;
            if (!wocu) xres = ((const unsigned*)x)[40 * xcd + 8 * a + (lane & 7)];
            lds_wait<true>(L, C_X, 1);
            tag = __builtin_amdgcn_readfirstlane(*(LAS unsigned*)(L + L_TAG));
            if (qcu) {
                bf16x8 xa[10];
#pragma unroll
                for (int s = 0; s < 10; ++s) xa[s] = *(LAS bf16x8*)(L + L_LN + (320 * qr + 32 * s + 8 * g) * 2);
                lds_wait<true>(L, C_WQ, 1);
                if (a == 0) STAMP(1);
                const unsigned ca = L_W + (8 * g + tq) * 128 + ((a ^ ((tq >> 1) | ((g & 1) << 1))) * 2 + (tp >> 1)) * 16 + (tp & 1) * 8;
                f32x4 q0 = z4, q1 = z4;
#pragma unroll
                for (int s = 0; s < 10; s += 2) {
                    q0 = mfma32(xa[s], cat8(trd(L, ca + s * 4096), trd(L, ca + s * 4096 + 512)), q0);
                    q1 = mfma32(xa[s + 1], cat8(trd(L, ca + s * 4096 + 4096), trd(L, ca + s * 4096 + 4608)), q1);
                }
                lds_signal(C_QP);
                if (!(KN7 & 2)) put1(rws, lane < 16 ? WS_QP + h * 3072 + qc * 1024 + qr * 512 + (16 * a + lane) * 8 : OOB, tag, __float_as_uint(q0[0] + q1[0]));
                if (a == 0) STAMP(2);
            }
            if (wocu) {
                unsigned oa = L_O + (lane < 48 ? 48 * a + lane : 192 + lane) * 2;
                PIN(oa);
                lds_wait<true>(L, C_W, 1);
                f32x4 w0 = *(LAS f32x4*)(L + L_WGT), w1 = *(LAS f32x4*)(L + L_WGT + 16);
                PIN(w0); PIN(w1);
                lds_wait<true>(L, C_PROG, pv0 + 84);
                until(t_end() + KN(18));
                unsigned own;
                lds_wait_rd<false>(L, C_OWN, 4, [&] { own = *(volatile LAS unsigned*)(L + L_OWN + (64 * a + lane) * 4); });
                const float o = combine_o<PUMP>(rws, lane * 8, WS_OP + (h * 8 * 192 + 48 * a) * 8, tag, j, own, w0, w1, D1);
                *(LAS u16*)(L + oa) = (u16)bfbits(o);
                lds_signal(C_O);
                if (a == 0) STAMP(8);
                if (gcu >= 0 && a == 0) {
                    lds_wait<true>(L, C_GO, 1);
                    float c0, c1;
                    sum_pairs<4, 0>(rws, lane * 16, WS_CP + gcu * 4096, tag, c0, c1);
                    auto sw = __builtin_amdgcn_permlane32_swap(__float_as_uint(c0), __float_as_uint(c1), false, false);
                    *(LAS float*)(L + L_LN + lane * 4) = fv(sw[0]) + fv(sw[1]);
                    MEMBAR();
                    if (lane == 0) *(LAS float*)(L + L_ST + 12) = 1.0f;
                }
            } else {
                make_h(a, xres);
                if (a == 0) STAMP(11);
            }
        } else {
            const int kw = wave - 5;
            bf16x8 kf[4][6];
            {
                const unsigned kb = L_V + (16 * kw + i16) * 384;
#pragma unroll
                for (int n = 0; n < 4; ++n) {
                    lds_wait<true>(L, C_PROG, 12 * ((kw >> 1) + 2 * n + 1));
#pragma unroll
                    for (int s = 0; s < 6; ++s) kf[n][s] = *(LAS bf16x8*)(L + kb + n * 24576 + ((4 * s + g) ^ (i16 >> 1)) * 16);
                    if (n & 1) lds_signal(n == 1 ? C_KA : C_KB);
                }
            }
            lds_wait<true>(L, C_Q, 1);
            tag = __builtin_amdgcn_readfirstlane(*(LAS unsigned*)(L + L_TAG));
            f32x4 sa[4] = {z4, z4, z4, z4};
#pragma unroll
            for (int s = 0; s < 6; ++s) {
                const bf16x8 qf = *(LAS bf16x8*)(L + L_Q + (32 * s + 8 * g) * 2);
#pragma unroll
                for (int n = 0; n < 4; ++n) sa[n] = mfma32(qf, kf[n][s], sa[n]);
            }
            const float raw = sel4(sa[0][0], sa[1][0], sa[2][0], sa[3][0]);
            const float s = rbf(rbf(raw) * 0.07216878364870322f);
            const float mw = wave_max(s);
            if (lane == 0) *(LAS float*)(L + L_MX + kw * 4) = mw;
            lds_signal(C_MAX);
            lds_wait<false>(L, C_MAX, 4);
            const f32x4 mx = *(LAS f32x4*)(L + L_MX);
            const float m = fmaxf(fmaxf(mx[0], mx[1]), fmaxf(mx[2], mx[3]));
            const float e = __expf(s - m);
            *(LAS u16*)(L + L_P + (16 * (kw + 4 * g) + i16) * 2) = (u16)bfbits(e);
            const float lw = wave_sum(e);
            if (lane == 0) *(LAS float*)(L + L_MX + 16 + kw * 4) = lw;
            lds_signal(C_P);
            lds_wait<false>(L, C_P, 4);
            if (kw == 0) STAMP(5);
            bf16x8 pf[8];
#pragma unroll
            for (int s8 = 0; s8 < 8; ++s8) pf[s8] = *(LAS bf16x8*)(L + L_P + (32 * s8 + 8 * g) * 2);
            const int fvs = (tq >> 1) | ((g & 1) << 1);
            unsigned vb[3], vl[3];
#pragma unroll
            for (int t = 0; t < 3; ++t) { vb[t] = (8 * g + tq) * 384 + (((3 * kw + t) ^ fvs) * 2 + (tp >> 1)) * 16 + (tp & 1) * 8; vl[t] = L_V + 6 * 12288 + vb[t]; PIN(vl[t]); }
            unsigned oo = lane < 48 ? WS_OP + ((h * 8 + j) * 192 + 48 * kw + lane) * 8 : OOB, ow = L_OWN + (64 * kw + lane) * 4;
            PIN(oo); PIN(ow);
            f32x4 oa[3] = {z4, z4, z4};
            lds_wait<true>(L, C_PROG, pv0 + 72);
#pragma unroll
            for (int s8 = 0; s8 < 6; ++s8)
#pragma unroll
                for (int t = 0; t < 3; ++t) oa[t] = mfma32(pf[s8], cat8(trd(L, L_V + vb[t] + s8 * 12288), trd(L, L_V + vb[t] + s8 * 12288 + 1536)), oa[t]);
            {
                s16x4 vf[6];
                spin_tr<0>(C_PROG, pv0 + 84, vl, vf);
#pragma unroll
                for (int t = 0; t < 3; ++t) oa[t] = mfma32(pf[6], cat8(vf[2 * t], vf[2 * t + 1]), oa[t]);
                spin_tr<12288>(C_PROG, pv0 + 96, vl, vf);
                if (kw == 0) STAMP(20);
#pragma unroll
                for (int t = 0; t < 3; ++t) oa[t] = mfma32(pf[7], cat8(vf[2 * t], vf[2 * t + 1]), oa[t]);
            }
            const float op = sel4(oa[0][0], oa[1][0], oa[2][0], oa[2][0]);
            put1(rws, oo, tag, __float_as_uint(op));
            *(LAS float*)(L + ow) = op;
            lds_signal(C_OWN);
            if (kw == 0) STAMP(7);
            if (wocu) {
                unsigned ao = lane < 32 ? WS_AP + (((8 * j + 2 * kw + g) * 32 + h) * 16 + i16) * 8 : OOB, al = L_O + 16 * g;
                PIN(ao); PIN(al);
                lds_wait<false>(L, C_PROG, P_WO);
                bf16x8 bw[2][6], af[6];
                const int f3 = tq | ((g & 1) << 2);
#pragma unroll
                for (int t = 0; t < 2; ++t) {
                    const unsigned cb = L_W + (8 * g + tq) * 256 + (((2 * kw + t) ^ f3) * 2 + (tp >> 1)) * 16 + (tp & 1) * 8;
#pragma unroll
                    for (int s6 = 0; s6 < 6; ++s6) bw[t][s6] = cat8(trd(L, cb + s6 * 8192), trd(L, cb + s6 * 8192 + 1024));
                }
                spin_rd(C_O, 4, al, af);
                f32x4 a0 = z4, a1 = z4;
#pragma unroll
                for (int s6 = 0; s6 < 6; ++s6) { a0 = mfma32(af[s6], bw[0][s6], a0); a1 = mfma32(af[s6], bw[1][s6], a1); }
                put1(rws, ao, tag, __float_as_uint((lane & 16) ? a1[0] : a0[0]));
                if (kw == 0) STAMP(10);
            } else {
                lds_wait<true>(L, C_PROG, P_WO);
                bf16x8 bw[2][2][3], af[3];
                const int f3 = tq | ((g & 1) << 2);
#pragma unroll
                for (int mt = 0; mt < 2; ++mt)
#pragma unroll
                    for (int t = 0; t < 2; ++t) {
                        const unsigned cb = L_W + mt * 24576 + (8 * g + tq) * 256 + ((2 * kw + t) ^ f3) * 32 + tp * 8;
#pragma unroll
                        for (int s3 = 0; s3 < 3; ++s3) bw[mt][t][s3] = cat8(trd(L, cb + s3 * 8192), trd(L, cb + s3 * 8192 + 1024));
                    }
                unsigned go = lane < 32 ? WS_CP + ((4 * oc + kw) * 8 + xcd) * 512 + lane * 16 : OOB, g2 = go + (WS_GP - WS_CP), al = L_O + 16 * g;
                PIN(al); PIN(g2);
                auto tile4 = [&](unsigned off) {
                    f32x4 d[2][2] = {{z4, z4}, {z4, z4}};
#pragma unroll
                    for (int s3 = 0; s3 < 3; ++s3)
#pragma unroll
                        for (int mt = 0; mt < 2; ++mt)
#pragma unroll
                            for (int t = 0; t < 2; ++t) d[mt][t] = mfma32(af[s3], bw[mt][t][s3], d[mt][t]);
                    SB();
                    put2(rws, off, tag, (lane & 16) ? d[0][1][0] : d[0][0][0], (lane & 16) ? d[1][1][0] : d[1][0][0]);
                };
                const v4u ones = {0x3F803F80u, 0x3F803F80u, 0x3F803F80u, 0x3F803F80u};
                af[0] = af[1] = af[2] = asb(ones);
                tile4(go);
                *(LAS unsigned*)(L + L_O + 160 + (lane & 7) * 4) = 0u;
                spin_rd(C_O, 5, al, af);
                tile4(g2);
                if (kw == 0) STAMP(21);
            }
        }
        if (gcu >= 0) {
            const int w8 = wave - 1;
            unsigned d0 = WS_DP + (((5 * w8 + g) * 48 + gcu) * 16 + i16) * 8, d1 = lane < 16 ? WS_DP + (((5 * w8 + 4) * 48 + gcu) * 16 + i16) * 8 : OOB, al = L_HH + 16 * g;
            PIN(d0); PIN(d1); PIN(al);
            bf16x8 af[1];
            lds_wait<true>(L, C_GO, 1);
            spin_rd(C_HH, 1, al, af);
            f32x4 d[5];
#pragma unroll
            for (int t = 0; t < 5; ++t) d[t] = mfma32(af[0], dw[t], z4);
            SB();
            put1(rws, d0, tag, __float_as_uint(sel4(d[0][0], d[1][0], d[2][0], d[3][0])));
            put1(rws, d1, tag, __float_as_uint(d[4][0]));
            if (w8 == 0) STAMP(13);
        }
    }
}

extern "C" void kernel_launch(void* const* d_in, const int* in_sizes, int n_in, void* d_out, int out_size, void* d_ws, size_t ws_size, hipStream_t stream) {
    (void)hipFuncSetAttribute((const void*)k_block, hipFuncAttributeMaxDynamicSharedMemorySize, L_END);
    k_block<<<256, 640, L_END, stream>>>((const u16*)d_in[0], (const u16*)d_in[1], (const u16*)d_in[2], (const u16*)d_in[3], (const u16*)d_in[4],
                                         (const u16*)d_in[6], (const u16*)d_in[7], (const u16*)d_in[8], (u16*)d_out, (unsigned char*)d_ws
        );
}
```

```cpp
#include <hip/hip_runtime.h>
#include <stdint.h>

typedef unsigned short u16;
typedef unsigned long long u64;
typedef unsigned v4u __attribute__((ext_vector_type(4)));
typedef unsigned v2u __attribute__((ext_vector_type(2)));
typedef float f32x4 __attribute__((ext_vector_type(4)));
typedef short s16x4 __attribute__((ext_vector_type(4)));
typedef __bf16 bf16x8 __attribute__((ext_vector_type(8)));
typedef __bf16 bf16x2 __attribute__((ext_vector_type(2)));
#define GAS __attribute__((address_space(1)))
#define LAS __attribute__((address_space(3)))
typedef GAS u64 gu64;
typedef GAS unsigned gu32;
typedef __amdgpu_buffer_rsrc_t rsrc_t;
#define RLX_AGENT __ATOMIC_RELAXED, __HIP_MEMORY_SCOPE_AGENT

constexpr int D = 640, H = 32, SK = 8192, DH = 192, W = 2048, F = 1536;

constexpr unsigned WS_EPOCH = 0;
constexpr unsigned WS_T0 = 1024;
constexpr unsigned WS_QP = 4096;
constexpr unsigned WS_ST = WS_QP + 32 * 3072;
constexpr unsigned WS_HB = WS_ST + 32 * 8 * 128;
constexpr unsigned WS_OP = WS_HB + 8 * 4096;
constexpr unsigned WS_AP = WS_OP + 32 * 6144;
constexpr unsigned WS_CP = WS_AP + 40 * 2048;
constexpr unsigned WS_GP = WS_CP + 48 * 2048;
constexpr unsigned WS_DP = WS_GP + 48 * 2048;
constexpr unsigned WS_END = WS_DP + 40 * 3072;
constexpr unsigned OOB = 0x7FFFFF00u;

constexpr unsigned L_W = 0;
constexpr unsigned L_V = 49152;
constexpr unsigned L_Q = L_V + 98304;
constexpr unsigned L_P = L_Q + 384;
constexpr unsigned L_O = L_P + 512;
constexpr unsigned L_LN = L_O + 512;
constexpr unsigned L_OWN = L_LN + 1280;
constexpr unsigned L_HH = L_OWN + 1024;
constexpr unsigned L_MX = L_HH + 128;
constexpr unsigned L_WGT = L_MX + 32;
constexpr unsigned L_CNT = L_WGT + 32;
constexpr unsigned L_ST = L_CNT + 64;
constexpr unsigned L_TAG = L_ST + 16;
constexpr unsigned L_STAMP = L_TAG + 16;
constexpr unsigned L_JUNK = L_STAMP + 256;
constexpr unsigned L_END = L_JUNK + 1024;
static_assert(L_END <= 163840, "LDS");
enum { C_X = 0, C_PROG, C_KA, C_KB, C_Q, C_MAX, C_P, C_W, C_OWN, C_O, C_HH, C_WQ, C_QP, C_GO, C_LATE };
constexpr int P_K = 96, P_WO = 240;

#define KNOB_DEFAULTS {2, 40, 100, 890, 80, 400, -80, 0, 3, 150, 50, 100, 170, 40, 282, 0, 0, 0, 0, 0, 2, 4, 16, 4}
constexpr int kn_c[24] = KNOB_DEFAULTS;
#define KN(i) kn_c[i]
#define KNARG
#define N1G KN(0)
#define TA0 KN(1)
#define TAQ KN(2)
#define TE KN(3)
#define FDELAY KN(4)
#define TQ KN(5)
#define TS KN(6)
#define D1 KN(8)
#define D2 KN(9)
#define D3 KN(10)
#define D4 KN(11)
#define G1 KN(12)
#define DOBS KN(13)
#define G2 KN(14)
constexpr int WMAX = 36;
constexpr unsigned SPIN_LDS = 1u << 24;
constexpr unsigned SPIN_GLB = 1u << 21;

__device__ __forceinline__ unsigned pk2(float lo, float hi) {
    const bf16x2 r = {(__bf16)lo, (__bf16)hi};
    return __builtin_bit_cast(unsigned, r);
}
__device__ __forceinline__ unsigned bfbits(float f) { return (unsigned)__builtin_bit_cast(u16, (__bf16)f); }
__device__ __forceinline__ float rbf(float f) { return __uint_as_float(bfbits(f) << 16); }
__device__ __forceinline__ float lo16(unsigned w) { return __uint_as_float(w << 16); }
__device__ __forceinline__ float hi16(unsigned w) { return __uint_as_float(w & 0xFFFF0000u); }
__device__ __forceinline__ float dot2(unsigned a, unsigned b, float c) {
    return __builtin_amdgcn_fdot2_f32_bf16(__builtin_bit_cast(bf16x2, a), __builtin_bit_cast(bf16x2, b), c, false);
}
template <int CTRL> __device__ __forceinline__ float dpp(float x) {
    return __builtin_bit_cast(float, __builtin_amdgcn_mov_dpp(__builtin_bit_cast(int, x), CTRL, 0xf, 0xf, true));
}
constexpr int XOR1 = 0xB1, XOR2 = 0x4E, XOR7 = 0x141, XOR8 = 0x128, ROR4 = 0x124;
__device__ __forceinline__ float sw16_sum(float x) {
    auto s = __builtin_amdgcn_permlane16_swap(__float_as_uint(x), __float_as_uint(x), false, false);
    return __uint_as_float(s[0]) + __uint_as_float(s[1]);
}
__device__ __forceinline__ float sw32_sum(float x) {
    auto s = __builtin_amdgcn_permlane32_swap(__float_as_uint(x), __float_as_uint(x), false, false);
    return __uint_as_float(s[0]) + __uint_as_float(s[1]);
}
__device__ __forceinline__ float sw16_max(float x) {
    auto s = __builtin_amdgcn_permlane16_swap(__float_as_uint(x), __float_as_uint(x), false, false);
    return fmaxf(__uint_as_float(s[0]), __uint_as_float(s[1]));
}
__device__ __forceinline__ float sw32_max(float x) {
    auto s = __builtin_amdgcn_permlane32_swap(__float_as_uint(x), __float_as_uint(x), false, false);
    return fmaxf(__uint_as_float(s[0]), __uint_as_float(s[1]));
}
__device__ __forceinline__ float wave_sum(float x) {
    x += dpp<XOR1>(x); x += dpp<XOR2>(x); x += dpp<XOR7>(x); x += dpp<XOR8>(x);
    return sw32_sum(sw16_sum(x));
}
__device__ __forceinline__ float wave_max(float x) {
    x = fmaxf(x, dpp<XOR1>(x)); x = fmaxf(x, dpp<XOR2>(x)); x = fmaxf(x, dpp<XOR7>(x)); x = fmaxf(x, dpp<XOR8>(x));
    return sw32_max(sw16_max(x));
}
__device__ __forceinline__ rsrc_t mkrs(const void* p, unsigned bytes) { return __builtin_amdgcn_make_buffer_rsrc((void*)p, 0, bytes, 0x00020000); }

template <int N> __device__ __forceinline__ void waitvm() { asm volatile("s_waitcnt vmcnt(%0)" :: "n"(N) : "memory"); }
#define LGKM_DRAIN() asm volatile("s_waitcnt lgkmcnt(0)" ::: "memory")
#define MEMBAR() asm volatile("" ::: "memory")
#define BAR() do { __builtin_amdgcn_sched_barrier(0); asm volatile("s_barrier" ::: "memory"); __builtin_amdgcn_sched_barrier(0); } while (0)

__device__ __forceinline__ void lds_signal(int c) {
    const unsigned addr = L_CNT + 4 * c, one = 1u;
    asm volatile("s_mov_b64 exec, 1\n\tds_add_u32 %0, %1\n\ts_mov_b64 exec, -1" :: "v"(addr), "v"(one) : "memory");
}
__device__ __forceinline__ void lds_set(int c, unsigned val) {
    const unsigned addr = L_CNT + 4 * c;
    asm volatile("s_mov_b64 exec, 1\n\tds_write_b32 %0, %1\n\ts_mov_b64 exec, -1" :: "v"(addr), "v"(val) : "memory");
}
template <bool SLEEP> __device__ __forceinline__ void lds_wait(LAS unsigned char* L, int c, unsigned n) {
    volatile LAS unsigned* p = (volatile LAS unsigned*)(L + L_CNT) + c;
    unsigned cap = SPIN_LDS;
#pragma unroll 1
    do {
        if ((unsigned)__builtin_amdgcn_readfirstlane(*p) >= n) break;
        if (SLEEP) __builtin_amdgcn_s_sleep(1);
        MEMBAR();
    } while (--cap != 0);
    MEMBAR();
}
__device__ __forceinline__ void until(u64 t) { while (__builtin_amdgcn_s_memrealtime() < t) __builtin_amdgcn_s_sleep(1); }
__device__ __forceinline__ void delay(int ticks) { until(__builtin_amdgcn_s_memrealtime() + ticks); }
__device__ __forceinline__ void nap(int n) { for (int i = 0; i < n; ++i) __builtin_amdgcn_s_sleep(1); }
#define PIN(x) asm volatile("" : "+v"(x))
#define SB() __builtin_amdgcn_sched_barrier(0)
__device__ __forceinline__ float fv(unsigned u) { return __uint_as_float(u); }
__device__ __forceinline__ void put1(rsrc_t rs, unsigned off, unsigned tag, unsigned val) { __builtin_amdgcn_raw_buffer_store_b64(v2u{val, tag}, rs, off, 0, 16); }
__device__ __forceinline__ void put2(rsrc_t rs, unsigned off, unsigned tag, float a, float b) {
    __builtin_amdgcn_raw_buffer_store_b128(v4u{__float_as_uint(a), tag, __float_as_uint(b), tag}, rs, off, 0, 16);
}
__device__ __forceinline__ u64 badmask(v2u x, unsigned tag) {
    u64 m;
    asm volatile("v_cmp_ne_u32_e64 %0, %1, %2" : "=s"(m) : "s"(tag), "v"(x[1]));
    return m;
}
__device__ __forceinline__ u64 badmask(v4u x, unsigned tag) {
    u64 m;
    const unsigned t = __builtin_amdgcn_bitop3_b32(x[1], x[3], tag, 0x7E);
    asm volatile("v_cmp_ne_u32_e64 %0, 0, %1" : "=s"(m) : "v"(t));
    return m;
}
__device__ __forceinline__ void gload(v2u& x, u64 m, rsrc_t rs, unsigned voff, unsigned soff) {
    asm volatile("s_mov_b64 exec, %2\n\tbuffer_load_dwordx2 %0, %1, %3, %4 offen sc1\n\ts_mov_b64 exec, -1" : "+v"(x) : "v"(voff), "s"(m), "s"(rs), "s"(soff) : "memory");
}
__device__ __forceinline__ void gload(v4u& x, u64 m, rsrc_t rs, unsigned voff, unsigned soff) {
    asm volatile("s_mov_b64 exec, %2\n\tbuffer_load_dwordx4 %0, %1, %3, %4 offen sc1\n\ts_mov_b64 exec, -1" : "+v"(x) : "v"(voff), "s"(m), "s"(rs), "s"(soff) : "memory");
}
template <int SLP, int N, class T> __device__ __forceinline__ void gather(rsrc_t rs, unsigned voff, unsigned base, unsigned stride, unsigned tag, T (&x)[N]) {
    u64 m[N];
#pragma unroll
    for (int k = 0; k < N; ++k) { PIN(x[k]); m[k] = badmask(x[k], tag); }
    unsigned cap = SPIN_GLB;
#pragma unroll 1
    do {
#pragma unroll
        for (int k = 0; k < N; ++k) gload(x[k], m[k], rs, voff, base + k * stride);
        waitvm<0>();
#pragma unroll
        for (int k = 0; k < N; ++k) PIN(x[k]);
        u64 any = 0;
#pragma unroll
        for (int k = 0; k < N; ++k) { m[k] = badmask(x[k], tag); any |= m[k]; }
        if (!any) break;
        if (SLP) __builtin_amdgcn_s_sleep(SLP);
        MEMBAR();
    } while (--cap != 0);
}
__device__ __forceinline__ unsigned enc(float v, unsigned tb) { return (__float_as_uint(v) & 0xFFFFFF00u) | tb; }
__device__ __forceinline__ void putw(rsrc_t rs, unsigned off, unsigned w) { __builtin_amdgcn_raw_buffer_store_b32(w, rs, off, 0, 16); }
__device__ __forceinline__ float fold4(float a0, float a1, float a2, float a3) {
    auto p = __builtin_amdgcn_permlane32_swap(__float_as_uint(a0), __float_as_uint(a2), false, false);
    auto q = __builtin_amdgcn_permlane32_swap(__float_as_uint(a1), __float_as_uint(a3), false, false);
    auto r = __builtin_amdgcn_permlane16_swap(__float_as_uint(fv(p[0]) + fv(p[1])), __float_as_uint(fv(q[0]) + fv(q[1])), false, false);
    return fv(r[0]) + fv(r[1]);
}
__device__ __forceinline__ float sum_words2(rsrc_t rs, unsigned voff, unsigned base, unsigned tag) {
    unsigned cap = SPIN_GLB;
    float a0, a1, a2, a3;
    asm volatile(
        "buffer_load_dwordx4 v[144:147], %[vo], %[rs], %[b] offen offset:0 sc1\n\t"
        "buffer_load_dwordx4 v[148:151], %[vo], %[rs], %[b] offen offset:1024 sc1\n\t"
        "s_branch 2f\n\t"
        "1:\n\t"
        "s_mov_b64 exec, s[78:79]\n\t"
        "buffer_load_dwordx4 v[144:147], %[vo], %[rs], %[b] offen offset:0 sc1\n\t"
        "s_mov_b64 exec, s[80:81]\n\t"
        "buffer_load_dwordx4 v[148:151], %[vo], %[rs], %[b] offen offset:1024 sc1\n\t"
        "s_mov_b64 exec, -1\n\t"
        "s_sub_u32 %[cap], %[cap], 1\n\t"
        "s_cbranch_scc1 3f\n\t"
        "2:\n\t"
        "s_waitcnt vmcnt(0)\n\t"
        "v_xor_b32 v156, %[t0], v144\n\t"
        "v_xor_b32 v157, %[t1], v145\n\t"
        "v_xor_b32 v158, %[t2], v146\n\t"
        "v_xor_b32 v159, %[t3], v147\n\t"
        "v_xor_b32 v160, %[t0], v148\n\t"
        "v_xor_b32 v161, %[t1], v149\n\t"
        "v_xor_b32 v162, %[t2], v150\n\t"
        "v_xor_b32 v163, %[t3], v151\n\t"
        "v_or3_b32 v140, v156, v157, v158\n\t"
        "v_or3_b32 v141, v160, v161, v162\n\t"
        "v_bitop3_b32 v140, v140, v159, %[ff] bitop3:0xa8\n\t"
        "v_bitop3_b32 v141, v141, v163, %[ff] bitop3:0xa8\n\t"
        "v_cmp_ne_u32_e64 s[78:79], 0, v140\n\t"
        "v_cmp_ne_u32_e64 s[80:81], 0, v141\n\t"
        "s_or_b64 s[92:93], s[78:79], s[80:81]\n\t"
        "s_cbranch_scc1 1b\n\t"
        "3:\n\t"
        "s_waitcnt vmcnt(0)\n\t"
        "v_add_f32 %[a0], v156, v160\n\t"
        "v_add_f32 %[a1], v157, v161\n\t"
        "v_add_f32 %[a2], v158, v162\n\t"
        "v_add_f32 %[a3], v159, v163\n\t"
        "s_nop 1\n\t"
        : [a0] "=&v"(a0), [a1] "=&v"(a1), [a2] "=&v"(a2), [a3] "=&v"(a3), [cap] "+&s"(cap)
        : [vo] "v"(voff), [rs] "s"(rs), [b] "s"(base), [t0] "s"(tag & 255u), [t1] "s"((tag >> 8) & 255u), [t2] "s"((tag >> 16) & 255u), [t3] "s"(tag >> 24),
          [ff] "s"(255u)
        : "v140", "v141", "v142", "v144", "v145", "v146", "v147", "v148", "v149", "v150", "v151", "v156", "v157", "v158", "v159", "v160", "v161", "v162", "v163", "s78", "s79", "s80", "s81", "s92", "s93", "scc", "memory");
    return fold4(a0, a1, a2, a3);
}
__device__ __forceinline__ float sum_words3(rsrc_t rs, unsigned voff, unsigned base, unsigned tag) {
    unsigned cap = SPIN_GLB;
    float a0, a1, a2, a3;
    asm volatile(
        "buffer_load_dwordx4 v[144:147], %[vo], %[rs], %[b] offen offset:0 sc1\n\t"
        "buffer_load_dwordx4 v[148:151], %[vo], %[rs], %[b] offen offset:1024 sc1\n\t"
        "buffer_load_dwordx4 v[152:155], %[vo], %[rs], %[b] offen offset:2048 sc1\n\t"
        "s_branch 2f\n\t"
        "1:\n\t"
        "s_mov_b64 exec, s[78:79]\n\t"
        "buffer_load_dwordx4 v[144:147], %[vo], %[rs], %[b] offen offset:0 sc1\n\t"
        "s_mov_b64 exec, s[80:81]\n\t"
        "buffer_load_dwordx4 v[148:151], %[vo], %[rs], %[b] offen offset:1024 sc1\n\t"
        "s_mov_b64 exec, s[82:83]\n\t"
        "buffer_load_dwordx4 v[152:155], %[vo], %[rs], %[b] offen offset:2048 sc1\n\t"
        "s_mov_b64 exec, -1\n\t"
        "s_sub_u32 %[cap], %[cap], 1\n\t"
        "s_cbranch_scc1 3f\n\t"
        "2:\n\t"
        "s_waitcnt vmcnt(0)\n\t"
        "v_xor_b32 v156, %[t0], v144\n\t"
        "v_xor_b32 v157, %[t1], v145\n\t"
        "v_xor_b32 v158, %[t2], v146\n\t"
        "v_xor_b32 v159, %[t3], v147\n\t"
        "v_xor_b32 v160, %[t0], v148\n\t"
        "v_xor_b32 v161, %[t1], v149\n\t"
        "v_xor_b32 v162, %[t2], v150\n\t"
        "v_xor_b32 v163, %[t3], v151\n\t"
        "v_xor_b32 v164, %[t0], v152\n\t"
        "v_xor_b32 v165, %[t1], v153\n\t"
        "v_xor_b32 v166, %[t2], v154\n\t"
        "v_xor_b32 v167, %[t3], v155\n\t"
        "v_or3_b32 v140, v156, v157, v158\n\t"
        "v_or3_b32 v141, v160, v161, v162\n\t"
        "v_or3_b32 v142, v164, v165, v166\n\t"
        "v_bitop3_b32 v140, v140, v159, %[ff] bitop3:0xa8\n\t"
        "v_bitop3_b32 v141, v141, v163, %[ff] bitop3:0xa8\n\t"
        "v_bitop3_b32 v142, v142, v167, %[ff] bitop3:0xa8\n\t"
        "v_cmp_ne_u32_e64 s[78:79], 0, v140\n\t"
        "v_cmp_ne_u32_e64 s[80:81], 0, v141\n\t"
        "v_cmp_ne_u32_e64 s[82:83], 0, v142\n\t"
        "s_or_b64 s[92:93], s[78:79], s[80:81]\n\t"
        "s_or_b64 s[92:93], s[92:93], s[82:83]\n\t"
        "s_cbranch_scc1 1b\n\t"
        "3:\n\t"
        "s_waitcnt vmcnt(0)\n\t"
        "v_add_f32 %[a0], v156, v160\n\t"
        "v_add_f32 %[a1], v157, v161\n\t"
        "v_add_f32 %[a2], v158, v162\n\t"
        "v_add_f32 %[a3], v159, v163\n\t"
        "v_add_f32 %[a0], %[a0], v164\n\t"
        "v_add_f32 %[a1], %[a1], v165\n\t"
        "v_add_f32 %[a2], %[a2], v166\n\t"
        "v_add_f32 %[a3], %[a3], v167\n\t"
        "s_nop 1\n\t"
        : [a0] "=&v"(a0), [a1] "=&v"(a1), [a2] "=&v"(a2), [a3] "=&v"(a3), [cap] "+&s"(cap)
        : [vo] "v"(voff), [rs] "s"(rs), [b] "s"(base), [t0] "s"(tag & 255u), [t1] "s"((tag >> 8) & 255u), [t2] "s"((tag >> 16) & 255u), [t3] "s"(tag >> 24),
          [ff] "s"(255u)
        : "v140", "v141", "v142", "v144", "v145", "v146", "v147", "v148", "v149", "v150", "v151", "v152", "v153", "v154", "v155", "v156", "v157", "v158", "v159", "v160", "v161", "v162", "v163", "v164", "v165", "v166", "v167", "s78", "s79", "s80", "s81", "s82", "s83", "s92", "s93", "scc", "memory");
    return fold4(a0, a1, a2, a3);
}
__device__ __forceinline__ float sum_words_w(rsrc_t rs, unsigned voff, unsigned base, unsigned tag, v4u own, u64 m0, u64 m1, float w0, float w1, unsigned napn) {
    unsigned cap = SPIN_GLB;
    float a0, a1, a2, a3;
    asm volatile(
        "s_mov_b64 s[78:79], %[m0]\n\t"
        "v_mov_b32 v144, %[o0]\n\t"
        "v_mov_b32 v145, %[o1]\n\t"
        "v_mov_b32 v146, %[o2]\n\t"
        "v_mov_b32 v147, %[o3]\n\t"
        "s_mov_b64 s[80:81], %[m1]\n\t"
        "v_mov_b32 v148, %[o0]\n\t"
        "v_mov_b32 v149, %[o1]\n\t"
        "v_mov_b32 v150, %[o2]\n\t"
        "v_mov_b32 v151, %[o3]\n\t"
        "s_cmp_eq_u32 %[nap], 0\n\t"
        "s_cbranch_scc1 1f\n\t"
        "4:\n\t"
        "s_sleep 1\n\t"
        "s_sub_u32 %[nap], %[nap], 1\n\t"
        "s_cmp_lg_u32 %[nap], 0\n\t"
        "s_cbranch_scc1 4b\n\t"
        "1:\n\t"
        "s_mov_b64 exec, s[78:79]\n\t"
        "buffer_load_dwordx4 v[144:147], %[vo], %[rs], %[b] offen offset:0 sc1\n\t"
        "s_mov_b64 exec, s[80:81]\n\t"
        "buffer_load_dwordx4 v[148:151], %[vo], %[rs], %[b] offen offset:3072 sc1\n\t"
        "s_mov_b64 exec, -1\n\t"
        "s_sub_u32 %[cap], %[cap], 1\n\t"
        "s_cbranch_scc1 3f\n\t"
        "2:\n\t"
        "s_waitcnt vmcnt(0)\n\t"
        "v_xor_b32 v156, %[t0], v144\n\t"
        "v_xor_b32 v157, %[t1], v145\n\t"
        "v_xor_b32 v158, %[t2], v146\n\t"
        "v_xor_b32 v159, %[t3], v147\n\t"
        "v_xor_b32 v160, %[t0], v148\n\t"
        "v_xor_b32 v161, %[t1], v149\n\t"
        "v_xor_b32 v162, %[t2], v150\n\t"
        "v_xor_b32 v163, %[t3], v151\n\t"
        "v_or3_b32 v140, v156, v157, v158\n\t"
        "v_or3_b32 v141, v160, v161, v162\n\t"
        "v_bitop3_b32 v140, v140, v159, %[ff] bitop3:0xa8\n\t"
        "v_bitop3_b32 v141, v141, v163, %[ff] bitop3:0xa8\n\t"
        "v_cmp_ne_u32_e64 s[78:79], 0, v140\n\t"
        "v_cmp_ne_u32_e64 s[80:81], 0, v141\n\t"
        "s_or_b64 s[92:93], s[78:79], s[80:81]\n\t"
        "s_cbranch_scc1 1b\n\t"
        "3:\n\t"
        "s_waitcnt vmcnt(0)\n\t"
        "v_mul_f32 %[a0], %[w0], v156\n\t"
        "v_mul_f32 %[a1], %[w0], v157\n\t"
        "v_mul_f32 %[a2], %[w0], v158\n\t"
        "v_mul_f32 %[a3], %[w0], v159\n\t"
        "v_fmac_f32 %[a0], %[w1], v160\n\t"
        "v_fmac_f32 %[a1], %[w1], v161\n\t"
        "v_fmac_f32 %[a2], %[w1], v162\n\t"
        "v_fmac_f32 %[a3], %[w1], v163\n\t"
        "s_nop 1\n\t"
        : [a0] "=&v"(a0), [a1] "=&v"(a1), [a2] "=&v"(a2), [a3] "=&v"(a3), [cap] "+&s"(cap), [nap] "+&s"(napn)
        : [vo] "v"(voff), [rs] "s"(rs), [b] "s"(base), [t0] "s"(tag & 255u), [t1] "s"((tag >> 8) & 255u), [t2] "s"((tag >> 16) & 255u), [t3] "s"(tag >> 24),
          [ff] "s"(255u), [o0] "v"(own[0]), [o1] "v"(own[1]), [o2] "v"(own[2]), [o3] "v"(own[3]), [m0] "s"(m0), [m1] "s"(m1), [w0] "v"(w0), [w1] "v"(w1)
        : "v140", "v141", "v142", "v144", "v145", "v146", "v147", "v148", "v149", "v150", "v151", "v156", "v157", "v158", "v159", "v160", "v161", "v162", "v163", "s78", "s79", "s80", "s81", "s92", "s93", "scc", "memory");
    return fold4(a0, a1, a2, a3);
}
__device__ __forceinline__ float look_words(rsrc_t rs, unsigned voff, unsigned base, unsigned tag) {
    unsigned cap = SPIN_GLB;
    float a0, a1, a2, a3;
    asm volatile(
        "buffer_load_dwordx4 v[144:147], %[vo], %[rs], %[b] offen offset:0 sc1\n\t"
        "buffer_load_dwordx4 v[148:151], %[vo], %[rs], %[b] offen offset:1024 sc1\n\t"
        "s_branch 2f\n\t"
        "1:\n\t"
        "s_sleep 2\n\t"
        "s_mov_b64 exec, s[78:79]\n\t"
        "buffer_load_dwordx4 v[144:147], %[vo], %[rs], %[b] offen offset:0 sc1\n\t"
        "s_mov_b64 exec, s[80:81]\n\t"
        "buffer_load_dwordx4 v[148:151], %[vo], %[rs], %[b] offen offset:1024 sc1\n\t"
        "s_mov_b64 exec, -1\n\t"
        "s_sub_u32 %[cap], %[cap], 1\n\t"
        "s_cbranch_scc1 3f\n\t"
        "2:\n\t"
        "s_waitcnt vmcnt(0)\n\t"
        "v_xor_b32 v156, %[t0], v144\n\t"
        "v_xor_b32 v157, %[t1], v145\n\t"
        "v_xor_b32 v158, %[t2], v146\n\t"
        "v_xor_b32 v159, %[t3], v147\n\t"
        "v_xor_b32 v160, %[t0], v148\n\t"
        "v_xor_b32 v161, %[t1], v149\n\t"
        "v_xor_b32 v162, %[t2], v150\n\t"
        "v_xor_b32 v163, %[t3], v151\n\t"
        "v_or3_b32 v140, v156, v157, v158\n\t"
        "v_or3_b32 v141, v160, v161, v162\n\t"
        "v_bitop3_b32 v140, v140, v159, %[ff] bitop3:0xa8\n\t"
        "v_bitop3_b32 v141, v141, v163, %[ff] bitop3:0xa8\n\t"
        "v_cmp_ne_u32_e64 s[78:79], 0, v140\n\t"
        "v_cmp_ne_u32_e64 s[80:81], 0, v141\n\t"
        "s_or_b64 s[92:93], s[78:79], s[80:81]\n\t"
        "s_cbranch_scc1 1b\n\t"
        "3:\n\t"
        "s_waitcnt vmcnt(0)\n\t"
        "v_add_f32 %[a0], v156, v160\n\t"
        "v_add_f32 %[a1], v157, v161\n\t"
        "v_add_f32 %[a2], v158, v162\n\t"
        "v_add_f32 %[a3], v159, v163\n\t"
        "s_nop 1\n\t"
        : [a0] "=&v"(a0), [a1] "=&v"(a1), [a2] "=&v"(a2), [a3] "=&v"(a3), [cap] "+&s"(cap)
        : [vo] "v"(voff), [rs] "s"(rs), [b] "s"(base), [t0] "s"(tag & 255u), [t1] "s"((tag >> 8) & 255u), [t2] "s"((tag >> 16) & 255u), [t3] "s"(tag >> 24),
          [ff] "s"(255u)
        : "v140", "v141", "v142", "v144", "v145", "v146", "v147", "v148", "v149", "v150", "v151", "v156", "v157", "v158", "v159", "v160", "v161", "v162", "v163", "s78", "s79", "s80", "s81", "s92", "s93", "scc", "memory");
    return fold4(a0, a1, a2, a3);
}
__device__ __forceinline__ s16x4 trd(LAS unsigned char* L, unsigned off) { return __builtin_amdgcn_ds_read_tr16_b64_v4i16((LAS s16x4*)(L + off)); }
__device__ __forceinline__ bf16x8 cat8(s16x4 a, s16x4 b) { return __builtin_bit_cast(bf16x8, __builtin_shufflevector(a, b, 0, 1, 2, 3, 4, 5, 6, 7)); }
__device__ __forceinline__ f32x4 mfma32(bf16x8 a, bf16x8 b, f32x4 c) { return __builtin_amdgcn_mfma_f32_16x16x32_bf16(a, b, c, 0, 0, 0); }
__device__ __forceinline__ bf16x8 asb(v4u a) { return __builtin_bit_cast(bf16x8, a); }

__device__ __forceinline__ void spin_rd(int c, unsigned n, unsigned a, bf16x8 (&o)[1]) {
    unsigned f, cap = SPIN_LDS;
    const unsigned fa = L_CNT + 4 * c;
    asm volatile(
        "1:\n\t"
        "ds_read_b32 %[f], %[fa]\n\t"
        "ds_read_b128 %[o0], %[a] offset:0\n\t"
        "s_sub_u32 %[cap], %[cap], 1\n\t"
        "s_cbranch_scc1 2f\n\t"
        "s_waitcnt lgkmcnt(1)\n\t"
        "v_readfirstlane_b32 s92, %[f]\n\t"
        "s_cmp_lt_u32 s92, %[n]\n\t"
        "s_cbranch_scc1 1b\n\t"
        "2:\n\t"
        "s_waitcnt lgkmcnt(0)"
        : [f] "=&v"(f), [cap] "+&s"(cap), [o0] "=&v"(o[0]) : [fa] "v"(fa), [a] "v"(a), [n] "s"(n) : "s92", "scc", "memory");
}
__device__ __forceinline__ void spin_rd(int c, unsigned n, unsigned a, bf16x8 (&o)[3]) {
    unsigned f, cap = SPIN_LDS;
    const unsigned fa = L_CNT + 4 * c;
    asm volatile(
        "1:\n\t"
        "ds_read_b32 %[f], %[fa]\n\t"
        "ds_read_b128 %[o0], %[a] offset:0\n\t"
        "ds_read_b128 %[o1], %[a] offset:64\n\t"
        "ds_read_b128 %[o2], %[a] offset:128\n\t"
        "s_sub_u32 %[cap], %[cap], 1\n\t"
        "s_cbranch_scc1 2f\n\t"
        "s_waitcnt lgkmcnt(3)\n\t"
        "v_readfirstlane_b32 s92, %[f]\n\t"
        "s_cmp_lt_u32 s92, %[n]\n\t"
        "s_cbranch_scc1 1b\n\t"
        "2:\n\t"
        "s_waitcnt lgkmcnt(0)"
        : [f] "=&v"(f), [cap] "+&s"(cap), [o0] "=&v"(o[0]), [o1] "=&v"(o[1]), [o2] "=&v"(o[2]) : [fa] "v"(fa), [a] "v"(a), [n] "s"(n) : "s92", "scc", "memory");
}
__device__ __forceinline__ void spin_rd(int c, unsigned n, unsigned a, bf16x8 (&o)[6]) {
    unsigned f, cap = SPIN_LDS;
    const unsigned fa = L_CNT + 4 * c;
    asm volatile(
        "1:\n\t"
        "ds_read_b32 %[f], %[fa]\n\t"
        "ds_read_b128 %[o0], %[a] offset:0\n\t"
        "ds_read_b128 %[o1], %[a] offset:64\n\t"
        "ds_read_b128 %[o2], %[a] offset:128\n\t"
        "ds_read_b128 %[o3], %[a] offset:192\n\t"
        "ds_read_b128 %[o4], %[a] offset:256\n\t"
        "ds_read_b128 %[o5], %[a] offset:320\n\t"
        "s_sub_u32 %[cap], %[cap], 1\n\t"
        "s_cbranch_scc1 2f\n\t"
        "s_waitcnt lgkmcnt(6)\n\t"
        "v_readfirstlane_b32 s92, %[f]\n\t"
        "s_cmp_lt_u32 s92, %[n]\n\t"
        "s_cbranch_scc1 1b\n\t"
        "2:\n\t"
        "s_waitcnt lgkmcnt(0)"
        : [f] "=&v"(f), [cap] "+&s"(cap), [o0] "=&v"(o[0]), [o1] "=&v"(o[1]), [o2] "=&v"(o[2]), [o3] "=&v"(o[3]), [o4] "=&v"(o[4]), [o5] "=&v"(o[5]) : [fa] "v"(fa), [a] "v"(a), [n] "s"(n) : "s92", "scc", "memory");
}
template <int OFF> __device__ __forceinline__ void spin_tr(int c, unsigned n, const unsigned (&a)[3], s16x4 (&o)[6]) {
    unsigned f, cap = SPIN_LDS;
    const unsigned fa = L_CNT + 4 * c;
    asm volatile(
        "1:\n\t"
        "ds_read_b32 %[f], %[fa]\n\t"
        "ds_read_b64_tr_b16 %[o0], %[a0] offset:%c[off0]\n\t"
        "ds_read_b64_tr_b16 %[o1], %[a0] offset:%c[off1]\n\t"
        "ds_read_b64_tr_b16 %[o2], %[a1] offset:%c[off0]\n\t"
        "ds_read_b64_tr_b16 %[o3], %[a1] offset:%c[off1]\n\t"
        "ds_read_b64_tr_b16 %[o4], %[a2] offset:%c[off0]\n\t"
        "ds_read_b64_tr_b16 %[o5], %[a2] offset:%c[off1]\n\t"
        "s_sub_u32 %[cap], %[cap], 1\n\t"
        "s_cbranch_scc1 2f\n\t"
        "s_waitcnt lgkmcnt(6)\n\t"
        "v_readfirstlane_b32 s92, %[f]\n\t"
        "s_cmp_lt_u32 s92, %[n]\n\t"
        "s_cbranch_scc1 1b\n\t"
        "2:\n\t"
        "s_waitcnt lgkmcnt(0)"
        : [f] "=&v"(f), [cap] "+&s"(cap), [o0] "=&v"(o[0]), [o1] "=&v"(o[1]), [o2] "=&v"(o[2]), [o3] "=&v"(o[3]), [o4] "=&v"(o[4]), [o5] "=&v"(o[5])
        : [fa] "v"(fa), [a0] "v"(a[0]), [a1] "v"(a[1]), [a2] "v"(a[2]), [n] "s"(n), [off0] "n"(OFF), [off1] "n"(OFF + 1536) : "s92", "scc", "memory");
}
__device__ __forceinline__ void ln_stats(const unsigned (&xp)[5], float& mb, float& rden) {
    const float c = lo16(__builtin_amdgcn_readfirstlane(xp[0]));
    float s = 0.f, s2 = 0.f;
#pragma unroll
    for (int i = 0; i < 5; ++i) {
        const float a = lo16(xp[i]) - c, b = hi16(xp[i]) - c;
        s += a + b; s2 = fmaf(a, a, s2); s2 = fmaf(b, b, s2);
    }
    s = wave_sum(s); s2 = wave_sum(s2);
    const float dm = s * (1.0f / D);
    const float var = fmaxf(s2 * (1.0f / D) - dm * dm, 0.f);
    mb = rbf(c + dm);
    const float den = rbf(__builtin_amdgcn_sqrtf(rbf(rbf(var) + 1e-5f)));
    rden = __builtin_amdgcn_rcpf(den);
}
__device__ __forceinline__ void ln_stats_fast(const unsigned (&xp)[5], float& mb, float& rden) {
    float s = dot2(xp[0], 0x3F803F80u, 0.f), s2 = dot2(xp[0], xp[0], 0.f);
#pragma unroll
    for (int i = 1; i < 5; ++i) { s = dot2(xp[i], 0x3F803F80u, s); s2 = dot2(xp[i], xp[i], s2); }
    auto w = __builtin_amdgcn_permlane32_swap(__float_as_uint(s), __float_as_uint(s2), false, false);
    float t = fv(w[0]) + fv(w[1]);
    t += dpp<XOR1>(t); t += dpp<XOR2>(t); t += dpp<XOR7>(t); t += dpp<XOR8>(t);
    t = sw16_sum(t) * (1.0f / D);
    auto u = __builtin_amdgcn_permlane32_swap(__float_as_uint(t), __float_as_uint(t), false, false);
    const float mean = fv(u[0]);
    const float var = fmaxf(fmaf(-mean, mean, fv(u[1])), 0.f);
    mb = rbf(mean);
    rden = __builtin_amdgcn_rcpf(rbf(__builtin_amdgcn_sqrtf(rbf(rbf(var) + 1e-5f))));
}
__device__ __forceinline__ unsigned ln_pair(unsigned w, float mb, float rden) {
    const unsigned d = pk2(lo16(w) - mb, hi16(w) - mb);
    return pk2(lo16(d) * rden, hi16(d) * rden);
}

__device__ __forceinline__ unsigned lateness(rsrc_t rs, int lane, u64 t_start) {
    const v2u t = __builtin_amdgcn_raw_buffer_load_b64(rs, WS_T0 + (lane & 7) * 128, 0, 16);
    const u64 d = t_start - (((u64)t[1] << 32) | t[0]);
    float l = d < 150ull ? (float)(unsigned)d : 0.f;
    l = fmaxf(l, dpp<XOR1>(l)); l = fmaxf(l, dpp<XOR2>(l)); l = fmaxf(l, dpp<XOR7>(l));
    return __builtin_amdgcn_readfirstlane((unsigned)l);
}
#define STAMP(i) do { } while (0)

#define LD_REPORT \
    "s_getreg_b32 s92, hwreg(HW_REG_IB_STS)\n\t" \
    "s_and_b32 s93, s92, 15\n\t" \
    "s_bfe_u32 s92, s92, 0x20016\n\t" \
    "s_lshl4_add_u32 s93, s92, s93\n\t" \
    "s_sub_u32 s92, %[iss], s93\n\t" \
    "v_mov_b32 %[vt], s92\n\t"
#define LD_WRITE \
    "s_mov_b64 exec, 1\n\t" \
    "ds_write_b32 %[va], %[vt]\n\t" \
    "s_mov_b64 exec, -1\n\t"
struct Ld { unsigned issued, tnext, pace, va, sh, w0; };
template <bool FLIP> __device__ __forceinline__ void ld_stream(Ld& s, rsrc_t rs, unsigned (&o)[6], unsigned soff, unsigned sstep, unsigned dst, int ng) {
    unsigned vt;
    const unsigned end = __builtin_amdgcn_readfirstlane(s.issued + 6 * ng);
    soff = __builtin_amdgcn_readfirstlane(soff); dst = __builtin_amdgcn_readfirstlane(dst);
    s.issued = __builtin_amdgcn_readfirstlane(s.issued); s.tnext = __builtin_amdgcn_readfirstlane(s.tnext); s.pace = __builtin_amdgcn_readfirstlane(s.pace);
    asm volatile(
        "s_memrealtime s[90:91]\n\t"
        "1:\n\t"
        LD_REPORT
        "s_waitcnt lgkmcnt(0)\n\t"
        "s_lshl_b32 s92, s90, 2\n\t"
        "s_memrealtime s[90:91]\n\t"
        LD_WRITE
        "s_sub_u32 s92, s92, %[tn]\n\t"
        "s_cmp_lt_i32 s92, 0\n\t"
        "s_cbranch_scc1 1b\n\t"
        "s_lshr_b32 s92, s92, %[sh]\n\t"
        "s_add_u32 s92, s92, %[w0]\n\t"
        "s_min_u32 s92, s92, %[wm]\n\t"
        "s_cmp_gt_u32 s93, s92\n\t"
        "s_cbranch_scc1 1b\n\t"
        "s_mov_b32 m0, %[dst]\n\t"
        "s_add_u32 %[tn], %[tn], %[pace]\n\t"
        "buffer_load_dwordx4 %[o0], %[rs], %[so] offen nt lds\n\t"
        "buffer_load_dwordx4 %[o1], %[rs], %[so] offen offset:1024 nt lds\n\t"
        "buffer_load_dwordx4 %[o2], %[rs], %[so] offen offset:2048 nt lds\n\t"
        "s_add_u32 m0, %[dst], 3072\n\t"
        "s_add_u32 %[iss], %[iss], 6\n\t"
        "buffer_load_dwordx4 %[o3], %[rs], %[so] offen nt lds\n\t"
        "buffer_load_dwordx4 %[o4], %[rs], %[so] offen offset:1024 nt lds\n\t"
        "buffer_load_dwordx4 %[o5], %[rs], %[so] offen offset:2048 nt lds\n\t"
        "s_add_u32 %[dst], %[dst], 6144\n\t"
        "s_add_u32 %[so], %[so], %[ss]\n\t"
        ".if %c[flip]\n\t"
        "v_xor_b32 %[o0], 0x80, %[o0]\n\tv_xor_b32 %[o1], 0x80, %[o1]\n\tv_xor_b32 %[o2], 0x80, %[o2]\n\t"
        "v_xor_b32 %[o3], 0x80, %[o3]\n\tv_xor_b32 %[o4], 0x80, %[o4]\n\tv_xor_b32 %[o5], 0x80, %[o5]\n\t"
        ".endif\n\t"
        "s_cmp_lg_u32 %[iss], %[end]\n\t"
        "s_cbranch_scc1 1b\n\t"
        "s_waitcnt lgkmcnt(0)"
        : [iss] "+&s"(s.issued), [tn] "+&s"(s.tnext), [dst] "+&s"(dst), [so] "+&s"(soff), [vt] "=&v"(vt),
          [o0] "+&v"(o[0]), [o1] "+&v"(o[1]), [o2] "+&v"(o[2]), [o3] "+&v"(o[3]), [o4] "+&v"(o[4]), [o5] "+&v"(o[5])
        : [rs] "s"(rs), [ss] "s"(sstep), [end] "s"(end), [pace] "s"(s.pace), [va] "v"(s.va), [wm] "n"(WMAX - 6), [sh] "s"(s.sh), [w0] "s"(s.w0), [flip] "n"(FLIP ? 1 : 0)
        : "s90", "s91", "s92", "s93", "scc", "memory");
    s.issued = end;
    s.tnext = __builtin_amdgcn_readfirstlane(s.tnext);
}
__device__ __forceinline__ void ld_gate(Ld& s, int c, unsigned n) {
    unsigned vt, vg, cap = SPIN_LDS;
    const unsigned ga = L_CNT + 4 * c;
    asm volatile(
        "1:\n\t"
        "ds_read_b32 %[vg], %[ga]\n\t"
        LD_REPORT
        LD_WRITE
        "s_sub_u32 %[cap], %[cap], 1\n\t"
        "s_cbranch_scc1 2f\n\t"
        "s_waitcnt lgkmcnt(1)\n\t"
        "v_readfirstlane_b32 s92, %[vg]\n\t"
        "s_cmp_lt_u32 s92, %[n]\n\t"
        "s_cbranch_scc1 1b\n\t"
        "2:\n\t"
        "s_waitcnt lgkmcnt(0)"
        : [vt] "=&v"(vt), [vg] "=&v"(vg), [cap] "+&s"(cap)
        : [iss] "s"(s.issued), [va] "v"(s.va), [ga] "v"(ga), [n] "s"(n)
        : "s92", "s93", "scc", "memory");
}
__device__ __forceinline__ void ld_drain(Ld& s) {
    unsigned vt;
    asm volatile(
        "1:\n\t"
        LD_REPORT
        LD_WRITE
        "s_cmp_lg_u32 s93, 0\n\t"
        "s_cbranch_scc1 1b\n\t"
        : [vt] "=&v"(vt) : [iss] "s"(s.issued), [va] "v"(s.va) : "s92", "s93", "scc", "memory");
}

__global__ void __launch_bounds__(640) k_block(const u16* __restrict__ x, const u16* __restrict__ kc, const u16* __restrict__ vc,
                                               const u16* __restrict__ wq, const u16* __restrict__ wo, const u16* __restrict__ wg,
                                               const u16* __restrict__ wu, const u16* __restrict__ wd, u16* __restrict__ out,
                                               unsigned char* ws KNARG) {
    extern __shared__ __attribute__((aligned(16))) unsigned char lds_raw[];
    LAS unsigned char* L = (LAS unsigned char*)lds_raw;
    unsigned ka0, ka1;
    asm volatile("s_load_dword %0, %2, 0x0\n\ts_load_dword %1, %2, 0x40" : "=&s"(ka0), "=&s"(ka1) : "s"(__builtin_amdgcn_kernarg_segment_ptr()));
#define CODEPF 18944
    if (__builtin_expect((unsigned)__builtin_amdgcn_readfirstlane(threadIdx.x >> 6) == 8u, 1)) {
        const rsrc_t rc = mkrs((const void*)(__builtin_amdgcn_s_getpc() & ~255ull), CODEPF);
        asm volatile("s_mov_b32 m0, %0\n\ts_nop 0\n\tbuffer_load_dwordx4 %1, %2, %3 offen lds" :: "s"(L_JUNK), "v"((threadIdx.x & 63) * 16), "s"(rc), "s"((blockIdx.x >> 3) * 1024) : "memory");
    }
    if (threadIdx.x < 20) ((LAS unsigned*)(L + L_CNT))[threadIdx.x] = 0u;
    const int b = blockIdx.x, xcd = b & 7, idx = b >> 3;
    const int h = xcd * 4 + (idx >> 3), j = idx & 7;
    const int tid = threadIdx.x, lane = tid & 63, wave = __builtin_amdgcn_readfirstlane(tid >> 6);
    const int g = lane >> 4, tq = (lane & 15) >> 2, tp = lane & 3, i16 = lane & 15;
    const bool qcu = j >= 2;
    const int qr = (j + 3) >> 3, qc = j - 2 - 3 * qr;
    const int gi = (idx >> 3) * 2 + j;
    const int gcu = (!qcu && gi < 6) ? xcd * 6 + gi : -1;
    const bool pre = qcu || gcu >= 0;
    const unsigned e0 = g * 256 + (i16 ^ (g << 1)) * 16;
    unsigned q0 = qcu ? (lane >> 3) * 12288 + ((lane & 7) ^ (g & 1) * 2) * 16 : e0, q1 = qcu ? (q0 ^ 64) + 98304 - 1024 : e0;
    unsigned q2 = qcu ? q0 + 2 * (98304 - 1024) : e0 ^ 128, q3 = qcu ? q1 + 2 * (98304 - 1024) : e0 ^ 128;
    unsigned qs = qcu ? 393216 : 4096, qb = qcu ? (320 * qr * (H * DH) + h * DH + 64 * qc) * 2 : 32 * gcu * D * 2;
    unsigned qn = (wave == 4 && pre) ? 10 : 0;
    qs = __builtin_amdgcn_readfirstlane(qs); qb = __builtin_amdgcn_readfirstlane(qb); qn = __builtin_amdgcn_readfirstlane(qn);
    asm volatile("" : "+v"(q0), "+v"(q1), "+v"(q2), "+v"(q3));
    asm volatile("" : "+s"(qs), "+s"(qb), "+s"(qn));
    SB();
    asm volatile("s_waitcnt lgkmcnt(0)\n\ts_barrier" :: "s"(ka0), "s"(ka1), "s"(x), "s"(kc), "s"(vc), "s"(wq), "s"(wo), "s"(wg), "s"(wu), "s"(wd), "s"(out), "s"(ws) : "memory");
    const u64 t_start = __builtin_amdgcn_s_memrealtime();
    if (__builtin_expect(qn != 0, 1)) {
        const rsrc_t rq = mkrs(qcu ? wq : wd, qcu ? D * H * DH * 2 : F * D * 2);
#pragma unroll 1
        for (unsigned n = 0; n < 10; ++n, qb += qs, nap(KN(20)))
            asm volatile("s_mov_b32 m0, %0\n\ts_nop 0\n\tbuffer_load_dwordx4 %1, %5, %6 offen nt lds\n\tbuffer_load_dwordx4 %2, %5, %6 offen offset:1024 nt lds\n\t"
                         "buffer_load_dwordx4 %3, %5, %6 offen offset:2048 nt lds\n\tbuffer_load_dwordx4 %4, %5, %6 offen offset:3072 nt lds"
                         :: "s"(L_W + n * 4096), "v"(q0), "v"(q1), "v"(q2), "v"(q3), "s"(rq), "s"(qb) : "memory");
        waitvm<0>();
        lds_signal(C_WQ);
    }
    unsigned tag, xp[5];
    asm volatile("" : "=v"(xp[0]), "=v"(xp[1]), "=v"(xp[2]), "=v"(xp[3]), "=v"(xp[4]), "=v"(tag));
    if (__builtin_expect(wave == 9, 1)) {
        const unsigned xo = lane * 4, zo = 0;
        asm volatile("global_load_dword %0, %6, %7\n\tglobal_load_dword %1, %6, %7 offset:256\n\tglobal_load_dword %2, %6, %7 offset:512\n\t"
                     "global_load_dword %3, %6, %7 offset:768\n\tglobal_load_dword %4, %6, %7 offset:1024\n\tglobal_load_dword %5, %8, %9 sc1"
                     : "+v"(xp[0]), "+v"(xp[1]), "+v"(xp[2]), "+v"(xp[3]), "+v"(xp[4]), "+v"(tag) : "v"(xo), "s"(x), "v"(zo), "s"(ws) : "memory");
    }
    const rsrc_t rws = mkrs(ws, WS_END);
    if (__builtin_expect(wave == 7 && idx == 0, 1)) __builtin_amdgcn_raw_buffer_store_b64(v2u{(unsigned)t_start, (unsigned)(t_start >> 32)}, rws, lane == 0 ? WS_T0 + xcd * 128 : OOB, 0, 16);
    const bool wocu = j < 5;
    const int oi = (idx >> 3) * 3 + (j - 2), oc = (idx >> 3) * 3 + (j - 5);
    const int ocu = (wocu && qcu && oi < 5) ? xcd * 5 + oi : -1;
    const int n1 = __builtin_amdgcn_readfirstlane(!pre ? N1G + 4 : wocu ? N1G : 0);
    const int pv0 = P_K + 6 * n1;
    const f32x4 z4 = {0.f, 0.f, 0.f, 0.f};
    auto sel4 = [&](float a0, float a1, float a2, float a3) {
        const float lo = (lane & 16) ? a1 : a0, hi = (lane & 16) ? a3 : a2;
        return (lane & 32) ? hi : lo;
    };
    auto t_end = [&]() { return t_start - __builtin_amdgcn_readfirstlane(*(volatile LAS unsigned*)(L + L_CNT + 4 * C_LATE)) + TE; };
    const int col = 4 * tp + g;
    auto make_h = [&](int a) {
        float xr = lo16(x[80 * xcd + 16 * a + col]);
        unsigned ha = L_O + (16 * a + col) * 2, ho = (tq == 0 && !(g & 1) && oc < 8) ? WS_HB + oc * 4096 + (40 * xcd + 8 * a + (col >> 1)) * 8 : OOB;
        PIN(ha); PIN(ho); PIN(xr);
        lds_wait<true>(L, C_OWN, 4);
        delay(D2);
        until(t_end() + G2);
        lds_wait<true>(L, C_PROG, P_WO);
        float s = sum_words2(rws, lane * 16, WS_AP + (5 * xcd + a) * 2048, tag);
        s += dpp<XOR8>(s); s += dpp<ROR4>(s);
        const unsigned hb = bfbits(xr + rbf(s));
        *(LAS u16*)(L + ha) = (u16)hb;
        lds_signal(C_O);
        auto pr = __builtin_amdgcn_permlane16_swap(hb, hb, false, false);
        put1(rws, ho, tag, pr[0] | (pr[1] << 16));
    };
    auto look_ap = [&](int i) {
        lds_wait<true>(L, C_O, 3);
        delay(DOBS);
        until(t_end() + G2);
        look_words(rws, lane * 16, WS_AP + (5 * xcd + i) * 2048, tag);
    };

    if (__builtin_expect(wave == 0, 1)) {
        __builtin_amdgcn_s_setprio(3);
        STAMP(14);
        unsigned ko[6], vo[6], po[6];
#pragma unroll
        for (int c = 0; c < 3; ++c) {
            const unsigned P = 64 * c + lane, r = P / 24, ch = P - r * 24;
            ko[c] = r * 384 + (ch ^ (r >> 1)) * 16 - c * 1024; ko[c + 3] = (ko[c] ^ 64) + 3072;
        }
        const size_t kvoff = ((size_t)h * SK + (SK - W) + j * 256) * DH;
        Ld s;
        s.issued = 0; s.va = L_CNT + 4 * C_PROG; s.w0 = KN(22); s.sh = KN(23);
        nap(KN(21));
        MEMBAR();
        const unsigned late = lateness(rws, lane, t_start);
        lds_set(C_LATE, late);
        s.pace = ((unsigned)(TE - (pre ? TAQ : TA0) - late) * (!pre ? 262144 / (35 + N1G) : wocu ? 262144 / (31 + N1G) : 262144 / 31)) >> 16;
        s.tnext = ((unsigned)t_start + (pre ? TAQ : TA0)) * 4;
        ld_stream<false>(s, mkrs(kc + kvoff, 98304), ko, 0, 6144, L_V, 16);
        STAMP(4);
#pragma unroll
        for (int c = 0; c < 3; ++c) {
            const unsigned P = 64 * c + lane, r = P / 24, ch = P - r * 24;
            vo[c] = r * 384 + (ch ^ (r & 2u)) * 16 - c * 1024; vo[c + 3] = (vo[c] ^ 64) + 3072;
            const unsigned wb = (i16 ^ (g << 1)) * 16;
            const unsigned rst = wocu ? 1280 : 3072;
            po[c] = (4 * c + g) * rst + (wb ^ ((c >> 1) << 7)) - c * 1024;
            po[c + 3] = (4 * c + 12 + g) * rst + (wb ^ ((((c + 3) >> 1) & 1) << 7)) - c * 1024;
        }
        const rsrc_t rv = mkrs(vc + kvoff, 98304);
        const rsrc_t ro = mkrs(wo + (size_t)(h * DH) * D + j * 128, 191 * 1280 + 256);
        ld_gate(s, C_QP, __builtin_amdgcn_readfirstlane(qcu ? 4 : pre ? 8 : 0));
        if (n1) ld_stream<true>(s, ro, po, 0, 30720, L_W, n1);
        ld_gate(s, C_KA, 4);
        ld_stream<false>(s, rv, vo, 0, 6144, L_V, 8);
        ld_gate(s, C_KB, 4);
        ld_stream<false>(s, rv, vo, 49152, 6144, L_V + 49152, 8);
        STAMP(6);
        s.pace = 0; s.w0 = WMAX - 6;
        if (wocu) {
            if (n1 < 8) ld_stream<true>(s, ro, po, n1 * 30720, 30720, L_W + n1 * 6144, 8 - n1);
        } else {
            const rsrc_t r0 = mkrs(wg + (size_t)(80 * xcd) * F + 128 * oc, 79 * 3072 + 256), r1 = mkrs(wu + (size_t)(80 * xcd) * F + 128 * oc, 79 * 3072 + 256);
            s.tnext += FDELAY * 4;
            ld_stream<true>(s, r0, po, 0, 73728, L_W, 4);
            ld_stream<true>(s, r1, po, 0, 73728, L_W + 24576, 4);
        }
        ld_drain(s);
        if (!wocu) STAMP(17); else STAMP(9);
        lds_wait<true>(L, C_X, 1);
        tag = __builtin_amdgcn_readfirstlane(*(LAS unsigned*)(L + L_TAG));
        if (gcu >= 0) {
            unsigned ha = L_HH + (4 * i16 + g) * 2, ca = L_LN + lane * 4;
            PIN(ha); PIN(ca);
            lds_wait<true>(L, C_GO, 1);
            STAMP(18);
            delay(D3);
            float sv = sum_words2(rws, lane * 16, WS_GP + gcu * 2048, tag);
            STAMP(12);
            f32x4 st;
            float cs;
            unsigned cap = SPIN_LDS;
#pragma unroll 1
            do {
                st = *(volatile LAS f32x4*)(L + L_ST);
                cs = *(volatile LAS float*)(L + ca);
            } while (__builtin_amdgcn_readfirstlane(__float_as_uint(st[2]) & __float_as_uint(st[3])) == 0u && --cap != 0);
            const float v = rbf((sv - st[0] * cs) * st[1]);
            const float sg = rbf(__builtin_amdgcn_rcpf(1.0f + __expf(-v)));
            *(LAS u16*)(L + ha) = (u16)bfbits(sg * dpp<XOR8>(v));
            lds_signal(C_HH);
        } else if (ocu >= 0) {
            const rsrc_t rout = mkrs(out, 1280);
            unsigned oo = tq == 0 ? (16 * ocu + col) * 2 : OOB;
            PIN(oo);
            look_ap(oi & 3);
            const u64 t4 = __builtin_amdgcn_s_memrealtime() + D3 + D4;
            delay(D3);
            v2u hres[1] = {v2u{0u, ~tag}};
            gather<4>(rws, (col >> 1) * 8, WS_HB + xcd * 4096 + 64 * ocu, 0, tag, hres);
            float hr = (g & 1) ? hi16(hres[0][0]) : lo16(hres[0][0]);
            PIN(hr);
            until(t4);
            STAMP(22);
            float s3 = sum_words3(rws, lane * 16, WS_DP + ocu * 3072, tag);
            s3 += dpp<XOR8>(s3); s3 += dpp<ROR4>(s3);
            __builtin_amdgcn_raw_buffer_store_b16((u16)bfbits(hr + rbf(s3)), rout, oo, 0, 0);
            if (ocu == 0 && lane == 0) __hip_atomic_store((gu32*)(ws + WS_EPOCH), tag, RLX_AGENT);
            STAMP(16);
        }
    } else if (wave == 9) {
        asm volatile("s_waitcnt vmcnt(0)" : "+v"(xp[0]), "+v"(xp[1]), "+v"(xp[2]), "+v"(xp[3]), "+v"(xp[4]), "+v"(tag) :: "memory");
        float mb, rden;
        ln_stats(xp, mb, rden);
#pragma unroll
        for (int i = 0; i < 5; ++i) *(LAS unsigned*)(L + L_LN + (lane + 64 * i) * 4) = ln_pair(xp[i], mb, rden);
        tag = __builtin_amdgcn_readfirstlane(tag) + 0x01010101u;
        if (lane == 0) *(LAS unsigned*)(L + L_TAG) = tag;
        lds_signal(C_X);
        STAMP(23);
        {
            until(t_start + TQ);
            v4u qp[3];
#pragma unroll
            for (int k = 0; k < 3; ++k) qp[k] = v4u{0u, ~tag, 0u, ~tag};
            gather<8>(rws, lane * 16, WS_QP + h * 3072, 1024, tag, qp);
#pragma unroll
            for (int k = 0; k < 3; ++k) *(LAS unsigned*)(L + L_Q + k * 128 + (lane & 31) * 4) = pk2(sw32_sum(fv(qp[k][0])), sw32_sum(fv(qp[k][2])));
            lds_signal(C_Q);
            STAMP(3);
        }
        lds_wait<true>(L, C_P, 4);
        const f32x4 mx = *(LAS f32x4*)(L + L_MX), ls = *(LAS f32x4*)(L + L_MX + 16);
        const float m = fmaxf(fmaxf(mx[0], mx[1]), fmaxf(mx[2], mx[3]));
        const float l = (ls[0] + ls[1]) + (ls[2] + ls[3]);
        put2(rws, lane == 0 ? WS_ST + (h * 8 + j) * 128 : OOB, tag, m, l);
        if (wocu) {
            until(t_end() + TS);
            const unsigned tt = lane < 8 ? ~tag : tag;
            v4u st[1] = {v4u{0u, tt, 0u, tt}};
            gather<8>(rws, lane * 128, WS_ST + h * 1024, 0, tag, st);
            const float ms = fv(st[0][0]);
            float M = fmaxf(ms, dpp<XOR1>(ms)); M = fmaxf(M, dpp<XOR2>(M)); M = fmaxf(M, dpp<XOR7>(M));
            const float e = __expf(ms - M);
            float Ls = e * fv(st[0][2]);
            Ls += dpp<XOR1>(Ls); Ls += dpp<XOR2>(Ls); Ls += dpp<XOR7>(Ls);
            if (lane < 8) *(LAS float*)(L + L_WGT + lane * 4) = e * __builtin_amdgcn_rcpf(Ls);
            lds_signal(C_W);
            STAMP(19);
            if (gcu >= 0) {
                look_ap(gi & 3);
                lds_signal(C_GO);
                delay(D3 - 15);
                v2u hv[5];
#pragma unroll
                for (int i = 0; i < 5; ++i) hv[i] = v2u{0u, ~tag};
                gather<0>(rws, lane * 8, WS_HB + xcd * 4096, 512, tag, hv);
#pragma unroll
                for (int i = 0; i < 5; ++i) xp[i] = hv[i][0];
                ln_stats_fast(xp, mb, rden);
                if (lane == 0) { *(LAS v2u*)(L + L_ST) = v2u{__float_as_uint(mb), __float_as_uint(rden)}; MEMBAR(); *(LAS float*)(L + L_ST + 8) = 1.0f; }
                STAMP(15);
            }
        } else {
            make_h(4);
        }
    } else {
        bf16x8 dw[5];
        if (gcu >= 0) {
            lds_wait<true>(L, C_WQ, 1);
#pragma unroll
            for (int t = 0; t < 5; ++t) {
                const int tt = 5 * (wave - 1) + t;
                unsigned ad[2];
#pragma unroll
                for (int u = 0; u < 2; ++u) {
                    const int r = 8 * g + 4 * u + tq, B = 5 * r + (tt >> 3);
                    ad[u] = L_W + r * 1280 + (tt ^ ((B & 3) | ((B >> 1) & 4))) * 32 + tp * 8;
                }
                dw[t] = cat8(trd(L, ad[0]), trd(L, ad[1]));
            }
            lds_signal(C_QP);
        }
        if (wave < 5) {
            const int a = wave - 1;
            lds_wait<true>(L, C_X, 1);
            tag = __builtin_amdgcn_readfirstlane(*(LAS unsigned*)(L + L_TAG));
            if (qcu) {
                bf16x8 xa[10];
#pragma unroll
                for (int s = 0; s < 10; ++s) xa[s] = *(LAS bf16x8*)(L + L_LN + (320 * qr + 32 * s + 8 * g) * 2);
                lds_wait<true>(L, C_WQ, 1);
                if (a == 0) STAMP(1);
                const unsigned ca = L_W + (8 * g + tq) * 128 + ((a ^ ((tq >> 1) | ((g & 1) << 1))) * 2 + (tp >> 1)) * 16 + (tp & 1) * 8;
                f32x4 q0 = z4, q1 = z4;
#pragma unroll
                for (int s = 0; s < 10; s += 2) {
                    q0 = mfma32(xa[s], cat8(trd(L, ca + s * 4096), trd(L, ca + s * 4096 + 512)), q0);
                    q1 = mfma32(xa[s + 1], cat8(trd(L, ca + s * 4096 + 4096), trd(L, ca + s * 4096 + 4608)), q1);
                }
                lds_signal(C_QP);
                put1(rws, lane < 16 ? WS_QP + h * 3072 + qc * 1024 + qr * 512 + (16 * a + lane) * 8 : OOB, tag, __float_as_uint(q0[0] + q1[0]));
                if (a == 0) STAMP(2);
            }
            if (wocu) {
                if (a < 3) {
                    unsigned oa = L_O + (64 * a + 4 * i16 + g) * 2, wa = L_OWN + (64 * a + 4 * i16) * 4;
                    PIN(oa); PIN(wa);
                    const u64 mo = ~(0xFFFFull << (16 * (j & 3)));
                    lds_wait<true>(L, C_W, 1);
                    float w0 = *(LAS float*)(L + L_WGT + 4 * g), w1 = *(LAS float*)(L + L_WGT + 16 + 4 * g);
                    PIN(w0); PIN(w1);
                    lds_wait<true>(L, C_PROG, pv0 + 84);
                    until(t_end() + G1);
                    bf16x8 own[1];
                    spin_rd(C_OWN, 4, wa, own);
                    const float o = sum_words_w(rws, g * 768 + i16 * 16, WS_OP + h * 6144 + a * 256, tag, __builtin_bit_cast(v4u, own[0]), j < 4 ? mo : ~0ull, j < 4 ? ~0ull : mo, w0, w1, D1);
                    *(LAS u16*)(L + oa) = (u16)bfbits(o);
                    lds_signal(C_O);
                    if (a == 0) STAMP(8);
                } else if (gcu >= 0) {
                    lds_wait<true>(L, C_GO, 1);
                    *(LAS float*)(L + L_LN + lane * 4) = sum_words2(rws, lane * 16, WS_CP + gcu * 2048, tag);
                    MEMBAR();
                    if (lane == 0) *(LAS float*)(L + L_ST + 12) = 1.0f;
                }
            } else {
                make_h(a);
                if (a == 0) STAMP(11);
            }
        } else {
            const int kw = wave - 5;
            bf16x8 kf[4][6];
            {
                const unsigned kb = L_V + (16 * kw + i16) * 384;
#pragma unroll
                for (int n = 0; n < 4; ++n) {
                    lds_wait<true>(L, C_PROG, 12 * ((kw >> 1) + 2 * n + 1));
#pragma unroll
                    for (int s = 0; s < 6; ++s) kf[n][s] = *(LAS bf16x8*)(L + kb + n * 24576 + ((4 * s + g) ^ (i16 >> 1)) * 16);
                    if (n & 1) lds_signal(n == 1 ? C_KA : C_KB);
                }
            }
            lds_wait<true>(L, C_Q, 1);
            tag = __builtin_amdgcn_readfirstlane(*(LAS unsigned*)(L + L_TAG));
            f32x4 sa[4] = {z4, z4, z4, z4};
#pragma unroll
            for (int s = 0; s < 6; ++s) {
                const bf16x8 qf = *(LAS bf16x8*)(L + L_Q + (32 * s + 8 * g) * 2);
#pragma unroll
                for (int n = 0; n < 4; ++n) sa[n] = mfma32(qf, kf[n][s], sa[n]);
            }
            const float raw = sel4(sa[0][0], sa[1][0], sa[2][0], sa[3][0]);
            const float s = rbf(rbf(raw) * 0.07216878364870322f);
            const float mw = wave_max(s);
            if (lane == 0) *(LAS float*)(L + L_MX + kw * 4) = mw;
            lds_signal(C_MAX);
            lds_wait<false>(L, C_MAX, 4);
            const f32x4 mx = *(LAS f32x4*)(L + L_MX);
            const float m = fmaxf(fmaxf(mx[0], mx[1]), fmaxf(mx[2], mx[3]));
            const float e = __expf(s - m);
            *(LAS u16*)(L + L_P + (16 * (kw + 4 * g) + i16) * 2) = (u16)bfbits(e);
            const float lw = wave_sum(e);
            if (lane == 0) *(LAS float*)(L + L_MX + 16 + kw * 4) = lw;
            lds_signal(C_P);
            lds_wait<false>(L, C_P, 4);
            if (kw == 0) STAMP(5);
            bf16x8 pf[8];
#pragma unroll
            for (int s8 = 0; s8 < 8; ++s8) pf[s8] = *(LAS bf16x8*)(L + L_P + (32 * s8 + 8 * g) * 2);
            const int fvs = (tq >> 1) | ((g & 1) << 1);
            unsigned vb[3], vl[3];
#pragma unroll
            for (int t = 0; t < 3; ++t) { vb[t] = (8 * g + tq) * 384 + (((3 * kw + t) ^ fvs) * 2 + (tp >> 1)) * 16 + (tp & 1) * 8; vl[t] = L_V + 6 * 12288 + vb[t]; PIN(vl[t]); }
            unsigned oo = lane < 48 ? WS_OP + ((h * 8 + j) * 192 + 48 * kw + lane) * 4 : OOB, ow = L_OWN + (lane < 48 ? 48 * kw + lane : 192 + lane) * 4;
            unsigned tb = (tag >> (8 * tp)) & 255u;
            PIN(tb);
            PIN(oo); PIN(ow);
            f32x4 oa[3] = {z4, z4, z4};
            lds_wait<true>(L, C_PROG, pv0 + 72);
#pragma unroll
            for (int s8 = 0; s8 < 6; ++s8)
#pragma unroll
                for (int t = 0; t < 3; ++t) oa[t] = mfma32(pf[s8], cat8(trd(L, L_V + vb[t] + s8 * 12288), trd(L, L_V + vb[t] + s8 * 12288 + 1536)), oa[t]);
            {
                s16x4 vf[6];
                spin_tr<0>(C_PROG, pv0 + 84, vl, vf);
#pragma unroll
                for (int t = 0; t < 3; ++t) oa[t] = mfma32(pf[6], cat8(vf[2 * t], vf[2 * t + 1]), oa[t]);
                spin_tr<12288>(C_PROG, pv0 + 96, vl, vf);
                if (kw == 0) STAMP(20);
#pragma unroll
                for (int t = 0; t < 3; ++t) oa[t] = mfma32(pf[7], cat8(vf[2 * t], vf[2 * t + 1]), oa[t]);
            }
            const unsigned op = enc(sel4(oa[0][0], oa[1][0], oa[2][0], oa[2][0]), tb);
            putw(rws, oo, op);
            *(LAS unsigned*)(L + ow) = op;
            lds_signal(C_OWN);
            if (kw == 0) STAMP(7);
            if (wocu) {
                unsigned ao = lane < 32 ? WS_AP + (((8 * j + 2 * kw + g) * 32 + h) * 16 + i16) * 4 : OOB, al = L_O + 16 * g;
                PIN(ao); PIN(al);
                lds_wait<false>(L, C_PROG, P_WO);
                bf16x8 bw[2][6], af[6];
                const int f3 = tq | ((g & 1) << 2);
#pragma unroll
                for (int t = 0; t < 2; ++t) {
                    const unsigned cb = L_W + (8 * g + tq) * 256 + (((2 * kw + t) ^ f3) * 2 + (tp >> 1)) * 16 + (tp & 1) * 8;
#pragma unroll
                    for (int s6 = 0; s6 < 6; ++s6) bw[t][s6] = cat8(trd(L, cb + s6 * 8192), trd(L, cb + s6 * 8192 + 1024));
                }
                spin_rd(C_O, 3, al, af);
                f32x4 a0 = z4, a1 = z4;
#pragma unroll
                for (int s6 = 0; s6 < 6; ++s6) { a0 = mfma32(af[s6], bw[0][s6], a0); a1 = mfma32(af[s6], bw[1][s6], a1); }
                putw(rws, ao, enc((lane & 16) ? a1[0] : a0[0], tb));
                if (kw == 0) STAMP(10);
            } else {
                lds_wait<true>(L, C_PROG, P_WO);
                bf16x8 bw[2][2][3], af[3];
                const int f3 = tq | ((g & 1) << 2);
#pragma unroll
                for (int mt = 0; mt < 2; ++mt)
#pragma unroll
                    for (int t = 0; t < 2; ++t) {
                        const unsigned cb = L_W + mt * 24576 + (8 * g + tq) * 256 + ((2 * kw + t) ^ f3) * 32 + tp * 8;
#pragma unroll
                        for (int s3 = 0; s3 < 3; ++s3) bw[mt][t][s3] = cat8(trd(L, cb + s3 * 8192), trd(L, cb + s3 * 8192 + 1024));
                    }
                unsigned go = WS_CP + ((4 * oc + kw) * 8 + xcd) * 256 + lane * 4, g2 = go + (WS_GP - WS_CP), al = L_O + 16 * g;
                PIN(al); PIN(g2);
                auto tile4 = [&](unsigned off) {
                    f32x4 d[2][2] = {{z4, z4}, {z4, z4}};
#pragma unroll
                    for (int s3 = 0; s3 < 3; ++s3)
#pragma unroll
                        for (int mt = 0; mt < 2; ++mt)
#pragma unroll
                            for (int t = 0; t < 2; ++t) d[mt][t] = mfma32(af[s3], bw[mt][t][s3], d[mt][t]);
                    SB();
                    putw(rws, off, enc(sel4(d[0][0][0], d[0][1][0], d[1][0][0], d[1][1][0]), tb));
                };
                const v4u ones = {0x3F803F80u, 0x3F803F80u, 0x3F803F80u, 0x3F803F80u};
                af[0] = af[1] = af[2] = asb(ones);
                tile4(go);
                *(LAS unsigned*)(L + L_O + 160 + (lane & 7) * 4) = 0u;
                spin_rd(C_O, 5, al, af);
                tile4(g2);
                if (kw == 0) STAMP(21);
            }
        }
        if (gcu >= 0) {
            const int w8 = wave - 1;
            unsigned d0 = WS_DP + (((5 * w8 + g) * 48 + gcu) * 16 + i16) * 4, d1 = lane < 16 ? WS_DP + (((5 * w8 + 4) * 48 + gcu) * 16 + i16) * 4 : OOB, al = L_HH + 16 * g;
            unsigned tb = (tag >> (8 * tp)) & 255u;
            PIN(tb);
            PIN(d0); PIN(d1); PIN(al);
            bf16x8 af[1];
            lds_wait<true>(L, C_GO, 1);
            spin_rd(C_HH, 1, al, af);
            f32x4 d[5];
#pragma unroll
            for (int t = 0; t < 5; ++t) d[t] = mfma32(af[0], dw[t], z4);
            SB();
            putw(rws, d0, enc(sel4(d[0][0], d[1][0], d[2][0], d[3][0]), tb));
            putw(rws, d1, enc(d[4][0], tb));
            if (w8 == 0) STAMP(13);
        }
    }
}

extern "C" void kernel_launch(void* const* d_in, const int* in_sizes, int n_in, void* d_out, int out_size, void* d_ws, size_t ws_size, hipStream_t stream) {
    (void)hipFuncSetAttribute((const void*)k_block, hipFuncAttributeMaxDynamicSharedMemorySize, L_END);
    k_block<<<256, 640, L_END, stream>>>((const u16*)d_in[0], (const u16*)d_in[1], (const u16*)d_in[2], (const u16*)d_in[3], (const u16*)d_in[4],
                                         (const u16*)d_in[6], (const u16*)d_in[7], (const u16*)d_in[8], (u16*)d_out, (unsigned char*)d_ws
        );
}
```

```cpp
#include <hip/hip_runtime.h>
#include <stdint.h>

typedef unsigned short u16;
typedef unsigned long long u64;
typedef unsigned v4u __attribute__((ext_vector_type(4)));
typedef unsigned v2u __attribute__((ext_vector_type(2)));
typedef float f32x4 __attribute__((ext_vector_type(4)));
typedef short s16x4 __attribute__((ext_vector_type(4)));
typedef __bf16 bf16x8 __attribute__((ext_vector_type(8)));
typedef __bf16 bf16x2 __attribute__((ext_vector_type(2)));
#define GAS __attribute__((address_space(1)))
#define LAS __attribute__((address_space(3)))
typedef GAS u64 gu64;
typedef GAS unsigned gu32;
typedef __amdgpu_buffer_rsrc_t rsrc_t;
#define RLX_AGENT __ATOMIC_RELAXED, __HIP_MEMORY_SCOPE_AGENT

constexpr int D = 640, H = 32, SK = 8192, DH = 192, W = 2048, F = 1536;

constexpr unsigned WS_EPOCH = 0;
constexpr unsigned WS_T0 = 1024;
constexpr unsigned WS_QP = 4096;
constexpr unsigned WS_ST = WS_QP + 32 * 3072;
constexpr unsigned WS_HB = WS_ST + 32 * 8 * 128;
constexpr unsigned WS_OP = WS_HB + 8 * 4096;
constexpr unsigned WS_AP = WS_OP + 32 * 6144;
constexpr unsigned WS_CP = WS_AP + 40 * 2048;
constexpr unsigned WS_GP = WS_CP + 48 * 2048;
constexpr unsigned WS_DP = WS_GP + 48 * 2048;
constexpr unsigned WS_TP = WS_DP + 40 * 3072;
constexpr unsigned WS_END = WS_TP + 8 * 8192;
constexpr unsigned OOB = 0x7FFFFF00u;

constexpr unsigned L_W = 0;
constexpr unsigned L_V = 49152;
constexpr unsigned L_Q = L_V + 98304;
constexpr unsigned L_P = L_Q + 384;
constexpr unsigned L_O = L_P + 512;
constexpr unsigned L_LN = L_O + 512;
constexpr unsigned L_OWN = L_LN + 1280;
constexpr unsigned L_HH = L_OWN + 1024;
constexpr unsigned L_MX = L_HH + 128;
constexpr unsigned L_WGT = L_MX + 32;
constexpr unsigned L_CNT = L_WGT + 32;
constexpr unsigned L_ST = L_CNT + 64;
constexpr unsigned L_TAG = L_ST + 16;
constexpr unsigned L_STAMP = L_TAG + 16;
constexpr unsigned L_JUNK = L_STAMP + 256;
constexpr unsigned L_END = L_JUNK + 1024;
static_assert(L_END <= 163840, "LDS");
enum { C_X = 0, C_PROG, C_KA, C_KB, C_Q, C_MAX, C_P, C_W, C_OWN, C_O, C_HH, C_WQ, C_QP, C_GO, C_LATE };
constexpr int P_K = 96, P_WO = 240;

#define KNOB_DEFAULTS {2, 40, 100, 862, 25, 400, -80, 0, 3, 150, 168, 100, 186, -8, 292, 98, 22, 16, 40, 50, 2, 4, 16, 4}
constexpr int kn_c[24] = KNOB_DEFAULTS;
#define KN(i) kn_c[i]
#define KNARG
#define N1G KN(0)
#define TA0 KN(1)
#define TAQ KN(2)
#define TE KN(3)
#define FDELAY KN(4)
#define TQ KN(5)
#define TS KN(6)
#define D1 KN(8)
#define D2 KN(9)
#define D3 KN(10)
#define D4 KN(11)
#define G1 KN(12)
#define DOBS KN(13)
#define G3 KN(15)
#define G2 KN(14)
constexpr int WMAX = 36;
constexpr unsigned SPIN_LDS = 1u << 24;
constexpr unsigned SPIN_GLB = 1u << 21;

__device__ __forceinline__ unsigned pk2(float lo, float hi) {
    const bf16x2 r = {(__bf16)lo, (__bf16)hi};
    return __builtin_bit_cast(unsigned, r);
}
__device__ __forceinline__ unsigned bfbits(float f) { return (unsigned)__builtin_bit_cast(u16, (__bf16)f); }
__device__ __forceinline__ float rbf(float f) { return __uint_as_float(bfbits(f) << 16); }
__device__ __forceinline__ float lo16(unsigned w) { return __uint_as_float(w << 16); }
__device__ __forceinline__ float hi16(unsigned w) { return __uint_as_float(w & 0xFFFF0000u); }
__device__ __forceinline__ float dot2(unsigned a, unsigned b, float c) {
    return __builtin_amdgcn_fdot2_f32_bf16(__builtin_bit_cast(bf16x2, a), __builtin_bit_cast(bf16x2, b), c, false);
}
template <int CTRL> __device__ __forceinline__ float dpp(float x) {
    return __builtin_bit_cast(float, __builtin_amdgcn_mov_dpp(__builtin_bit_cast(int, x), CTRL, 0xf, 0xf, true));
}
constexpr int XOR1 = 0xB1, XOR2 = 0x4E, XOR7 = 0x141, XOR8 = 0x128, ROR4 = 0x124;
__device__ __forceinline__ float sw16_sum(float x) {
    auto s = __builtin_amdgcn_permlane16_swap(__float_as_uint(x), __float_as_uint(x), false, false);
    return __uint_as_float(s[0]) + __uint_as_float(s[1]);
}
__device__ __forceinline__ float sw32_sum(float x) {
    auto s = __builtin_amdgcn_permlane32_swap(__float_as_uint(x), __float_as_uint(x), false, false);
    return __uint_as_float(s[0]) + __uint_as_float(s[1]);
}
__device__ __forceinline__ float sw16_max(float x) {
    auto s = __builtin_amdgcn_permlane16_swap(__float_as_uint(x), __float_as_uint(x), false, false);
    return fmaxf(__uint_as_float(s[0]), __uint_as_float(s[1]));
}
__device__ __forceinline__ float sw32_max(float x) {
    auto s = __builtin_amdgcn_permlane32_swap(__float_as_uint(x), __float_as_uint(x), false, false);
    return fmaxf(__uint_as_float(s[0]), __uint_as_float(s[1]));
}
__device__ __forceinline__ float wave_sum(float x) {
    x += dpp<XOR1>(x); x += dpp<XOR2>(x); x += dpp<XOR7>(x); x += dpp<XOR8>(x);
    return sw32_sum(sw16_sum(x));
}
__device__ __forceinline__ float wave_max(float x) {
    x = fmaxf(x, dpp<XOR1>(x)); x = fmaxf(x, dpp<XOR2>(x)); x = fmaxf(x, dpp<XOR7>(x)); x = fmaxf(x, dpp<XOR8>(x));
    return sw32_max(sw16_max(x));
}
__device__ __forceinline__ rsrc_t mkrs(const void* p, unsigned bytes) { return __builtin_amdgcn_make_buffer_rsrc((void*)p, 0, bytes, 0x00020000); }

template <int N> __device__ __forceinline__ void waitvm() { asm volatile("s_waitcnt vmcnt(%0)" :: "n"(N) : "memory"); }
#define LGKM_DRAIN() asm volatile("s_waitcnt lgkmcnt(0)" ::: "memory")
#define MEMBAR() asm volatile("" ::: "memory")
#define BAR() do { __builtin_amdgcn_sched_barrier(0); asm volatile("s_barrier" ::: "memory"); __builtin_amdgcn_sched_barrier(0); } while (0)

__device__ __forceinline__ void lds_signal(int c) {
    const unsigned addr = L_CNT + 4 * c, one = 1u;
    asm volatile("s_mov_b64 exec, 1\n\tds_add_u32 %0, %1\n\ts_mov_b64 exec, -1" :: "v"(addr), "v"(one) : "memory");
}
__device__ __forceinline__ void lds_set(int c, unsigned val) {
    const unsigned addr = L_CNT + 4 * c;
    asm volatile("s_mov_b64 exec, 1\n\tds_write_b32 %0, %1\n\ts_mov_b64 exec, -1" :: "v"(addr), "v"(val) : "memory");
}
template <bool SLEEP> __device__ __forceinline__ void lds_wait(LAS unsigned char* L, int c, unsigned n) {
    volatile LAS unsigned* p = (volatile LAS unsigned*)(L + L_CNT) + c;
    unsigned cap = SPIN_LDS;
#pragma unroll 1
    do {
        if ((unsigned)__builtin_amdgcn_readfirstlane(*p) >= n) break;
        if (SLEEP) __builtin_amdgcn_s_sleep(1);
        MEMBAR();
    } while (--cap != 0);
    MEMBAR();
}
__device__ __forceinline__ void until(u64 t) { while (__builtin_amdgcn_s_memrealtime() < t) __builtin_amdgcn_s_sleep(1); }
__device__ __forceinline__ void until_hot(u64 t) { until(t - 12); while (__builtin_amdgcn_s_memrealtime() < t) { } }
__device__ __forceinline__ void delay(int ticks) { until(__builtin_amdgcn_s_memrealtime() + ticks); }
__device__ __forceinline__ void nap(int n) { for (int i = 0; i < n; ++i) __builtin_amdgcn_s_sleep(1); }
#define PIN(x) asm volatile("" : "+v"(x))
#define SB() __builtin_amdgcn_sched_barrier(0)
__device__ __forceinline__ float fv(unsigned u) { return __uint_as_float(u); }
__device__ __forceinline__ void put1(rsrc_t rs, unsigned off, unsigned tag, unsigned val) { __builtin_amdgcn_raw_buffer_store_b64(v2u{val, tag}, rs, off, 0, 16); }
__device__ __forceinline__ void put2(rsrc_t rs, unsigned off, unsigned tag, float a, float b) {
    __builtin_amdgcn_raw_buffer_store_b128(v4u{__float_as_uint(a), tag, __float_as_uint(b), tag}, rs, off, 0, 16);
}
__device__ __forceinline__ u64 badmask(v2u x, unsigned tag) {
    u64 m;
    asm volatile("v_cmp_ne_u32_e64 %0, %1, %2" : "=s"(m) : "s"(tag), "v"(x[1]));
    return m;
}
__device__ __forceinline__ u64 badmask(v4u x, unsigned tag) {
    u64 m;
    const unsigned t = __builtin_amdgcn_bitop3_b32(x[1], x[3], tag, 0x7E);
    asm volatile("v_cmp_ne_u32_e64 %0, 0, %1" : "=s"(m) : "v"(t));
    return m;
}
__device__ __forceinline__ void gload(v2u& x, u64 m, rsrc_t rs, unsigned voff, unsigned soff) {
    asm volatile("s_mov_b64 exec, %2\n\tbuffer_load_dwordx2 %0, %1, %3, %4 offen sc1\n\ts_mov_b64 exec, -1" : "+v"(x) : "v"(voff), "s"(m), "s"(rs), "s"(soff) : "memory");
}
__device__ __forceinline__ void gload(v4u& x, u64 m, rsrc_t rs, unsigned voff, unsigned soff) {
    asm volatile("s_mov_b64 exec, %2\n\tbuffer_load_dwordx4 %0, %1, %3, %4 offen sc1\n\ts_mov_b64 exec, -1" : "+v"(x) : "v"(voff), "s"(m), "s"(rs), "s"(soff) : "memory");
}
template <int SLP, int N, class T> __device__ __forceinline__ unsigned gather(rsrc_t rs, unsigned voff, unsigned base, unsigned stride, unsigned tag, T (&x)[N]) {
    u64 m[N];
#pragma unroll
    for (int k = 0; k < N; ++k) { PIN(x[k]); m[k] = badmask(x[k], tag); }
    unsigned cap = SPIN_GLB;
#pragma unroll 1
    do {
#pragma unroll
        for (int k = 0; k < N; ++k) gload(x[k], m[k], rs, voff, base + k * stride);
        waitvm<0>();
#pragma unroll
        for (int k = 0; k < N; ++k) PIN(x[k]);
        u64 any = 0;
#pragma unroll
        for (int k = 0; k < N; ++k) { m[k] = badmask(x[k], tag); any |= m[k]; }
        if (!any) break;
        if (SLP) __builtin_amdgcn_s_sleep(SLP);
        MEMBAR();
    } while (--cap != 0);
    return cap;
}
__device__ __forceinline__ unsigned enc(float v, unsigned tb) { return (__float_as_uint(v) & 0xFFFFFF00u) | tb; }
__device__ __forceinline__ void putw(rsrc_t rs, unsigned off, unsigned w) { __builtin_amdgcn_raw_buffer_store_b32(w, rs, off, 0, 16); }
__device__ __forceinline__ float fold4(float a0, float a1, float a2, float a3) {
    auto p = __builtin_amdgcn_permlane32_swap(__float_as_uint(a0), __float_as_uint(a2), false, false);
    auto q = __builtin_amdgcn_permlane32_swap(__float_as_uint(a1), __float_as_uint(a3), false, false);
    auto r = __builtin_amdgcn_permlane16_swap(__float_as_uint(fv(p[0]) + fv(p[1])), __float_as_uint(fv(q[0]) + fv(q[1])), false, false);
    return fv(r[0]) + fv(r[1]);
}
__device__ __forceinline__ float sum_words2(rsrc_t rs, unsigned voff, unsigned base, unsigned tag, unsigned& cap) {
    cap = SPIN_GLB;
    float a0, a1, a2, a3;
    asm volatile(
        "buffer_load_dwordx4 v[144:147], %[vo], %[rs], %[b] offen offset:0 sc1\n\t"
        "buffer_load_dwordx4 v[148:151], %[vo], %[rs], %[b] offen offset:1024 sc1\n\t"
        "s_branch 2f\n\t"
        "1:\n\t"
        "s_mov_b64 exec, s[78:79]\n\t"
        "buffer_load_dwordx4 v[144:147], %[vo], %[rs], %[b] offen offset:0 sc1\n\t"
        "s_mov_b64 exec, s[80:81]\n\t"
        "buffer_load_dwordx4 v[148:151], %[vo], %[rs], %[b] offen offset:1024 sc1\n\t"
        "s_mov_b64 exec, -1\n\t"
        "s_sub_u32 %[cap], %[cap], 1\n\t"
        "s_cbranch_scc1 3f\n\t"
        "2:\n\t"
        "s_waitcnt vmcnt(0)\n\t"
        "v_xor_b32 v156, %[t0], v144\n\t"
        "v_xor_b32 v157, %[t1], v145\n\t"
        "v_xor_b32 v158, %[t2], v146\n\t"
        "v_xor_b32 v159, %[t3], v147\n\t"
        "v_xor_b32 v160, %[t0], v148\n\t"
        "v_xor_b32 v161, %[t1], v149\n\t"
        "v_xor_b32 v162, %[t2], v150\n\t"
        "v_xor_b32 v163, %[t3], v151\n\t"
        "v_or3_b32 v140, v156, v157, v158\n\t"
        "v_or3_b32 v141, v160, v161, v162\n\t"
        "v_bitop3_b32 v140, v140, v159, %[ff] bitop3:0xa8\n\t"
        "v_bitop3_b32 v141, v141, v163, %[ff] bitop3:0xa8\n\t"
        "v_cmp_ne_u32_e64 s[78:79], 0, v140\n\t"
        "v_cmp_ne_u32_e64 s[80:81], 0, v141\n\t"
        "s_or_b64 s[92:93], s[78:79], s[80:81]\n\t"
        "s_cbranch_scc1 1b\n\t"
        "3:\n\t"
        "s_waitcnt vmcnt(0)\n\t"
        "v_add_f32 %[a0], v156, v160\n\t"
        "v_add_f32 %[a1], v157, v161\n\t"
        "v_add_f32 %[a2], v158, v162\n\t"
        "v_add_f32 %[a3], v159, v163\n\t"
        "s_nop 1\n\t"
        : [a0] "=&v"(a0), [a1] "=&v"(a1), [a2] "=&v"(a2), [a3] "=&v"(a3), [cap] "+&s"(cap)
        : [vo] "v"(voff), [rs] "s"(rs), [b] "s"(base), [t0] "s"(tag & 255u), [t1] "s"((tag >> 8) & 255u), [t2] "s"((tag >> 16) & 255u), [t3] "s"(tag >> 24),
          [ff] "s"(255u)
        : "v140", "v141", "v142", "v144", "v145", "v146", "v147", "v148", "v149", "v150", "v151", "v156", "v157", "v158", "v159", "v160", "v161", "v162", "v163", "s78", "s79", "s80", "s81", "s92", "s93", "scc", "memory");
    return fold4(a0, a1, a2, a3);
}
__device__ __forceinline__ float sum_words3(rsrc_t rs, unsigned voff, unsigned base, unsigned tag, unsigned& cap) {
    cap = SPIN_GLB;
    float a0, a1, a2, a3;
    asm volatile(
        "buffer_load_dwordx4 v[144:147], %[vo], %[rs], %[b] offen offset:0 sc1\n\t"
        "buffer_load_dwordx4 v[148:151], %[vo], %[rs], %[b] offen offset:1024 sc1\n\t"
        "buffer_load_dwordx4 v[152:155], %[vo], %[rs], %[b] offen offset:2048 sc1\n\t"
        "s_branch 2f\n\t"
        "1:\n\t"
        "s_mov_b64 exec, s[78:79]\n\t"
        "buffer_load_dwordx4 v[144:147], %[vo], %[rs], %[b] offen offset:0 sc1\n\t"
        "s_mov_b64 exec, s[80:81]\n\t"
        "buffer_load_dwordx4 v[148:151], %[vo], %[rs], %[b] offen offset:1024 sc1\n\t"
        "s_mov_b64 exec, s[82:83]\n\t"
        "buffer_load_dwordx4 v[152:155], %[vo], %[rs], %[b] offen offset:2048 sc1\n\t"
        "s_mov_b64 exec, -1\n\t"
        "s_sub_u32 %[cap], %[cap], 1\n\t"
        "s_cbranch_scc1 3f\n\t"
        "2:\n\t"
        "s_waitcnt vmcnt(0)\n\t"
        "v_xor_b32 v156, %[t0], v144\n\t"
        "v_xor_b32 v157, %[t1], v145\n\t"
        "v_xor_b32 v158, %[t2], v146\n\t"
        "v_xor_b32 v159, %[t3], v147\n\t"
        "v_xor_b32 v160, %[t0], v148\n\t"
        "v_xor_b32 v161, %[t1], v149\n\t"
        "v_xor_b32 v162, %[t2], v150\n\t"
        "v_xor_b32 v163, %[t3], v151\n\t"
        "v_xor_b32 v164, %[t0], v152\n\t"
        "v_xor_b32 v165, %[t1], v153\n\t"
        "v_xor_b32 v166, %[t2], v154\n\t"
        "v_xor_b32 v167, %[t3], v155\n\t"
        "v_or3_b32 v140, v156, v157, v158\n\t"
        "v_or3_b32 v141, v160, v161, v162\n\t"
        "v_or3_b32 v142, v164, v165, v166\n\t"
        "v_bitop3_b32 v140, v140, v159, %[ff] bitop3:0xa8\n\t"
        "v_bitop3_b32 v141, v141, v163, %[ff] bitop3:0xa8\n\t"
        "v_bitop3_b32 v142, v142, v167, %[ff] bitop3:0xa8\n\t"
        "v_cmp_ne_u32_e64 s[78:79], 0, v140\n\t"
        "v_cmp_ne_u32_e64 s[80:81], 0, v141\n\t"
        "v_cmp_ne_u32_e64 s[82:83], 0, v142\n\t"
        "s_or_b64 s[92:93], s[78:79], s[80:81]\n\t"
        "s_or_b64 s[92:93], s[92:93], s[82:83]\n\t"
        "s_cbranch_scc1 1b\n\t"
        "3:\n\t"
        "s_waitcnt vmcnt(0)\n\t"
        "v_add_f32 %[a0], v156, v160\n\t"
        "v_add_f32 %[a1], v157, v161\n\t"
        "v_add_f32 %[a2], v158, v162\n\t"
        "v_add_f32 %[a3], v159, v163\n\t"
        "v_add_f32 %[a0], %[a0], v164\n\t"
        "v_add_f32 %[a1], %[a1], v165\n\t"
        "v_add_f32 %[a2], %[a2], v166\n\t"
        "v_add_f32 %[a3], %[a3], v167\n\t"
        "s_nop 1\n\t"
        : [a0] "=&v"(a0), [a1] "=&v"(a1), [a2] "=&v"(a2), [a3] "=&v"(a3), [cap] "+&s"(cap)
        : [vo] "v"(voff), [rs] "s"(rs), [b] "s"(base), [t0] "s"(tag & 255u), [t1] "s"((tag >> 8) & 255u), [t2] "s"((tag >> 16) & 255u), [t3] "s"(tag >> 24),
          [ff] "s"(255u)
        : "v140", "v141", "v142", "v144", "v145", "v146", "v147", "v148", "v149", "v150", "v151", "v152", "v153", "v154", "v155", "v156", "v157", "v158", "v159", "v160", "v161", "v162", "v163", "v164", "v165", "v166", "v167", "s78", "s79", "s80", "s81", "s82", "s83", "s92", "s93", "scc", "memory");
    return fold4(a0, a1, a2, a3);
}
__device__ __forceinline__ float sum_words_w(rsrc_t rs, unsigned voff, unsigned base, unsigned tag, unsigned& cap, v4u own, u64 m0, u64 m1, float w0, float w1, unsigned napn) {
    cap = SPIN_GLB;
    float a0, a1, a2, a3;
    asm volatile(
        "s_mov_b64 s[78:79], %[m0]\n\t"
        "v_mov_b32 v144, %[o0]\n\t"
        "v_mov_b32 v145, %[o1]\n\t"
        "v_mov_b32 v146, %[o2]\n\t"
        "v_mov_b32 v147, %[o3]\n\t"
        "s_mov_b64 s[80:81], %[m1]\n\t"
        "v_mov_b32 v148, %[o0]\n\t"
        "v_mov_b32 v149, %[o1]\n\t"
        "v_mov_b32 v150, %[o2]\n\t"
        "v_mov_b32 v151, %[o3]\n\t"
        "s_cmp_eq_u32 %[nap], 0\n\t"
        "s_cbranch_scc1 1f\n\t"
        "4:\n\t"
        "s_sleep 1\n\t"
        "s_sub_u32 %[nap], %[nap], 1\n\t"
        "s_cmp_lg_u32 %[nap], 0\n\t"
        "s_cbranch_scc1 4b\n\t"
        "1:\n\t"
        "s_mov_b64 exec, s[78:79]\n\t"
        "buffer_load_dwordx4 v[144:147], %[vo], %[rs], %[b] offen offset:0 sc1\n\t"
        "s_mov_b64 exec, s[80:81]\n\t"
        "buffer_load_dwordx4 v[148:151], %[vo], %[rs], %[b] offen offset:3072 sc1\n\t"
        "s_mov_b64 exec, -1\n\t"
        "s_sub_u32 %[cap], %[cap], 1\n\t"
        "s_cbranch_scc1 3f\n\t"
        "2:\n\t"
        "s_waitcnt vmcnt(0)\n\t"
        "v_xor_b32 v156, %[t0], v144\n\t"
        "v_xor_b32 v157, %[t1], v145\n\t"
        "v_xor_b32 v158, %[t2], v146\n\t"
        "v_xor_b32 v159, %[t3], v147\n\t"
        "v_xor_b32 v160, %[t0], v148\n\t"
        "v_xor_b32 v161, %[t1], v149\n\t"
        "v_xor_b32 v162, %[t2], v150\n\t"
        "v_xor_b32 v163, %[t3], v151\n\t"
        "v_or3_b32 v140, v156, v157, v158\n\t"
        "v_or3_b32 v141, v160, v161, v162\n\t"
        "v_bitop3_b32 v140, v140, v159, %[ff] bitop3:0xa8\n\t"
        "v_bitop3_b32 v141, v141, v163, %[ff] bitop3:0xa8\n\t"
        "v_cmp_ne_u32_e64 s[78:79], 0, v140\n\t"
        "v_cmp_ne_u32_e64 s[80:81], 0, v141\n\t"
        "s_or_b64 s[92:93], s[78:79], s[80:81]\n\t"
        "s_cbranch_scc1 1b\n\t"
        "3:\n\t"
        "s_waitcnt vmcnt(0)\n\t"
        "v_mul_f32 %[a0], %[w0], v156\n\t"
        "v_mul_f32 %[a1], %[w0], v157\n\t"
        "v_mul_f32 %[a2], %[w0], v158\n\t"
        "v_mul_f32 %[a3], %[w0], v159\n\t"
        "v_fmac_f32 %[a0], %[w1], v160\n\t"
        "v_fmac_f32 %[a1], %[w1], v161\n\t"
        "v_fmac_f32 %[a2], %[w1], v162\n\t"
        "v_fmac_f32 %[a3], %[w1], v163\n\t"
        "s_nop 1\n\t"
        : [a0] "=&v"(a0), [a1] "=&v"(a1), [a2] "=&v"(a2), [a3] "=&v"(a3), [cap] "+&s"(cap), [nap] "+&s"(napn)
        : [vo] "v"(voff), [rs] "s"(rs), [b] "s"(base), [t0] "s"(tag & 255u), [t1] "s"((tag >> 8) & 255u), [t2] "s"((tag >> 16) & 255u), [t3] "s"(tag >> 24),
          [ff] "s"(255u), [o0] "v"(own[0]), [o1] "v"(own[1]), [o2] "v"(own[2]), [o3] "v"(own[3]), [m0] "s"(m0), [m1] "s"(m1), [w0] "v"(w0), [w1] "v"(w1)
        : "v140", "v141", "v142", "v144", "v145", "v146", "v147", "v148", "v149", "v150", "v151", "v156", "v157", "v158", "v159", "v160", "v161", "v162", "v163", "s78", "s79", "s80", "s81", "s92", "s93", "scc", "memory");
    return fold4(a0, a1, a2, a3);
}
__device__ __forceinline__ s16x4 trd(LAS unsigned char* L, unsigned off) { return __builtin_amdgcn_ds_read_tr16_b64_v4i16((LAS s16x4*)(L + off)); }
__device__ __forceinline__ bf16x8 cat8(s16x4 a, s16x4 b) { return __builtin_bit_cast(bf16x8, __builtin_shufflevector(a, b, 0, 1, 2, 3, 4, 5, 6, 7)); }
__device__ __forceinline__ f32x4 mfma32(bf16x8 a, bf16x8 b, f32x4 c) { return __builtin_amdgcn_mfma_f32_16x16x32_bf16(a, b, c, 0, 0, 0); }
__device__ __forceinline__ bf16x8 asb(v4u a) { return __builtin_bit_cast(bf16x8, a); }

__device__ __forceinline__ void spin_rd(int c, unsigned n, unsigned a, bf16x8 (&o)[1]) {
    unsigned f, cap = SPIN_LDS;
    const unsigned fa = L_CNT + 4 * c;
    asm volatile(
        "1:\n\t"
        "ds_read_b32 %[f], %[fa]\n\t"
        "ds_read_b128 %[o0], %[a] offset:0\n\t"
        "s_sub_u32 %[cap], %[cap], 1\n\t"
        "s_cbranch_scc1 2f\n\t"
        "s_waitcnt lgkmcnt(1)\n\t"
        "v_readfirstlane_b32 s92, %[f]\n\t"
        "s_cmp_lt_u32 s92, %[n]\n\t"
        "s_cbranch_scc1 1b\n\t"
        "2:\n\t"
        "s_waitcnt lgkmcnt(0)"
        : [f] "=&v"(f), [cap] "+&s"(cap), [o0] "=&v"(o[0]) : [fa] "v"(fa), [a] "v"(a), [n] "s"(n) : "s92", "scc", "memory");
}
__device__ __forceinline__ void spin_rd(int c, unsigned n, unsigned a, bf16x8 (&o)[3]) {
    unsigned f, cap = SPIN_LDS;
    const unsigned fa = L_CNT + 4 * c;
    asm volatile(
        "1:\n\t"
        "ds_read_b32 %[f], %[fa]\n\t"
        "ds_read_b128 %[o0], %[a] offset:0\n\t"
        "ds_read_b128 %[o1], %[a] offset:64\n\t"
        "ds_read_b128 %[o2], %[a] offset:128\n\t"
        "s_sub_u32 %[cap], %[cap], 1\n\t"
        "s_cbranch_scc1 2f\n\t"
        "s_waitcnt lgkmcnt(3)\n\t"
        "v_readfirstlane_b32 s92, %[f]\n\t"
        "s_cmp_lt_u32 s92, %[n]\n\t"
        "s_cbranch_scc1 1b\n\t"
        "2:\n\t"
        "s_waitcnt lgkmcnt(0)"
        : [f] "=&v"(f), [cap] "+&s"(cap), [o0] "=&v"(o[0]), [o1] "=&v"(o[1]), [o2] "=&v"(o[2]) : [fa] "v"(fa), [a] "v"(a), [n] "s"(n) : "s92", "scc", "memory");
}
__device__ __forceinline__ void spin_rd(int c, unsigned n, unsigned a, bf16x8 (&o)[6]) {
    unsigned f, cap = SPIN_LDS;
    const unsigned fa = L_CNT + 4 * c;
    asm volatile(
        "1:\n\t"
        "ds_read_b32 %[f], %[fa]\n\t"
        "ds_read_b128 %[o0], %[a] offset:0\n\t"
        "ds_read_b128 %[o1], %[a] offset:64\n\t"
        "ds_read_b128 %[o2], %[a] offset:128\n\t"
        "ds_read_b128 %[o3], %[a] offset:192\n\t"
        "ds_read_b128 %[o4], %[a] offset:256\n\t"
        "ds_read_b128 %[o5], %[a] offset:320\n\t"
        "s_sub_u32 %[cap], %[cap], 1\n\t"
        "s_cbranch_scc1 2f\n\t"
        "s_waitcnt lgkmcnt(6)\n\t"
        "v_readfirstlane_b32 s92, %[f]\n\t"
        "s_cmp_lt_u32 s92, %[n]\n\t"
        "s_cbranch_scc1 1b\n\t"
        "2:\n\t"
        "s_waitcnt lgkmcnt(0)"
        : [f] "=&v"(f), [cap] "+&s"(cap), [o0] "=&v"(o[0]), [o1] "=&v"(o[1]), [o2] "=&v"(o[2]), [o3] "=&v"(o[3]), [o4] "=&v"(o[4]), [o5] "=&v"(o[5]) : [fa] "v"(fa), [a] "v"(a), [n] "s"(n) : "s92", "scc", "memory");
}
template <int OFF> __device__ __forceinline__ void spin_tr(int c, unsigned n, const unsigned (&a)[3], s16x4 (&o)[6]) {
    unsigned f, cap = SPIN_LDS;
    const unsigned fa = L_CNT + 4 * c;
    asm volatile(
        "1:\n\t"
        "ds_read_b32 %[f], %[fa]\n\t"
        "ds_read_b64_tr_b16 %[o0], %[a0] offset:%c[off0]\n\t"
        "ds_read_b64_tr_b16 %[o1], %[a0] offset:%c[off1]\n\t"
        "ds_read_b64_tr_b16 %[o2], %[a1] offset:%c[off0]\n\t"
        "ds_read_b64_tr_b16 %[o3], %[a1] offset:%c[off1]\n\t"
        "ds_read_b64_tr_b16 %[o4], %[a2] offset:%c[off0]\n\t"
        "ds_read_b64_tr_b16 %[o5], %[a2] offset:%c[off1]\n\t"
        "s_sub_u32 %[cap], %[cap], 1\n\t"
        "s_cbranch_scc1 2f\n\t"
        "s_waitcnt lgkmcnt(6)\n\t"
        "v_readfirstlane_b32 s92, %[f]\n\t"
        "s_cmp_lt_u32 s92, %[n]\n\t"
        "s_cbranch_scc1 1b\n\t"
        "2:\n\t"
        "s_waitcnt lgkmcnt(0)"
        : [f] "=&v"(f), [cap] "+&s"(cap), [o0] "=&v"(o[0]), [o1] "=&v"(o[1]), [o2] "=&v"(o[2]), [o3] "=&v"(o[3]), [o4] "=&v"(o[4]), [o5] "=&v"(o[5])
        : [fa] "v"(fa), [a0] "v"(a[0]), [a1] "v"(a[1]), [a2] "v"(a[2]), [n] "s"(n), [off0] "n"(OFF), [off1] "n"(OFF + 1536) : "s92", "scc", "memory");
}
__device__ __forceinline__ void ln_stats(const unsigned (&xp)[5], float& mb, float& rden) {
    const float c = lo16(__builtin_amdgcn_readfirstlane(xp[0]));
    float s = 0.f, s2 = 0.f;
#pragma unroll
    for (int i = 0; i < 5; ++i) {
        const float a = lo16(xp[i]) - c, b = hi16(xp[i]) - c;
        s += a + b; s2 = fmaf(a, a, s2); s2 = fmaf(b, b, s2);
    }
    s = wave_sum(s); s2 = wave_sum(s2);
    const float dm = s * (1.0f / D);
    const float var = fmaxf(s2 * (1.0f / D) - dm * dm, 0.f);
    mb = rbf(c + dm);
    const float den = rbf(__builtin_amdgcn_sqrtf(rbf(rbf(var) + 1e-5f)));
    rden = __builtin_amdgcn_rcpf(den);
}
__device__ __forceinline__ void ln_stats_fast(const unsigned (&xp)[5], float& mb, float& rden) {
    float s = dot2(xp[0], 0x3F803F80u, 0.f), s2 = dot2(xp[0], xp[0], 0.f);
#pragma unroll
    for (int i = 1; i < 5; ++i) { s = dot2(xp[i], 0x3F803F80u, s); s2 = dot2(xp[i], xp[i], s2); }
    auto w = __builtin_amdgcn_permlane32_swap(__float_as_uint(s), __float_as_uint(s2), false, false);
    float t = fv(w[0]) + fv(w[1]);
    t += dpp<XOR1>(t); t += dpp<XOR2>(t); t += dpp<XOR7>(t); t += dpp<XOR8>(t);
    t = sw16_sum(t) * (1.0f / D);
    auto u = __builtin_amdgcn_permlane32_swap(__float_as_uint(t), __float_as_uint(t), false, false);
    const float mean = fv(u[0]);
    const float var = fmaxf(fmaf(-mean, mean, fv(u[1])), 0.f);
    mb = rbf(mean);
    rden = __builtin_amdgcn_rcpf(rbf(__builtin_amdgcn_sqrtf(rbf(rbf(var) + 1e-5f))));
}
__device__ __forceinline__ unsigned ln_pair(unsigned w, float mb, float rden) {
    const unsigned d = pk2(lo16(w) - mb, hi16(w) - mb);
    return pk2(lo16(d) * rden, hi16(d) * rden);
}

__device__ __forceinline__ unsigned lateness(rsrc_t rs, int lane, u64 t_start) {
    const v2u t = __builtin_amdgcn_raw_buffer_load_b64(rs, WS_T0 + (lane & 7) * 128, 0, 16);
    const u64 d = t_start - (((u64)t[1] << 32) | t[0]);
    float l = d < 150ull ? (float)(unsigned)d : 0.f;
    l = fmaxf(l, dpp<XOR1>(l)); l = fmaxf(l, dpp<XOR2>(l)); l = fmaxf(l, dpp<XOR7>(l));
    return __builtin_amdgcn_readfirstlane((unsigned)l);
}
#define NOTE(i) do { } while (0)
#define STAMP(i) do { } while (0)

#define LD_REPORT \
    "s_getreg_b32 s92, hwreg(HW_REG_IB_STS)\n\t" \
    "s_and_b32 s93, s92, 15\n\t" \
    "s_bfe_u32 s92, s92, 0x20016\n\t" \
    "s_lshl4_add_u32 s93, s92, s93\n\t" \
    "s_sub_u32 s92, %[iss], s93\n\t" \
    "v_mov_b32 %[vt], s92\n\t"
#define LD_WRITE \
    "s_mov_b64 exec, 1\n\t" \
    "ds_write_b32 %[va], %[vt]\n\t" \
    "s_mov_b64 exec, -1\n\t"
struct Ld { unsigned issued, tnext, pace, va, sh, w0; };
template <bool FLIP> __device__ __forceinline__ void ld_stream(Ld& s, rsrc_t rs, unsigned (&o)[6], unsigned soff, unsigned sstep, unsigned dst, int ng) {
    unsigned vt;
    const unsigned end = __builtin_amdgcn_readfirstlane(s.issued + 6 * ng);
    soff = __builtin_amdgcn_readfirstlane(soff); dst = __builtin_amdgcn_readfirstlane(dst);
    s.issued = __builtin_amdgcn_readfirstlane(s.issued); s.tnext = __builtin_amdgcn_readfirstlane(s.tnext); s.pace = __builtin_amdgcn_readfirstlane(s.pace);
    asm volatile(
        "s_memrealtime s[90:91]\n\t"
        "1:\n\t"
        LD_REPORT
        "s_waitcnt lgkmcnt(0)\n\t"
        "s_lshl_b32 s92, s90, 2\n\t"
        "s_memrealtime s[90:91]\n\t"
        LD_WRITE
        "s_sub_u32 s92, s92, %[tn]\n\t"
        "s_cmp_lt_i32 s92, 0\n\t"
        "s_cbranch_scc1 1b\n\t"
        "s_lshr_b32 s92, s92, %[sh]\n\t"
        "s_add_u32 s92, s92, %[w0]\n\t"
        "s_min_u32 s92, s92, %[wm]\n\t"
        "s_cmp_gt_u32 s93, s92\n\t"
        "s_cbranch_scc1 1b\n\t"
        "s_mov_b32 m0, %[dst]\n\t"
        "s_add_u32 %[tn], %[tn], %[pace]\n\t"
        "buffer_load_dwordx4 %[o0], %[rs], %[so] offen nt lds\n\t"
        "buffer_load_dwordx4 %[o1], %[rs], %[so] offen offset:1024 nt lds\n\t"
        "buffer_load_dwordx4 %[o2], %[rs], %[so] offen offset:2048 nt lds\n\t"
        "s_add_u32 m0, %[dst], 3072\n\t"
        "s_add_u32 %[iss], %[iss], 6\n\t"
        "buffer_load_dwordx4 %[o3], %[rs], %[so] offen nt lds\n\t"
        "buffer_load_dwordx4 %[o4], %[rs], %[so] offen offset:1024 nt lds\n\t"
        "buffer_load_dwordx4 %[o5], %[rs], %[so] offen offset:2048 nt lds\n\t"
        "s_add_u32 %[dst], %[dst], 6144\n\t"
        "s_add_u32 %[so], %[so], %[ss]\n\t"
        ".if %c[flip]\n\t"
        "v_xor_b32 %[o0], 0x80, %[o0]\n\tv_xor_b32 %[o1], 0x80, %[o1]\n\tv_xor_b32 %[o2], 0x80, %[o2]\n\t"
        "v_xor_b32 %[o3], 0x80, %[o3]\n\tv_xor_b32 %[o4], 0x80, %[o4]\n\tv_xor_b32 %[o5], 0x80, %[o5]\n\t"
        ".endif\n\t"
        "s_cmp_lg_u32 %[iss], %[end]\n\t"
        "s_cbranch_scc1 1b\n\t"
        "s_waitcnt lgkmcnt(0)"
        : [iss] "+&s"(s.issued), [tn] "+&s"(s.tnext), [dst] "+&s"(dst), [so] "+&s"(soff), [vt] "=&v"(vt),
          [o0] "+&v"(o[0]), [o1] "+&v"(o[1]), [o2] "+&v"(o[2]), [o3] "+&v"(o[3]), [o4] "+&v"(o[4]), [o5] "+&v"(o[5])
        : [rs] "s"(rs), [ss] "s"(sstep), [end] "s"(end), [pace] "s"(s.pace), [va] "v"(s.va), [wm] "n"(WMAX - 6), [sh] "s"(s.sh), [w0] "s"(s.w0), [flip] "n"(FLIP ? 1 : 0)
        : "s90", "s91", "s92", "s93", "scc", "memory");
    s.issued = end;
    s.tnext = __builtin_amdgcn_readfirstlane(s.tnext);
}
__device__ __forceinline__ void ld_gate(Ld& s, int c, unsigned n) {
    unsigned vt, vg, cap = SPIN_LDS;
    const unsigned ga = L_CNT + 4 * c;
    asm volatile(
        "1:\n\t"
        "ds_read_b32 %[vg], %[ga]\n\t"
        LD_REPORT
        LD_WRITE
        "s_sub_u32 %[cap], %[cap], 1\n\t"
        "s_cbranch_scc1 2f\n\t"
        "s_waitcnt lgkmcnt(1)\n\t"
        "v_readfirstlane_b32 s92, %[vg]\n\t"
        "s_cmp_lt_u32 s92, %[n]\n\t"
        "s_cbranch_scc1 1b\n\t"
        "2:\n\t"
        "s_waitcnt lgkmcnt(0)"
        : [vt] "=&v"(vt), [vg] "=&v"(vg), [cap] "+&s"(cap)
        : [iss] "s"(s.issued), [va] "v"(s.va), [ga] "v"(ga), [n] "s"(n)
        : "s92", "s93", "scc", "memory");
}
__device__ __forceinline__ void ld_drain(Ld& s) {
    unsigned vt;
    asm volatile(
        "1:\n\t"
        LD_REPORT
        LD_WRITE
        "s_cmp_lg_u32 s93, 0\n\t"
        "s_cbranch_scc1 1b\n\t"
        : [vt] "=&v"(vt) : [iss] "s"(s.issued), [va] "v"(s.va) : "s92", "s93", "scc", "memory");
}

__global__ void __launch_bounds__(640) k_block(const u16* __restrict__ x, const u16* __restrict__ kc, const u16* __restrict__ vc,
                                               const u16* __restrict__ wq, const u16* __restrict__ wo, const u16* __restrict__ wg,
                                               const u16* __restrict__ wu, const u16* __restrict__ wd, u16* __restrict__ out,
                                               unsigned char* ws KNARG) {
    extern __shared__ __attribute__((aligned(16))) unsigned char lds_raw[];
    LAS unsigned char* L = (LAS unsigned char*)lds_raw;
    unsigned ka0, ka1;
    asm volatile("s_load_dword %0, %2, 0x0\n\ts_load_dword %1, %2, 0x40" : "=&s"(ka0), "=&s"(ka1) : "s"(__builtin_amdgcn_kernarg_segment_ptr()));
#define CODEPF 20400
    if (__builtin_expect((unsigned)__builtin_amdgcn_readfirstlane(threadIdx.x >> 6) == 8u, 1)) {
        const rsrc_t rc = mkrs((const void*)(__builtin_amdgcn_s_getpc() & ~255ull), CODEPF);
        asm volatile("s_mov_b32 m0, %0\n\ts_nop 0\n\tbuffer_load_dwordx4 %1, %2, %3 offen lds" :: "s"(L_JUNK), "v"((threadIdx.x & 63) * 16), "s"(rc), "s"((blockIdx.x >> 3) * 1024) : "memory");
    }
    if (threadIdx.x < 20) ((LAS unsigned*)(L + L_CNT))[threadIdx.x] = 0u;
    const int b = blockIdx.x, xcd = b & 7, idx = b >> 3;
    const int h = xcd * 4 + (idx >> 3), j = idx & 7;
    const int tid = threadIdx.x, lane = tid & 63, wave = __builtin_amdgcn_readfirstlane(tid >> 6);
    const int g = lane >> 4, tq = (lane & 15) >> 2, tp = lane & 3, i16 = lane & 15;
    const bool qcu = j >= 2;
    const int qr = (j + 3) >> 3, qc = j - 2 - 3 * qr;
    const int gi = (idx >> 3) * 2 + j;
    const int gcu = (!qcu && gi < 6) ? xcd * 6 + gi : -1;
    const bool pre = qcu || gcu >= 0;
    const unsigned e0 = g * 256 + (i16 ^ (g << 1)) * 16;
    unsigned q0 = qcu ? (lane >> 3) * 12288 + ((lane & 7) ^ (g & 1) * 2) * 16 : e0, q1 = qcu ? (q0 ^ 64) + 98304 - 1024 : e0;
    unsigned q2 = qcu ? q0 + 2 * (98304 - 1024) : e0 ^ 128, q3 = qcu ? q1 + 2 * (98304 - 1024) : e0 ^ 128;
    unsigned qs = qcu ? 393216 : 4096, qb = qcu ? (320 * qr * (H * DH) + h * DH + 64 * qc) * 2 : 32 * gcu * D * 2;
    unsigned qn = (wave == 4 && pre) ? 10 : 0;
    qs = __builtin_amdgcn_readfirstlane(qs); qb = __builtin_amdgcn_readfirstlane(qb); qn = __builtin_amdgcn_readfirstlane(qn);
    asm volatile("" : "+v"(q0), "+v"(q1), "+v"(q2), "+v"(q3));
    asm volatile("" : "+s"(qs), "+s"(qb), "+s"(qn));
    SB();
    asm volatile("s_waitcnt lgkmcnt(0)\n\ts_barrier" :: "s"(ka0), "s"(ka1), "s"(x), "s"(kc), "s"(vc), "s"(wq), "s"(wo), "s"(wg), "s"(wu), "s"(wd), "s"(out), "s"(ws) : "memory");
    const u64 t_start = __builtin_amdgcn_s_memrealtime();
    if (__builtin_expect(qn != 0, 1)) {
        const rsrc_t rq = mkrs(qcu ? wq : wd, qcu ? D * H * DH * 2 : F * D * 2);
#pragma unroll 1
        for (unsigned n = 0; n < 10; ++n, qb += qs, nap(KN(20)))
            asm volatile("s_mov_b32 m0, %0\n\ts_nop 0\n\tbuffer_load_dwordx4 %1, %5, %6 offen nt lds\n\tbuffer_load_dwordx4 %2, %5, %6 offen offset:1024 nt lds\n\t"
                         "buffer_load_dwordx4 %3, %5, %6 offen offset:2048 nt lds\n\tbuffer_load_dwordx4 %4, %5, %6 offen offset:3072 nt lds"
                         :: "s"(L_W + n * 4096), "v"(q0), "v"(q1), "v"(q2), "v"(q3), "s"(rq), "s"(qb) : "memory");
        waitvm<0>();
        lds_signal(C_WQ);
    }
    unsigned tag, xp[5];
    asm volatile("" : "=v"(xp[0]), "=v"(xp[1]), "=v"(xp[2]), "=v"(xp[3]), "=v"(xp[4]), "=v"(tag));
    if (__builtin_expect(wave == 9, 1)) {
        const unsigned xo = lane * 4, zo = 0;
        asm volatile("global_load_dword %0, %6, %7\n\tglobal_load_dword %1, %6, %7 offset:256\n\tglobal_load_dword %2, %6, %7 offset:512\n\t"
                     "global_load_dword %3, %6, %7 offset:768\n\tglobal_load_dword %4, %6, %7 offset:1024\n\tglobal_load_dword %5, %8, %9 sc1"
                     : "+v"(xp[0]), "+v"(xp[1]), "+v"(xp[2]), "+v"(xp[3]), "+v"(xp[4]), "+v"(tag) : "v"(xo), "s"(x), "v"(zo), "s"(ws) : "memory");
    }
    const rsrc_t rws = mkrs(ws, WS_END);
    if (__builtin_expect(wave == 7 && idx == 0, 1)) __builtin_amdgcn_raw_buffer_store_b64(v2u{(unsigned)t_start, (unsigned)(t_start >> 32)}, rws, lane == 0 ? WS_T0 + xcd * 128 : OOB, 0, 16);
    const bool wocu = j < 5;
    const int oi = (idx >> 3) * 3 + (j - 2), oc = (idx >> 3) * 3 + (j - 5);
    const int ocu = (wocu && qcu && oi < 5) ? xcd * 5 + oi : -1;
    const int n1 = __builtin_amdgcn_readfirstlane(!pre ? N1G + 4 : wocu ? N1G : 0);
    const int pv0 = P_K + 6 * n1;
    const f32x4 z4 = {0.f, 0.f, 0.f, 0.f};
    auto sel4 = [&](float a0, float a1, float a2, float a3) {
        const float lo = (lane & 16) ? a1 : a0, hi = (lane & 16) ? a3 : a2;
        return (lane & 32) ? hi : lo;
    };
    auto t_end = [&]() { return t_start - __builtin_amdgcn_readfirstlane(*(volatile LAS unsigned*)(L + L_CNT + 4 * C_LATE)) + TE; };
    unsigned rc;
    const int col = 4 * tp + g;
    auto make_h = [&](int a) {
        float xr = lo16(x[80 * xcd + 16 * a + col]);
        unsigned ha = L_O + (16 * a + col) * 2, ho = (tq == 0 && !(g & 1) && oc < 8) ? WS_HB + oc * 4096 + (40 * xcd + 8 * a + (col >> 1)) * 8 : OOB;
        PIN(ha); PIN(ho); PIN(xr);
        lds_wait<true>(L, C_OWN, 4);
        delay(D2);
        lds_wait<true>(L, C_PROG, P_WO);
        until_hot(t_end() + G2);
        float s = sum_words2(rws, lane * 16, WS_AP + (5 * xcd + a) * 2048, tag, rc);
        s += dpp<XOR8>(s); s += dpp<ROR4>(s);
        const unsigned hb = bfbits(xr + rbf(s));
        *(LAS u16*)(L + ha) = (u16)hb;
        lds_signal(C_O);
        auto pr = __builtin_amdgcn_permlane16_swap(hb, hb, false, false);
        put1(rws, ho, tag, pr[0] | (pr[1] << 16));
        NOTE(3 + a);
    };
    auto look_ap = [&](bool all) {
        lds_wait<true>(L, C_O, 3);
        const unsigned s0 = (unsigned)t_start, t2 = (unsigned)(t_end() - t_start) + G2;
        const bool fl = lane >= 32 && lane < 56;
        until(t_start + t2 + (all ? KN(19) : DOBS));
        unsigned tl = 0;
#pragma unroll 1
        for (int it = 0; it < 128; ++it) {
            MEMBAR();
            const v4u t = __builtin_amdgcn_raw_buffer_load_b128(rws, WS_TP + xcd * 8192 + ((fl && !all) ? 0 : lane * 128), 0, 16);
            const unsigned mx = max(max(t[0] - s0, t[1] - s0), max(t[2] - s0, t[3] - s0));
            if (__all(mx < 8192u)) {
                const unsigned c = (fl && all) ? (mx <= t2 + 8 ? 0u : mx + G3 + KN(18)) : mx + KN(16) <= t2 ? 0u : mx + D3;
                tl = __builtin_amdgcn_readfirstlane((unsigned)wave_max((float)c));
                break;
            }
            __builtin_amdgcn_s_sleep(2);
        }
        return t_start + max(tl, t2 + G3);
    };

    if (__builtin_expect(wave == 0, 1)) {
        __builtin_amdgcn_s_setprio(3);
        STAMP(14);
        unsigned ko[6], vo[6], po[6];
#pragma unroll
        for (int c = 0; c < 3; ++c) {
            const unsigned P = 64 * c + lane, r = P / 24, ch = P - r * 24;
            ko[c] = r * 384 + (ch ^ (r >> 1)) * 16 - c * 1024; ko[c + 3] = (ko[c] ^ 64) + 3072;
        }
        const size_t kvoff = ((size_t)h * SK + (SK - W) + j * 256) * DH;
        Ld s;
        s.issued = 0; s.va = L_CNT + 4 * C_PROG; s.w0 = KN(22); s.sh = KN(23);
        nap(KN(21));
        MEMBAR();
        const unsigned late = lateness(rws, lane, t_start);
        lds_set(C_LATE, late);
        s.pace = ((unsigned)(TE - (pre ? TAQ : TA0) - late) * (!pre ? 262144 / (35 + N1G) : wocu ? 262144 / (31 + N1G) : 262144 / 31)) >> 16;
        s.tnext = ((unsigned)t_start + (pre ? TAQ : TA0)) * 4;
        ld_stream<false>(s, mkrs(kc + kvoff, 98304), ko, 0, 6144, L_V, 16);
        STAMP(4);
#pragma unroll
        for (int c = 0; c < 3; ++c) {
            const unsigned P = 64 * c + lane, r = P / 24, ch = P - r * 24;
            vo[c] = r * 384 + (ch ^ (r & 2u)) * 16 - c * 1024; vo[c + 3] = (vo[c] ^ 64) + 3072;
            const unsigned wb = (i16 ^ (g << 1)) * 16;
            const unsigned rst = wocu ? 1280 : 3072;
            po[c] = (4 * c + g) * rst + (wb ^ ((c >> 1) << 7)) - c * 1024;
            po[c + 3] = (4 * c + 12 + g) * rst + (wb ^ ((((c + 3) >> 1) & 1) << 7)) - c * 1024;
        }
        const rsrc_t rv = mkrs(vc + kvoff, 98304);
        const rsrc_t ro = mkrs(wo + (size_t)(h * DH) * D + j * 128, 191 * 1280 + 256);
        ld_gate(s, C_QP, __builtin_amdgcn_readfirstlane(qcu ? 4 : pre ? 8 : 0));
        if (n1) ld_stream<true>(s, ro, po, 0, 30720, L_W, n1);
        ld_gate(s, C_KA, 4);
        ld_stream<false>(s, rv, vo, 0, 6144, L_V, 8);
        ld_gate(s, C_KB, 4);
        ld_stream<false>(s, rv, vo, 49152, 6144, L_V + 49152, 8);
        STAMP(6);
        s.pace = 0; s.w0 = WMAX - 6;
        if (wocu) {
            if (n1 < 8) ld_stream<true>(s, ro, po, n1 * 30720, 30720, L_W + n1 * 6144, 8 - n1);
        } else {
            const rsrc_t r0 = mkrs(wg + (size_t)(80 * xcd) * F + 128 * oc, 79 * 3072 + 256), r1 = mkrs(wu + (size_t)(80 * xcd) * F + 128 * oc, 79 * 3072 + 256);
            s.tnext += FDELAY * 4;
            ld_stream<true>(s, r0, po, 0, 73728, L_W, 4);
            ld_stream<true>(s, r1, po, 0, 73728, L_W + 24576, 4);
        }
        ld_drain(s);
        if (!wocu) putw(rws, lane < 8 ? WS_TP + lane * 8192 + (32 + xcd * 3 + (oc >> 2)) * 128 + (oc & 3) * 4 : OOB, (unsigned)__builtin_amdgcn_s_memrealtime());
        if (!wocu) STAMP(17); else STAMP(9);
        lds_wait<true>(L, C_X, 1);
        tag = __builtin_amdgcn_readfirstlane(*(LAS unsigned*)(L + L_TAG));
        if (gcu >= 0) {
            unsigned ha = L_HH + (4 * i16 + g) * 2, ca = L_LN + lane * 4;
            PIN(ha); PIN(ca);
            const u64 t3 = look_ap(false);
            lds_set(C_GO, (unsigned)(t3 - t_start));
            STAMP(18);
            until_hot(t3);
            float sv = sum_words2(rws, lane * 16, WS_GP + gcu * 2048, tag, rc);
            STAMP(12);
            f32x4 st;
            float cs;
            unsigned cap = SPIN_LDS;
#pragma unroll 1
            do {
                st = *(volatile LAS f32x4*)(L + L_ST);
                cs = *(volatile LAS float*)(L + ca);
            } while (__builtin_amdgcn_readfirstlane(__float_as_uint(st[2]) & __float_as_uint(st[3])) == 0u && --cap != 0);
            const float v = rbf((sv - st[0] * cs) * st[1]);
            const float sg = rbf(__builtin_amdgcn_rcpf(1.0f + __expf(-v)));
            *(LAS u16*)(L + ha) = (u16)bfbits(sg * dpp<XOR8>(v));
            lds_signal(C_HH);
            NOTE(8);
        } else if (ocu >= 0) {
            const rsrc_t rout = mkrs(out, 1280);
            unsigned oo = tq == 0 ? (16 * ocu + col) * 2 : OOB;
            PIN(oo);
            const u64 t3 = look_ap(true), t4 = t3 + D4;
            until(t3);
            v2u hres[1] = {v2u{0u, ~tag}};
            gather<4>(rws, (col >> 1) * 8, WS_HB + xcd * 4096 + 64 * ocu, 0, tag, hres);
            float hr = (g & 1) ? hi16(hres[0][0]) : lo16(hres[0][0]);
            PIN(hr);
            until_hot(t4);
            STAMP(22);
            float s3 = sum_words3(rws, lane * 16, WS_DP + ocu * 3072, tag, rc);
            s3 += dpp<XOR8>(s3); s3 += dpp<ROR4>(s3);
            __builtin_amdgcn_raw_buffer_store_b16((u16)bfbits(hr + rbf(s3)), rout, oo, 0, 0);
            if (ocu == 0 && lane == 0) __hip_atomic_store((gu32*)(ws + WS_EPOCH), tag, RLX_AGENT);
            STAMP(16);
            NOTE(9);
        }
    } else if (wave == 9) {
        asm volatile("s_waitcnt vmcnt(0)" : "+v"(xp[0]), "+v"(xp[1]), "+v"(xp[2]), "+v"(xp[3]), "+v"(xp[4]), "+v"(tag) :: "memory");
        float mb, rden;
        ln_stats(xp, mb, rden);
#pragma unroll
        for (int i = 0; i < 5; ++i) *(LAS unsigned*)(L + L_LN + (lane + 64 * i) * 4) = ln_pair(xp[i], mb, rden);
        tag = __builtin_amdgcn_readfirstlane(tag) + 0x01010101u;
        if (lane == 0) *(LAS unsigned*)(L + L_TAG) = tag;
        lds_signal(C_X);
        STAMP(23);
        {
            until(t_start + TQ);
            v4u qp[3];
#pragma unroll
            for (int k = 0; k < 3; ++k) qp[k] = v4u{0u, ~tag, 0u, ~tag};
            gather<8>(rws, lane * 16, WS_QP + h * 3072, 1024, tag, qp);
#pragma unroll
            for (int k = 0; k < 3; ++k) *(LAS unsigned*)(L + L_Q + k * 128 + (lane & 31) * 4) = pk2(sw32_sum(fv(qp[k][0])), sw32_sum(fv(qp[k][2])));
            lds_signal(C_Q);
            STAMP(3);
        }
        lds_wait<true>(L, C_P, 4);
        const f32x4 mx = *(LAS f32x4*)(L + L_MX), ls = *(LAS f32x4*)(L + L_MX + 16);
        const float m = fmaxf(fmaxf(mx[0], mx[1]), fmaxf(mx[2], mx[3]));
        const float l = (ls[0] + ls[1]) + (ls[2] + ls[3]);
        put2(rws, lane == 0 ? WS_ST + (h * 8 + j) * 128 : OOB, tag, m, l);
        if (wocu) {
            until(t_end() + TS);
            const unsigned tt = lane < 8 ? ~tag : tag;
            v4u st[1] = {v4u{0u, tt, 0u, tt}};
            gather<8>(rws, lane * 128, WS_ST + h * 1024, 0, tag, st);
            const float ms = fv(st[0][0]);
            float M = fmaxf(ms, dpp<XOR1>(ms)); M = fmaxf(M, dpp<XOR2>(M)); M = fmaxf(M, dpp<XOR7>(M));
            const float e = __expf(ms - M);
            float Ls = e * fv(st[0][2]);
            Ls += dpp<XOR1>(Ls); Ls += dpp<XOR2>(Ls); Ls += dpp<XOR7>(Ls);
            if (lane < 8) *(LAS float*)(L + L_WGT + lane * 4) = e * __builtin_amdgcn_rcpf(Ls);
            lds_signal(C_W);
            STAMP(19);
            if (gcu >= 0) {
                lds_wait<true>(L, C_O, 3);
                const u64 th = t_end() + G2 + G3 - KN(17);
                until(th - 12);
                until(t_start + __builtin_amdgcn_readfirstlane(*(volatile LAS unsigned*)(L + L_CNT + 4 * C_GO)) - KN(17) - 12);
                until_hot(th);
                v2u hv[5];
#pragma unroll
                for (int i = 0; i < 5; ++i) hv[i] = v2u{0u, ~tag};
                rc = gather<2>(rws, lane * 8, WS_HB + xcd * 4096, 512, tag, hv);
#pragma unroll
                for (int i = 0; i < 5; ++i) xp[i] = hv[i][0];
                ln_stats_fast(xp, mb, rden);
                if (lane == 0) { *(LAS v2u*)(L + L_ST) = v2u{__float_as_uint(mb), __float_as_uint(rden)}; MEMBAR(); *(LAS float*)(L + L_ST + 8) = 1.0f; }
                STAMP(15);
                NOTE(11);
            }
        } else {
            make_h(4);
        }
    } else {
        bf16x8 dw[5];
        if (gcu >= 0) {
            lds_wait<true>(L, C_WQ, 1);
#pragma unroll
            for (int t = 0; t < 5; ++t) {
                const int tt = 5 * (wave - 1) + t;
                unsigned ad[2];
#pragma unroll
                for (int u = 0; u < 2; ++u) {
                    const int r = 8 * g + 4 * u + tq, B = 5 * r + (tt >> 3);
                    ad[u] = L_W + r * 1280 + (tt ^ ((B & 3) | ((B >> 1) & 4))) * 32 + tp * 8;
                }
                dw[t] = cat8(trd(L, ad[0]), trd(L, ad[1]));
            }
            lds_signal(C_QP);
        }
        if (wave < 5) {
            const int a = wave - 1;
            lds_wait<true>(L, C_X, 1);
            tag = __builtin_amdgcn_readfirstlane(*(LAS unsigned*)(L + L_TAG));
            if (qcu) {
                bf16x8 xa[10];
#pragma unroll
                for (int s = 0; s < 10; ++s) xa[s] = *(LAS bf16x8*)(L + L_LN + (320 * qr + 32 * s + 8 * g) * 2);
                lds_wait<true>(L, C_WQ, 1);
                if (a == 0) STAMP(1);
                const unsigned ca = L_W + (8 * g + tq) * 128 + ((a ^ ((tq >> 1) | ((g & 1) << 1))) * 2 + (tp >> 1)) * 16 + (tp & 1) * 8;
                f32x4 q0 = z4, q1 = z4;
#pragma unroll
                for (int s = 0; s < 10; s += 2) {
                    q0 = mfma32(xa[s], cat8(trd(L, ca + s * 4096), trd(L, ca + s * 4096 + 512)), q0);
                    q1 = mfma32(xa[s + 1], cat8(trd(L, ca + s * 4096 + 4096), trd(L, ca + s * 4096 + 4608)), q1);
                }
                lds_signal(C_QP);
                put1(rws, lane < 16 ? WS_QP + h * 3072 + qc * 1024 + qr * 512 + (16 * a + lane) * 8 : OOB, tag, __float_as_uint(q0[0] + q1[0]));
                if (a == 0) STAMP(2);
            }
            if (wocu) {
                if (a < 3) {
                    unsigned oa = L_O + (64 * a + 4 * i16 + g) * 2, wa = L_OWN + (64 * a + 4 * i16) * 4;
                    PIN(oa); PIN(wa);
                    const u64 mo = ~(0xFFFFull << (16 * (j & 3)));
                    lds_wait<true>(L, C_W, 1);
                    float w0 = *(LAS float*)(L + L_WGT + 4 * g), w1 = *(LAS float*)(L + L_WGT + 16 + 4 * g);
                    PIN(w0); PIN(w1);
                    lds_wait<true>(L, C_PROG, pv0 + 84);
                    const u64 t1 = t_end() + G1;
                    bf16x8 own[1];
                    spin_rd(C_OWN, 4, wa, own);
                    nap(D1);
                    until_hot(t1);
                    const float o = sum_words_w(rws, g * 768 + i16 * 16, WS_OP + h * 6144 + a * 256, tag, rc, __builtin_bit_cast(v4u, own[0]), j < 4 ? mo : ~0ull, j < 4 ? ~0ull : mo, w0, w1, 0);
                    *(LAS u16*)(L + oa) = (u16)bfbits(o);
                    lds_signal(C_O);
                    if (a == 0) STAMP(8);
                    rc += 1;
                    NOTE(a);
                } else {
                    unsigned to = lane < 8 ? WS_TP + lane * 8192 + (j < 4 ? h * 128 + j * 4 : (56 + (h >> 2)) * 128 + (h & 3) * 4) : OOB;
                    PIN(to);
                    lds_wait<true>(L, C_PROG, P_WO);
                    until(t_end() + G1);
                    lds_wait<false>(L, C_O, 3);
                    putw(rws, to, (unsigned)__builtin_amdgcn_s_memrealtime());
                    if (gcu >= 0) {
                        until(t_end() + G2 + 45);
                        *(LAS float*)(L + L_LN + lane * 4) = sum_words2(rws, lane * 16, WS_CP + gcu * 2048, tag, rc);
                        MEMBAR();
                        if (lane == 0) *(LAS float*)(L + L_ST + 12) = 1.0f;
                        NOTE(10);
                    }
                }
            } else {
                make_h(a);
                if (a == 0) STAMP(11);
            }
        } else {
            const int kw = wave - 5;
            bf16x8 kf[4][6];
            {
                const unsigned kb = L_V + (16 * kw + i16) * 384;
#pragma unroll
                for (int n = 0; n < 4; ++n) {
                    lds_wait<true>(L, C_PROG, 12 * ((kw >> 1) + 2 * n + 1));
#pragma unroll
                    for (int s = 0; s < 6; ++s) kf[n][s] = *(LAS bf16x8*)(L + kb + n * 24576 + ((4 * s + g) ^ (i16 >> 1)) * 16);
                    if (n & 1) lds_signal(n == 1 ? C_KA : C_KB);
                }
            }
            lds_wait<true>(L, C_Q, 1);
            tag = __builtin_amdgcn_readfirstlane(*(LAS unsigned*)(L + L_TAG));
            f32x4 sa[4] = {z4, z4, z4, z4};
#pragma unroll
            for (int s = 0; s < 6; ++s) {
                const bf16x8 qf = *(LAS bf16x8*)(L + L_Q + (32 * s + 8 * g) * 2);
#pragma unroll
                for (int n = 0; n < 4; ++n) sa[n] = mfma32(qf, kf[n][s], sa[n]);
            }
            const float raw = sel4(sa[0][0], sa[1][0], sa[2][0], sa[3][0]);
            const float s = rbf(rbf(raw) * 0.07216878364870322f);
            const float mw = wave_max(s);
            if (lane == 0) *(LAS float*)(L + L_MX + kw * 4) = mw;
            lds_signal(C_MAX);
            lds_wait<false>(L, C_MAX, 4);
            const f32x4 mx = *(LAS f32x4*)(L + L_MX);
            const float m = fmaxf(fmaxf(mx[0], mx[1]), fmaxf(mx[2], mx[3]));
            const float e = __expf(s - m);
            *(LAS u16*)(L + L_P + (16 * (kw + 4 * g) + i16) * 2) = (u16)bfbits(e);
            const float lw = wave_sum(e);
            if (lane == 0) *(LAS float*)(L + L_MX + 16 + kw * 4) = lw;
            lds_signal(C_P);
            lds_wait<false>(L, C_P, 4);
            if (kw == 0) STAMP(5);
            bf16x8 pf[8];
#pragma unroll
            for (int s8 = 0; s8 < 8; ++s8) pf[s8] = *(LAS bf16x8*)(L + L_P + (32 * s8 + 8 * g) * 2);
            const int fvs = (tq >> 1) | ((g & 1) << 1);
            unsigned vb[3], vl[3];
#pragma unroll
            for (int t = 0; t < 3; ++t) { vb[t] = (8 * g + tq) * 384 + (((3 * kw + t) ^ fvs) * 2 + (tp >> 1)) * 16 + (tp & 1) * 8; vl[t] = L_V + 6 * 12288 + vb[t]; PIN(vl[t]); }
            unsigned oo = lane < 48 ? WS_OP + ((h * 8 + j) * 192 + 48 * kw + lane) * 4 : OOB, ow = L_OWN + (lane < 48 ? 48 * kw + lane : 192 + lane) * 4;
            unsigned tb = (tag >> (8 * tp)) & 255u;
            PIN(tb);
            PIN(oo); PIN(ow);
            f32x4 oa[3] = {z4, z4, z4};
            lds_wait<true>(L, C_PROG, pv0 + 72);
#pragma unroll
            for (int s8 = 0; s8 < 6; ++s8)
#pragma unroll
                for (int t = 0; t < 3; ++t) oa[t] = mfma32(pf[s8], cat8(trd(L, L_V + vb[t] + s8 * 12288), trd(L, L_V + vb[t] + s8 * 12288 + 1536)), oa[t]);
            {
                s16x4 vf[6];
                spin_tr<0>(C_PROG, pv0 + 84, vl, vf);
#pragma unroll
                for (int t = 0; t < 3; ++t) oa[t] = mfma32(pf[6], cat8(vf[2 * t], vf[2 * t + 1]), oa[t]);
                spin_tr<12288>(C_PROG, pv0 + 96, vl, vf);
                if (kw == 0) STAMP(20);
#pragma unroll
                for (int t = 0; t < 3; ++t) oa[t] = mfma32(pf[7], cat8(vf[2 * t], vf[2 * t + 1]), oa[t]);
            }
            const unsigned op = enc(sel4(oa[0][0], oa[1][0], oa[2][0], oa[2][0]), tb);
            putw(rws, oo, op);
            *(LAS unsigned*)(L + ow) = op;
            lds_signal(C_OWN);
            if (kw == 0) STAMP(7);
            if (wocu) {
                unsigned ao = lane < 32 ? WS_AP + (((8 * j + 2 * kw + g) * 32 + h) * 16 + i16) * 4 : OOB, al = L_O + 16 * g;
                PIN(ao); PIN(al);
                lds_wait<false>(L, C_PROG, P_WO);
                bf16x8 bw[2][6], af[6];
                const int f3 = tq | ((g & 1) << 2);
#pragma unroll
                for (int t = 0; t < 2; ++t) {
                    const unsigned cb = L_W + (8 * g + tq) * 256 + (((2 * kw + t) ^ f3) * 2 + (tp >> 1)) * 16 + (tp & 1) * 8;
#pragma unroll
                    for (int s6 = 0; s6 < 6; ++s6) bw[t][s6] = cat8(trd(L, cb + s6 * 8192), trd(L, cb + s6 * 8192 + 1024));
                }
                spin_rd(C_O, 3, al, af);
                f32x4 a0 = z4, a1 = z4;
#pragma unroll
                for (int s6 = 0; s6 < 6; ++s6) { a0 = mfma32(af[s6], bw[0][s6], a0); a1 = mfma32(af[s6], bw[1][s6], a1); }
                putw(rws, ao, enc((lane & 16) ? a1[0] : a0[0], tb));
                if (kw == 0) STAMP(10);
            } else {
                lds_wait<true>(L, C_PROG, P_WO);
                bf16x8 bw[2][2][3], af[3];
                const int f3 = tq | ((g & 1) << 2);
#pragma unroll
                for (int mt = 0; mt < 2; ++mt)
#pragma unroll
                    for (int t = 0; t < 2; ++t) {
                        const unsigned cb = L_W + mt * 24576 + (8 * g + tq) * 256 + ((2 * kw + t) ^ f3) * 32 + tp * 8;
#pragma unroll
                        for (int s3 = 0; s3 < 3; ++s3) bw[mt][t][s3] = cat8(trd(L, cb + s3 * 8192), trd(L, cb + s3 * 8192 + 1024));
                    }
                unsigned go = WS_CP + ((4 * oc + kw) * 8 + xcd) * 256 + lane * 4, g2 = go + (WS_GP - WS_CP), al = L_O + 16 * g;
                PIN(al); PIN(g2);
                auto tile4 = [&](unsigned off) {
                    f32x4 d[2][2] = {{z4, z4}, {z4, z4}};
#pragma unroll
                    for (int s3 = 0; s3 < 3; ++s3)
#pragma unroll
                        for (int mt = 0; mt < 2; ++mt)
#pragma unroll
                            for (int t = 0; t < 2; ++t) d[mt][t] = mfma32(af[s3], bw[mt][t][s3], d[mt][t]);
                    SB();
                    putw(rws, off, enc(sel4(d[0][0][0], d[0][1][0], d[1][0][0], d[1][1][0]), tb));
                };
                const v4u ones = {0x3F803F80u, 0x3F803F80u, 0x3F803F80u, 0x3F803F80u};
                af[0] = af[1] = af[2] = asb(ones);
                tile4(go);
                *(LAS unsigned*)(L + L_O + 160 + (lane & 7) * 4) = 0u;
                spin_rd(C_O, 5, al, af);
                tile4(g2);
                if (kw == 0) STAMP(21);
            }
        }
        if (gcu >= 0) {
            const int w8 = wave - 1;
            unsigned d0 = WS_DP + (((5 * w8 + g) * 48 + gcu) * 16 + i16) * 4, d1 = lane < 16 ? WS_DP + (((5 * w8 + 4) * 48 + gcu) * 16 + i16) * 4 : OOB, al = L_HH + 16 * g;
            unsigned tb = (tag >> (8 * tp)) & 255u;
            PIN(tb);
            PIN(d0); PIN(d1); PIN(al);
            bf16x8 af[1];
            lds_wait<true>(L, C_GO, 1);
            spin_rd(C_HH, 1, al, af);
            f32x4 d[5];
#pragma unroll
            for (int t = 0; t < 5; ++t) d[t] = mfma32(af[0], dw[t], z4);
            SB();
            putw(rws, d0, enc(sel4(d[0][0], d[1][0], d[2][0], d[3][0]), tb));
            putw(rws, d1, enc(d[4][0], tb));
            if (w8 == 0) STAMP(13);
        }
    }
}

extern "C" void kernel_launch(void* const* d_in, const int* in_sizes, int n_in, void* d_out, int out_size, void* d_ws, size_t ws_size, hipStream_t stream) {
    (void)hipFuncSetAttribute((const void*)k_block, hipFuncAttributeMaxDynamicSharedMemorySize, L_END);
    k_block<<<256, 640, L_END, stream>>>((const u16*)d_in[0], (const u16*)d_in[1], (const u16*)d_in[2], (const u16*)d_in[3], (const u16*)d_in[4],
                                         (const u16*)d_in[6], (const u16*)d_in[7], (const u16*)d_in[8], (u16*)d_out, (unsigned char*)d_ws
        );
}
```

```cpp
#include <hip/hip_runtime.h>
#include <stdint.h>

typedef unsigned short u16;
typedef unsigned long long u64;
typedef unsigned v4u __attribute__((ext_vector_type(4)));
typedef unsigned v2u __attribute__((ext_vector_type(2)));
typedef float f32x4 __attribute__((ext_vector_type(4)));
typedef short s16x4 __attribute__((ext_vector_type(4)));
typedef __bf16 bf16x8 __attribute__((ext_vector_type(8)));
typedef __bf16 bf16x2 __attribute__((ext_vector_type(2)));
#define GAS __attribute__((address_space(1)))
#define LAS __attribute__((address_space(3)))
typedef GAS u64 gu64;
typedef GAS unsigned gu32;
typedef __amdgpu_buffer_rsrc_t rsrc_t;
#define RLX_AGENT __ATOMIC_RELAXED, __HIP_MEMORY_SCOPE_AGENT

constexpr int D = 640, H = 32, SK = 8192, DH = 192, W = 2048, F = 1536;

constexpr unsigned WS_EPOCH = 0;
constexpr unsigned WS_T0 = 1024;
constexpr unsigned WS_LK = 2048;
constexpr unsigned WS_QP = 4096;
constexpr unsigned WS_ST = WS_QP + 32 * 3072;
constexpr unsigned WS_HB = WS_ST + 32 * 8 * 128;
constexpr unsigned WS_OP = WS_HB + 8 * 4096;
constexpr unsigned WS_AP = WS_OP + 32 * 6144;
constexpr unsigned WS_CP = WS_AP + 40 * 2048;
constexpr unsigned WS_GP = WS_CP + 48 * 2048;
constexpr unsigned WS_DP = WS_GP + 48 * 2048;
constexpr unsigned WS_TP = WS_DP + 40 * 3072;
constexpr unsigned WS_END = WS_TP + 8 * 256;
constexpr unsigned PEND = 0x40000000u;
constexpr unsigned OOB = 0x7FFFFF00u;

constexpr unsigned L_W = 0;
constexpr unsigned L_V = 49152;
constexpr unsigned L_Q = L_V + 98304;
constexpr unsigned L_P = L_Q + 384;
constexpr unsigned L_O = L_P + 512;
constexpr unsigned L_LN = L_O + 512;
constexpr unsigned L_OWN = L_LN + 1280;
constexpr unsigned L_HH = L_OWN + 1024;
constexpr unsigned L_MX = L_HH + 128;
constexpr unsigned L_WGT = L_MX + 32;
constexpr unsigned L_CNT = L_WGT + 32;
constexpr unsigned L_ST = L_CNT + 96;
constexpr unsigned L_TAG = L_ST + 16;
constexpr unsigned L_STAMP = L_TAG + 16;
constexpr unsigned L_JUNK = L_STAMP + 256;
constexpr unsigned L_END = L_JUNK + 1280;
static_assert(L_END <= 163840, "LDS");
enum { C_X = 0, C_PROG, C_KA, C_KB, C_Q, C_MAX, C_P, C_W, C_OWN, C_O, C_HH, C_WQ, C_QP, C_GO, C_LATE, C_LK, C_EXT, C_LV, C_EX2 };
constexpr int P_K = 96, P_WO = 240;

constexpr int N1G = 2;
constexpr int TAQ = 100;
constexpr int TE = 824;
constexpr int FDELAY = 38;
constexpr int TQ = 430;
constexpr int TS = -80;
constexpr int TAPER = 10;
constexpr int D2 = 113;
constexpr int D3 = 165;
constexpr int D4 = 93;
constexpr int G1 = 188;
constexpr int DOBS = 8;
constexpr int G2 = 289;
constexpr int G3 = 88;
constexpr int LATE_AP = 21;
constexpr int HB_LEAD = 16;
constexpr int LATE_F1_ADD = 10;
constexpr int DOBS_ALL = 110;
constexpr int LATE_F1 = 50;
constexpr int TLK = 503;
constexpr int TLV = 690;
constexpr int ENDPAD = 8;
constexpr int LV1 = 31;
constexpr int LVS = 6;
constexpr int LK1 = 30;
constexpr int LKS = 6;
constexpr int WIN0 = 16;
constexpr int WINSH = 4;
constexpr int WMAX = 36;
constexpr unsigned SPIN_LDS = 1u << 23;
constexpr unsigned SPIN_GLB = 1u << 20;

__device__ __forceinline__ unsigned pk2(float lo, float hi) {
    const bf16x2 r = {(__bf16)lo, (__bf16)hi};
    return __builtin_bit_cast(unsigned, r);
}
__device__ __forceinline__ unsigned bfbits(float f) { return (unsigned)__builtin_bit_cast(u16, (__bf16)f); }
__device__ __forceinline__ float rbf(float f) { return __uint_as_float(bfbits(f) << 16); }
__device__ __forceinline__ float lo16(unsigned w) { return __uint_as_float(w << 16); }
__device__ __forceinline__ float hi16(unsigned w) { return __uint_as_float(w & 0xFFFF0000u); }
__device__ __forceinline__ float dot2(unsigned a, unsigned b, float c) {
    return __builtin_amdgcn_fdot2_f32_bf16(__builtin_bit_cast(bf16x2, a), __builtin_bit_cast(bf16x2, b), c, false);
}
template <int CTRL> __device__ __forceinline__ float dpp(float x) {
    return __builtin_bit_cast(float, __builtin_amdgcn_mov_dpp(__builtin_bit_cast(int, x), CTRL, 0xf, 0xf, true));
}
constexpr int XOR1 = 0xB1, XOR2 = 0x4E, XOR7 = 0x141, XOR8 = 0x128, ROR4 = 0x124;
__device__ __forceinline__ float sw16_sum(float x) {
    auto s = __builtin_amdgcn_permlane16_swap(__float_as_uint(x), __float_as_uint(x), false, false);
    return __uint_as_float(s[0]) + __uint_as_float(s[1]);
}
__device__ __forceinline__ float sw32_sum(float x) {
    auto s = __builtin_amdgcn_permlane32_swap(__float_as_uint(x), __float_as_uint(x), false, false);
    return __uint_as_float(s[0]) + __uint_as_float(s[1]);
}
__device__ __forceinline__ float sw16_max(float x) {
    auto s = __builtin_amdgcn_permlane16_swap(__float_as_uint(x), __float_as_uint(x), false, false);
    return fmaxf(__uint_as_float(s[0]), __uint_as_float(s[1]));
}
__device__ __forceinline__ float sw32_max(float x) {
    auto s = __builtin_amdgcn_permlane32_swap(__float_as_uint(x), __float_as_uint(x), false, false);
    return fmaxf(__uint_as_float(s[0]), __uint_as_float(s[1]));
}
__device__ __forceinline__ float wave_sum(float x) {
    x += dpp<XOR1>(x); x += dpp<XOR2>(x); x += dpp<XOR7>(x); x += dpp<XOR8>(x);
    return sw32_sum(sw16_sum(x));
}
__device__ __forceinline__ float wave_max(float x) {
    x = fmaxf(x, dpp<XOR1>(x)); x = fmaxf(x, dpp<XOR2>(x)); x = fmaxf(x, dpp<XOR7>(x)); x = fmaxf(x, dpp<XOR8>(x));
    return sw32_max(sw16_max(x));
}
__device__ __forceinline__ rsrc_t mkrs(const void* p, unsigned bytes) { return __builtin_amdgcn_make_buffer_rsrc((void*)p, 0, bytes, 0x00020000); }

template <int N> __device__ __forceinline__ void waitvm() { asm volatile("s_waitcnt vmcnt(%0)" :: "n"(N) : "memory"); }
#define LGKM_DRAIN() asm volatile("s_waitcnt lgkmcnt(0)" ::: "memory")
#define MEMBAR() asm volatile("" ::: "memory")
#define BAR() do { __builtin_amdgcn_sched_barrier(0); asm volatile("s_barrier" ::: "memory"); __builtin_amdgcn_sched_barrier(0); } while (0)

__device__ __forceinline__ void lds_signal(int c) {
    const unsigned addr = L_CNT + 4 * c, one = 1u;
    asm volatile("s_mov_b64 exec, 1\n\tds_add_u32 %0, %1\n\ts_mov_b64 exec, -1" :: "v"(addr), "v"(one) : "memory");
}
__device__ __forceinline__ void lds_set(int c, unsigned val) {
    const unsigned addr = L_CNT + 4 * c;
    asm volatile("s_mov_b64 exec, 1\n\tds_write_b32 %0, %1\n\ts_mov_b64 exec, -1" :: "v"(addr), "v"(val) : "memory");
}
template <bool SLEEP> __device__ __forceinline__ void lds_wait(LAS unsigned char* L, int c, unsigned n) {
    volatile LAS unsigned* p = (volatile LAS unsigned*)(L + L_CNT) + c;
    unsigned cap = SPIN_LDS;
#pragma unroll 1
    do {
        if ((unsigned)__builtin_amdgcn_readfirstlane(*p) >= n) break;
        if (SLEEP) __builtin_amdgcn_s_sleep(1);
        MEMBAR();
    } while (--cap != 0);
    MEMBAR();
}
__device__ __forceinline__ void until(u64 t) { while (__builtin_amdgcn_s_memrealtime() < t) __builtin_amdgcn_s_sleep(1); }
__device__ __forceinline__ void until_hot(u64 t) { until(t - 12); while (__builtin_amdgcn_s_memrealtime() < t) { } }
__device__ __forceinline__ void delay(int ticks) { until(__builtin_amdgcn_s_memrealtime() + ticks); }
__device__ __forceinline__ void nap(int n) { n = __builtin_amdgcn_readfirstlane(n); asm volatile("1:\n\ts_sleep 1\n\ts_sub_u32 %0, %0, 1\n\ts_cmp_gt_i32 %0, 0\n\ts_cbranch_scc1 1b" : "+s"(n) :: "scc"); }
#define PIN(x) asm volatile("" : "+v"(x))
#define SB() __builtin_amdgcn_sched_barrier(0)
__device__ __forceinline__ float fv(unsigned u) { return __uint_as_float(u); }
__device__ __forceinline__ void put1(rsrc_t rs, unsigned off, unsigned tag, unsigned val) { __builtin_amdgcn_raw_buffer_store_b64(v2u{val, tag}, rs, off, 0, 16); }
__device__ __forceinline__ void put2(rsrc_t rs, unsigned off, unsigned tag, float a, float b) {
    __builtin_amdgcn_raw_buffer_store_b128(v4u{__float_as_uint(a), tag, __float_as_uint(b), tag}, rs, off, 0, 16);
}
__device__ __forceinline__ u64 badmask(v2u x, unsigned tag) {
    u64 m;
    asm volatile("v_cmp_ne_u32_e64 %0, %1, %2" : "=s"(m) : "s"(tag), "v"(x[1]));
    return m;
}
__device__ __forceinline__ u64 badmask(v4u x, unsigned tag) {
    u64 m;
    const unsigned t = __builtin_amdgcn_bitop3_b32(x[1], x[3], tag, 0x7E);
    asm volatile("v_cmp_ne_u32_e64 %0, 0, %1" : "=s"(m) : "v"(t));
    return m;
}
__device__ __forceinline__ void gload(v2u& x, u64 m, rsrc_t rs, unsigned voff, unsigned soff) {
    asm volatile("s_mov_b64 exec, %2\n\tbuffer_load_dwordx2 %0, %1, %3, %4 offen sc1\n\ts_mov_b64 exec, -1" : "+v"(x) : "v"(voff), "s"(m), "s"(rs), "s"(soff) : "memory");
}
__device__ __forceinline__ void gload(v4u& x, u64 m, rsrc_t rs, unsigned voff, unsigned soff) {
    asm volatile("s_mov_b64 exec, %2\n\tbuffer_load_dwordx4 %0, %1, %3, %4 offen sc1\n\ts_mov_b64 exec, -1" : "+v"(x) : "v"(voff), "s"(m), "s"(rs), "s"(soff) : "memory");
}
template <int SLP, int N, class T> __device__ __forceinline__ unsigned gather(rsrc_t rs, unsigned voff, unsigned base, unsigned stride, unsigned tag, T (&x)[N]) {
    u64 m[N];
#pragma unroll
    for (int k = 0; k < N; ++k) { PIN(x[k]); m[k] = badmask(x[k], tag); }
    unsigned cap = SPIN_GLB;
#pragma unroll 1
    do {
#pragma unroll
        for (int k = 0; k < N; ++k) gload(x[k], m[k], rs, voff, base + k * stride);
        waitvm<0>();
#pragma unroll
        for (int k = 0; k < N; ++k) PIN(x[k]);
        u64 any = 0;
#pragma unroll
        for (int k = 0; k < N; ++k) { m[k] = badmask(x[k], tag); any |= m[k]; }
        if (!any) break;
        if (SLP) __builtin_amdgcn_s_sleep(SLP);
        MEMBAR();
    } while (--cap != 0);
    return cap;
}
__device__ __forceinline__ unsigned enc(float v, unsigned tb) { return (__float_as_uint(v) & 0xFFFFFF00u) | tb; }
__device__ __forceinline__ void putw(rsrc_t rs, unsigned off, unsigned w) { __builtin_amdgcn_raw_buffer_store_b32(w, rs, off, 0, 16); }
__device__ __forceinline__ float fold4(float a0, float a1, float a2, float a3) {
    auto p = __builtin_amdgcn_permlane32_swap(__float_as_uint(a0), __float_as_uint(a2), false, false);
    auto q = __builtin_amdgcn_permlane32_swap(__float_as_uint(a1), __float_as_uint(a3), false, false);
    auto r = __builtin_amdgcn_permlane16_swap(__float_as_uint(fv(p[0]) + fv(p[1])), __float_as_uint(fv(q[0]) + fv(q[1])), false, false);
    return fv(r[0]) + fv(r[1]);
}
__device__ __forceinline__ float sum_words2(rsrc_t rs, unsigned voff, unsigned base, unsigned tag, unsigned& cap) {
    cap = SPIN_GLB;
    float a0, a1, a2, a3;
    asm volatile(
        "buffer_load_dwordx4 v[144:147], %[vo], %[rs], %[b] offen offset:0 sc1\n\t"
        "buffer_load_dwordx4 v[148:151], %[vo], %[rs], %[b] offen offset:1024 sc1\n\t"
        "s_branch 2f\n\t"
        "1:\n\t"
        "s_mov_b64 exec, s[78:79]\n\t"
        "buffer_load_dwordx4 v[144:147], %[vo], %[rs], %[b] offen offset:0 sc1\n\t"
        "s_mov_b64 exec, s[80:81]\n\t"
        "buffer_load_dwordx4 v[148:151], %[vo], %[rs], %[b] offen offset:1024 sc1\n\t"
        "s_mov_b64 exec, -1\n\t"
        "s_sub_u32 %[cap], %[cap], 1\n\t"
        "s_cbranch_scc1 3f\n\t"
        "2:\n\t"
        "s_waitcnt vmcnt(0)\n\t"
        "v_xor_b32 v156, %[t0], v144\n\t"
        "v_xor_b32 v157, %[t1], v145\n\t"
        "v_xor_b32 v158, %[t2], v146\n\t"
        "v_xor_b32 v159, %[t3], v147\n\t"
        "v_xor_b32 v160, %[t0], v148\n\t"
        "v_xor_b32 v161, %[t1], v149\n\t"
        "v_xor_b32 v162, %[t2], v150\n\t"
        "v_xor_b32 v163, %[t3], v151\n\t"
        "v_or3_b32 v140, v156, v157, v158\n\t"
        "v_or3_b32 v141, v160, v161, v162\n\t"
        "v_bitop3_b32 v140, v140, v159, %[ff] bitop3:0xa8\n\t"
        "v_bitop3_b32 v141, v141, v163, %[ff] bitop3:0xa8\n\t"
        "v_cmp_ne_u32_e64 s[78:79], 0, v140\n\t"
        "v_cmp_ne_u32_e64 s[80:81], 0, v141\n\t"
        "s_or_b64 s[92:93], s[78:79], s[80:81]\n\t"
        "s_cbranch_scc1 1b\n\t"
        "3:\n\t"
        "s_waitcnt vmcnt(0)\n\t"
        "v_add_f32 %[a0], v156, v160\n\t"
        "v_add_f32 %[a1], v157, v161\n\t"
        "v_add_f32 %[a2], v158, v162\n\t"
        "v_add_f32 %[a3], v159, v163\n\t"
        "s_nop 1\n\t"
        : [a0] "=&v"(a0), [a1] "=&v"(a1), [a2] "=&v"(a2), [a3] "=&v"(a3), [cap] "+&s"(cap)
        : [vo] "v"(voff), [rs] "s"(rs), [b] "s"(base), [t0] "s"(tag & 255u), [t1] "s"((tag >> 8) & 255u), [t2] "s"((tag >> 16) & 255u), [t3] "s"(tag >> 24),
          [ff] "s"(255u)
        : "v140", "v141", "v142", "v144", "v145", "v146", "v147", "v148", "v149", "v150", "v151", "v156", "v157", "v158", "v159", "v160", "v161", "v162", "v163", "s78", "s79", "s80", "s81", "s92", "s93", "scc", "memory");
    return fold4(a0, a1, a2, a3);
}
__device__ __forceinline__ float sum_words3(rsrc_t rs, unsigned voff, unsigned base, unsigned tag, unsigned& cap) {
    cap = SPIN_GLB;
    float a0, a1, a2, a3;
    asm volatile(
        "buffer_load_dwordx4 v[144:147], %[vo], %[rs], %[b] offen offset:0 sc1\n\t"
        "buffer_load_dwordx4 v[148:151], %[vo], %[rs], %[b] offen offset:1024 sc1\n\t"
        "buffer_load_dwordx4 v[152:155], %[vo], %[rs], %[b] offen offset:2048 sc1\n\t"
        "s_branch 2f\n\t"
        "1:\n\t"
        "s_mov_b64 exec, s[78:79]\n\t"
        "buffer_load_dwordx4 v[144:147], %[vo], %[rs], %[b] offen offset:0 sc1\n\t"
        "s_mov_b64 exec, s[80:81]\n\t"
        "buffer_load_dwordx4 v[148:151], %[vo], %[rs], %[b] offen offset:1024 sc1\n\t"
        "s_mov_b64 exec, s[82:83]\n\t"
        "buffer_load_dwordx4 v[152:155], %[vo], %[rs], %[b] offen offset:2048 sc1\n\t"
        "s_mov_b64 exec, -1\n\t"
        "s_sub_u32 %[cap], %[cap], 1\n\t"
        "s_cbranch_scc1 3f\n\t"
        "2:\n\t"
        "s_waitcnt vmcnt(0)\n\t"
        "v_xor_b32 v156, %[t0], v144\n\t"
        "v_xor_b32 v157, %[t1], v145\n\t"
        "v_xor_b32 v158, %[t2], v146\n\t"
        "v_xor_b32 v159, %[t3], v147\n\t"
        "v_xor_b32 v160, %[t0], v148\n\t"
        "v_xor_b32 v161, %[t1], v149\n\t"
        "v_xor_b32 v162, %[t2], v150\n\t"
        "v_xor_b32 v163, %[t3], v151\n\t"
        "v_xor_b32 v164, %[t0], v152\n\t"
        "v_xor_b32 v165, %[t1], v153\n\t"
        "v_xor_b32 v166, %[t2], v154\n\t"
        "v_xor_b32 v167, %[t3], v155\n\t"
        "v_or3_b32 v140, v156, v157, v158\n\t"
        "v_or3_b32 v141, v160, v161, v162\n\t"
        "v_or3_b32 v142, v164, v165, v166\n\t"
        "v_bitop3_b32 v140, v140, v159, %[ff] bitop3:0xa8\n\t"
        "v_bitop3_b32 v141, v141, v163, %[ff] bitop3:0xa8\n\t"
        "v_bitop3_b32 v142, v142, v167, %[ff] bitop3:0xa8\n\t"
        "v_cmp_ne_u32_e64 s[78:79], 0, v140\n\t"
        "v_cmp_ne_u32_e64 s[80:81], 0, v141\n\t"
        "v_cmp_ne_u32_e64 s[82:83], 0, v142\n\t"
        "s_or_b64 s[92:93], s[78:79], s[80:81]\n\t"
        "s_or_b64 s[92:93], s[92:93], s[82:83]\n\t"
        "s_cbranch_scc1 1b\n\t"
        "3:\n\t"
        "s_waitcnt vmcnt(0)\n\t"
        "v_add_f32 %[a0], v156, v160\n\t"
        "v_add_f32 %[a1], v157, v161\n\t"
        "v_add_f32 %[a2], v158, v162\n\t"
        "v_add_f32 %[a3], v159, v163\n\t"
        "v_add_f32 %[a0], %[a0], v164\n\t"
        "v_add_f32 %[a1], %[a1], v165\n\t"
        "v_add_f32 %[a2], %[a2], v166\n\t"
        "v_add_f32 %[a3], %[a3], v167\n\t"
        "s_nop 1\n\t"
        : [a0] "=&v"(a0), [a1] "=&v"(a1), [a2] "=&v"(a2), [a3] "=&v"(a3), [cap] "+&s"(cap)
        : [vo] "v"(voff), [rs] "s"(rs), [b] "s"(base), [t0] "s"(tag & 255u), [t1] "s"((tag >> 8) & 255u), [t2] "s"((tag >> 16) & 255u), [t3] "s"(tag >> 24),
          [ff] "s"(255u)
        : "v140", "v141", "v142", "v144", "v145", "v146", "v147", "v148", "v149", "v150", "v151", "v152", "v153", "v154", "v155", "v156", "v157", "v158", "v159", "v160", "v161", "v162", "v163", "v164", "v165", "v166", "v167", "s78", "s79", "s80", "s81", "s82", "s83", "s92", "s93", "scc", "memory");
    return fold4(a0, a1, a2, a3);
}
__device__ __forceinline__ float sum_words_w(rsrc_t rs, unsigned voff, unsigned base, unsigned tag, unsigned& cap, v4u own, u64 m0, u64 m1, float w0, float w1, unsigned napn) {
    cap = SPIN_GLB;
    float a0, a1, a2, a3;
    asm volatile(
        "s_mov_b64 s[78:79], %[m0]\n\t"
        "v_mov_b32 v144, %[o0]\n\t"
        "v_mov_b32 v145, %[o1]\n\t"
        "v_mov_b32 v146, %[o2]\n\t"
        "v_mov_b32 v147, %[o3]\n\t"
        "s_mov_b64 s[80:81], %[m1]\n\t"
        "v_mov_b32 v148, %[o0]\n\t"
        "v_mov_b32 v149, %[o1]\n\t"
        "v_mov_b32 v150, %[o2]\n\t"
        "v_mov_b32 v151, %[o3]\n\t"
        "s_cmp_eq_u32 %[nap], 0\n\t"
        "s_cbranch_scc1 1f\n\t"
        "4:\n\t"
        "s_sleep 1\n\t"
        "s_sub_u32 %[nap], %[nap], 1\n\t"
        "s_cmp_lg_u32 %[nap], 0\n\t"
        "s_cbranch_scc1 4b\n\t"
        "1:\n\t"
        "s_mov_b64 exec, s[78:79]\n\t"
        "buffer_load_dwordx4 v[144:147], %[vo], %[rs], %[b] offen offset:0 sc1\n\t"
        "s_mov_b64 exec, s[80:81]\n\t"
        "buffer_load_dwordx4 v[148:151], %[vo], %[rs], %[b] offen offset:3072 sc1\n\t"
        "s_mov_b64 exec, -1\n\t"
        "s_sub_u32 %[cap], %[cap], 1\n\t"
        "s_cbranch_scc1 3f\n\t"
        "2:\n\t"
        "s_waitcnt vmcnt(0)\n\t"
        "v_xor_b32 v156, %[t0], v144\n\t"
        "v_xor_b32 v157, %[t1], v145\n\t"
        "v_xor_b32 v158, %[t2], v146\n\t"
        "v_xor_b32 v159, %[t3], v147\n\t"
        "v_xor_b32 v160, %[t0], v148\n\t"
        "v_xor_b32 v161, %[t1], v149\n\t"
        "v_xor_b32 v162, %[t2], v150\n\t"
        "v_xor_b32 v163, %[t3], v151\n\t"
        "v_or3_b32 v140, v156, v157, v158\n\t"
        "v_or3_b32 v141, v160, v161, v162\n\t"
        "v_bitop3_b32 v140, v140, v159, %[ff] bitop3:0xa8\n\t"
        "v_bitop3_b32 v141, v141, v163, %[ff] bitop3:0xa8\n\t"
        "v_cmp_ne_u32_e64 s[78:79], 0, v140\n\t"
        "v_cmp_ne_u32_e64 s[80:81], 0, v141\n\t"
        "s_or_b64 s[92:93], s[78:79], s[80:81]\n\t"
        "s_cbranch_scc1 1b\n\t"
        "3:\n\t"
        "s_waitcnt vmcnt(0)\n\t"
        "v_mul_f32 %[a0], %[w0], v156\n\t"
        "v_mul_f32 %[a1], %[w0], v157\n\t"
        "v_mul_f32 %[a2], %[w0], v158\n\t"
        "v_mul_f32 %[a3], %[w0], v159\n\t"
        "v_fmac_f32 %[a0], %[w1], v160\n\t"
        "v_fmac_f32 %[a1], %[w1], v161\n\t"
        "v_fmac_f32 %[a2], %[w1], v162\n\t"
        "v_fmac_f32 %[a3], %[w1], v163\n\t"
        "s_nop 1\n\t"
        : [a0] "=&v"(a0), [a1] "=&v"(a1), [a2] "=&v"(a2), [a3] "=&v"(a3), [cap] "+&s"(cap), [nap] "+&s"(napn)
        : [vo] "v"(voff), [rs] "s"(rs), [b] "s"(base), [t0] "s"(tag & 255u), [t1] "s"((tag >> 8) & 255u), [t2] "s"((tag >> 16) & 255u), [t3] "s"(tag >> 24),
          [ff] "s"(255u), [o0] "v"(own[0]), [o1] "v"(own[1]), [o2] "v"(own[2]), [o3] "v"(own[3]), [m0] "s"(m0), [m1] "s"(m1), [w0] "v"(w0), [w1] "v"(w1)
        : "v140", "v141", "v142", "v144", "v145", "v146", "v147", "v148", "v149", "v150", "v151", "v156", "v157", "v158", "v159", "v160", "v161", "v162", "v163", "s78", "s79", "s80", "s81", "s92", "s93", "scc", "memory");
    return fold4(a0, a1, a2, a3);
}
__device__ __forceinline__ s16x4 trd(LAS unsigned char* L, unsigned off) { return __builtin_amdgcn_ds_read_tr16_b64_v4i16((LAS s16x4*)(L + off)); }
__device__ __forceinline__ bf16x8 cat8(s16x4 a, s16x4 b) { return __builtin_bit_cast(bf16x8, __builtin_shufflevector(a, b, 0, 1, 2, 3, 4, 5, 6, 7)); }
__device__ __forceinline__ f32x4 mfma32(bf16x8 a, bf16x8 b, f32x4 c) { return __builtin_amdgcn_mfma_f32_16x16x32_bf16(a, b, c, 0, 0, 0); }
__device__ __forceinline__ bf16x8 asb(v4u a) { return __builtin_bit_cast(bf16x8, a); }

__device__ __forceinline__ void spin_rd(int c, unsigned n, unsigned a, bf16x8 (&o)[1]) {
    unsigned f, cap = SPIN_LDS;
    const unsigned fa = L_CNT + 4 * c;
    asm volatile(
        "1:\n\t"
        "ds_read_b32 %[f], %[fa]\n\t"
        "ds_read_b128 %[o0], %[a] offset:0\n\t"
        "s_sub_u32 %[cap], %[cap], 1\n\t"
        "s_cbranch_scc1 2f\n\t"
        "s_waitcnt lgkmcnt(1)\n\t"
        "v_readfirstlane_b32 s92, %[f]\n\t"
        "s_cmp_lt_u32 s92, %[n]\n\t"
        "s_cbranch_scc1 1b\n\t"
        "2:\n\t"
        "s_waitcnt lgkmcnt(0)"
        : [f] "=&v"(f), [cap] "+&s"(cap), [o0] "=&v"(o[0]) : [fa] "v"(fa), [a] "v"(a), [n] "s"(n) : "s92", "scc", "memory");
}
__device__ __forceinline__ void spin_rd(int c, unsigned n, unsigned a, bf16x8 (&o)[3]) {
    unsigned f, cap = SPIN_LDS;
    const unsigned fa = L_CNT + 4 * c;
    asm volatile(
        "1:\n\t"
        "ds_read_b32 %[f], %[fa]\n\t"
        "ds_read_b128 %[o0], %[a] offset:0\n\t"
        "ds_read_b128 %[o1], %[a] offset:64\n\t"
        "ds_read_b128 %[o2], %[a] offset:128\n\t"
        "s_sub_u32 %[cap], %[cap], 1\n\t"
        "s_cbranch_scc1 2f\n\t"
        "s_waitcnt lgkmcnt(3)\n\t"
        "v_readfirstlane_b32 s92, %[f]\n\t"
        "s_cmp_lt_u32 s92, %[n]\n\t"
        "s_cbranch_scc1 1b\n\t"
        "2:\n\t"
        "s_waitcnt lgkmcnt(0)"
        : [f] "=&v"(f), [cap] "+&s"(cap), [o0] "=&v"(o[0]), [o1] "=&v"(o[1]), [o2] "=&v"(o[2]) : [fa] "v"(fa), [a] "v"(a), [n] "s"(n) : "s92", "scc", "memory");
}
__device__ __forceinline__ void spin_rd(int c, unsigned n, unsigned a, bf16x8 (&o)[6]) {
    unsigned f, cap = SPIN_LDS;
    const unsigned fa = L_CNT + 4 * c;
    asm volatile(
        "1:\n\t"
        "ds_read_b32 %[f], %[fa]\n\t"
        "ds_read_b128 %[o0], %[a] offset:0\n\t"
        "ds_read_b128 %[o1], %[a] offset:64\n\t"
        "ds_read_b128 %[o2], %[a] offset:128\n\t"
        "ds_read_b128 %[o3], %[a] offset:192\n\t"
        "ds_read_b128 %[o4], %[a] offset:256\n\t"
        "ds_read_b128 %[o5], %[a] offset:320\n\t"
        "s_sub_u32 %[cap], %[cap], 1\n\t"
        "s_cbranch_scc1 2f\n\t"
        "s_waitcnt lgkmcnt(6)\n\t"
        "v_readfirstlane_b32 s92, %[f]\n\t"
        "s_cmp_lt_u32 s92, %[n]\n\t"
        "s_cbranch_scc1 1b\n\t"
        "2:\n\t"
        "s_waitcnt lgkmcnt(0)"
        : [f] "=&v"(f), [cap] "+&s"(cap), [o0] "=&v"(o[0]), [o1] "=&v"(o[1]), [o2] "=&v"(o[2]), [o3] "=&v"(o[3]), [o4] "=&v"(o[4]), [o5] "=&v"(o[5]) : [fa] "v"(fa), [a] "v"(a), [n] "s"(n) : "s92", "scc", "memory");
}
template <int OFF> __device__ __forceinline__ void spin_tr(int c, unsigned n, const unsigned (&a)[3], s16x4 (&o)[6]) {
    unsigned f, cap = SPIN_LDS;
    const unsigned fa = L_CNT + 4 * c;
    asm volatile(
        "1:\n\t"
        "ds_read_b32 %[f], %[fa]\n\t"
        "ds_read_b64_tr_b16 %[o0], %[a0] offset:%c[off0]\n\t"
        "ds_read_b64_tr_b16 %[o1], %[a0] offset:%c[off1]\n\t"
        "ds_read_b64_tr_b16 %[o2], %[a1] offset:%c[off0]\n\t"
        "ds_read_b64_tr_b16 %[o3], %[a1] offset:%c[off1]\n\t"
        "ds_read_b64_tr_b16 %[o4], %[a2] offset:%c[off0]\n\t"
        "ds_read_b64_tr_b16 %[o5], %[a2] offset:%c[off1]\n\t"
        "s_sub_u32 %[cap], %[cap], 1\n\t"
        "s_cbranch_scc1 2f\n\t"
        "s_waitcnt lgkmcnt(6)\n\t"
        "v_readfirstlane_b32 s92, %[f]\n\t"
        "s_cmp_lt_u32 s92, %[n]\n\t"
        "s_cbranch_scc1 1b\n\t"
        "2:\n\t"
        "s_waitcnt lgkmcnt(0)"
        : [f] "=&v"(f), [cap] "+&s"(cap), [o0] "=&v"(o[0]), [o1] "=&v"(o[1]), [o2] "=&v"(o[2]), [o3] "=&v"(o[3]), [o4] "=&v"(o[4]), [o5] "=&v"(o[5])
        : [fa] "v"(fa), [a0] "v"(a[0]), [a1] "v"(a[1]), [a2] "v"(a[2]), [n] "s"(n), [off0] "n"(OFF), [off1] "n"(OFF + 1536) : "s92", "scc", "memory");
}
__device__ __forceinline__ void ln_stats(const unsigned (&xp)[5], float& mb, float& rden) {
    const float c = lo16(__builtin_amdgcn_readfirstlane(xp[0]));
    float s = 0.f, s2 = 0.f;
#pragma unroll
    for (int i = 0; i < 5; ++i) {
        const float a = lo16(xp[i]) - c, b = hi16(xp[i]) - c;
        s += a + b; s2 = fmaf(a, a, s2); s2 = fmaf(b, b, s2);
    }
    s = wave_sum(s); s2 = wave_sum(s2);
    const float dm = s * (1.0f / D);
    const float var = fmaxf(s2 * (1.0f / D) - dm * dm, 0.f);
    mb = rbf(c + dm);
    const float den = rbf(__builtin_amdgcn_sqrtf(rbf(rbf(var) + 1e-5f)));
    rden = __builtin_amdgcn_rcpf(den);
}
__device__ __forceinline__ void ln_stats_fast(const unsigned (&xp)[5], float& mb, float& rden) {
    float s = dot2(xp[0], 0x3F803F80u, 0.f), s2 = dot2(xp[0], xp[0], 0.f);
#pragma unroll
    for (int i = 1; i < 5; ++i) { s = dot2(xp[i], 0x3F803F80u, s); s2 = dot2(xp[i], xp[i], s2); }
    auto w = __builtin_amdgcn_permlane32_swap(__float_as_uint(s), __float_as_uint(s2), false, false);
    float t = fv(w[0]) + fv(w[1]);
    t += dpp<XOR1>(t); t += dpp<XOR2>(t); t += dpp<XOR7>(t); t += dpp<XOR8>(t);
    t = sw16_sum(t) * (1.0f / D);
    auto u = __builtin_amdgcn_permlane32_swap(__float_as_uint(t), __float_as_uint(t), false, false);
    const float mean = fv(u[0]);
    const float var = fmaxf(fmaf(-mean, mean, fv(u[1])), 0.f);
    mb = rbf(mean);
    rden = __builtin_amdgcn_rcpf(rbf(__builtin_amdgcn_sqrtf(rbf(rbf(var) + 1e-5f))));
}
__device__ __forceinline__ unsigned ln_pair(unsigned w, float mb, float rden) {
    const unsigned d = pk2(lo16(w) - mb, hi16(w) - mb);
    return pk2(lo16(d) * rden, hi16(d) * rden);
}

__device__ __forceinline__ unsigned lateness(rsrc_t rs, int lane, u64 t_start) {
    const v2u t = __builtin_amdgcn_raw_buffer_load_b64(rs, WS_T0 + (lane & 7) * 128, 0, 16);
    const u64 d = t_start - (((u64)t[1] << 32) | t[0]);
    float l = d < 150ull ? (float)(unsigned)d : 0.f;
    l = fmaxf(l, dpp<XOR1>(l)); l = fmaxf(l, dpp<XOR2>(l)); l = fmaxf(l, dpp<XOR7>(l));
    return __builtin_amdgcn_readfirstlane((unsigned)l);
}

#define LD_REPORT \
    "s_getreg_b32 s92, hwreg(HW_REG_IB_STS)\n\t" \
    "s_and_b32 s93, s92, 15\n\t" \
    "s_bfe_u32 s92, s92, 0x20016\n\t" \
    "s_lshl4_add_u32 s93, s92, s93\n\t" \
    "s_sub_u32 s92, %[iss], s93\n\t" \
    "v_mov_b32 %[vt], s92\n\t"
#define LD_WRITE \
    "s_mov_b64 exec, 1\n\t" \
    "ds_write_b32 %[va], %[vt]\n\t" \
    "s_mov_b64 exec, -1\n\t"
struct Ld { unsigned issued, tnext, pace, va, sh, w0; };
template <bool FLIP> __device__ __forceinline__ void ld_stream(Ld& s, rsrc_t rs, unsigned (&o)[6], unsigned soff, unsigned sstep, unsigned dst, int ng, unsigned wrap = ~0u) {
    unsigned vt;
    const unsigned end = __builtin_amdgcn_readfirstlane(s.issued + 6 * ng);
    soff = __builtin_amdgcn_readfirstlane(soff); dst = __builtin_amdgcn_readfirstlane(dst);
    s.issued = __builtin_amdgcn_readfirstlane(s.issued); s.tnext = __builtin_amdgcn_readfirstlane(s.tnext); s.pace = __builtin_amdgcn_readfirstlane(s.pace);
    asm volatile(
        "s_memrealtime s[90:91]\n\t"
        "1:\n\t"
        LD_REPORT
        "s_waitcnt lgkmcnt(0)\n\t"
        "s_lshl_b32 s92, s90, 2\n\t"
        "s_memrealtime s[90:91]\n\t"
        LD_WRITE
        "s_sub_u32 s92, s92, %[tn]\n\t"
        "s_cmp_lt_i32 s92, 0\n\t"
        "s_cbranch_scc1 1b\n\t"
        "s_lshr_b32 s92, s92, %[sh]\n\t"
        "s_add_u32 s92, s92, %[w0]\n\t"
        "s_min_u32 s92, s92, %[wm]\n\t"
        "s_cmp_gt_u32 s93, s92\n\t"
        "s_cbranch_scc1 1b\n\t"
        "s_mov_b32 m0, %[dst]\n\t"
        "s_add_u32 %[tn], %[tn], %[pace]\n\t"
        "buffer_load_dwordx4 %[o0], %[rs], %[so] offen nt lds\n\t"
        "buffer_load_dwordx4 %[o1], %[rs], %[so] offen offset:1024 nt lds\n\t"
        "buffer_load_dwordx4 %[o2], %[rs], %[so] offen offset:2048 nt lds\n\t"
        "s_add_u32 m0, %[dst], 3072\n\t"
        "s_add_u32 %[iss], %[iss], 6\n\t"
        "buffer_load_dwordx4 %[o3], %[rs], %[so] offen nt lds\n\t"
        "buffer_load_dwordx4 %[o4], %[rs], %[so] offen offset:1024 nt lds\n\t"
        "buffer_load_dwordx4 %[o5], %[rs], %[so] offen offset:2048 nt lds\n\t"
        "s_add_u32 %[dst], %[dst], 6144\n\t"
        "s_add_u32 %[so], %[so], %[ss]\n\t"
        "s_cmp_eq_u32 %[so], %[wrap]\n\t"
        "s_cselect_b32 %[so], 0, %[so]\n\t"
        ".if %c[flip]\n\t"
        "v_xor_b32 %[o0], 0x80, %[o0]\n\tv_xor_b32 %[o1], 0x80, %[o1]\n\tv_xor_b32 %[o2], 0x80, %[o2]\n\t"
        "v_xor_b32 %[o3], 0x80, %[o3]\n\tv_xor_b32 %[o4], 0x80, %[o4]\n\tv_xor_b32 %[o5], 0x80, %[o5]\n\t"
        ".endif\n\t"
        "s_cmp_lg_u32 %[iss], %[end]\n\t"
        "s_cbranch_scc1 1b\n\t"
        "s_waitcnt lgkmcnt(0)"
        : [iss] "+&s"(s.issued), [tn] "+&s"(s.tnext), [dst] "+&s"(dst), [so] "+&s"(soff), [vt] "=&v"(vt),
          [o0] "+&v"(o[0]), [o1] "+&v"(o[1]), [o2] "+&v"(o[2]), [o3] "+&v"(o[3]), [o4] "+&v"(o[4]), [o5] "+&v"(o[5])
        : [rs] "s"(rs), [ss] "s"(sstep), [end] "s"(end), [pace] "s"(s.pace), [va] "v"(s.va), [wrap] "s"(wrap), [wm] "n"(WMAX - 6), [sh] "s"(s.sh), [w0] "s"(s.w0), [flip] "n"(FLIP ? 1 : 0)
        : "s90", "s91", "s92", "s93", "scc", "memory");
    s.issued = end;
    s.tnext = __builtin_amdgcn_readfirstlane(s.tnext);
}
__device__ __forceinline__ void ld_gate(Ld& s, int c, unsigned n) {
    unsigned vt, vg, cap = SPIN_LDS;
    const unsigned ga = L_CNT + 4 * c;
    asm volatile(
        "1:\n\t"
        "ds_read_b32 %[vg], %[ga]\n\t"
        LD_REPORT
        LD_WRITE
        "s_sub_u32 %[cap], %[cap], 1\n\t"
        "s_cbranch_scc1 2f\n\t"
        "s_waitcnt lgkmcnt(1)\n\t"
        "v_readfirstlane_b32 s92, %[vg]\n\t"
        "s_cmp_lt_u32 s92, %[n]\n\t"
        "s_cbranch_scc1 1b\n\t"
        "2:\n\t"
        "s_waitcnt lgkmcnt(0)"
        : [vt] "=&v"(vt), [vg] "=&v"(vg), [cap] "+&s"(cap)
        : [iss] "s"(s.issued), [va] "v"(s.va), [ga] "v"(ga), [n] "s"(n)
        : "s92", "s93", "scc", "memory");
}
__device__ __forceinline__ void ld_drain(Ld& s) {
    unsigned vt;
    asm volatile(
        "1:\n\t"
        LD_REPORT
        LD_WRITE
        "s_cmp_lg_u32 s93, 0\n\t"
        "s_cbranch_scc1 1b\n\t"
        : [vt] "=&v"(vt) : [iss] "s"(s.issued), [va] "v"(s.va) : "s92", "s93", "scc", "memory");
}

__global__ void __launch_bounds__(640, 3) k_block(const u16* __restrict__ x, const u16* __restrict__ kc, const u16* __restrict__ vc,
                                               const u16* __restrict__ wq, const u16* __restrict__ wo, const u16* __restrict__ wg,
                                               const u16* __restrict__ wu, const u16* __restrict__ wd, u16* __restrict__ out,
                                               unsigned char* ws) {
    extern __shared__ __attribute__((aligned(16))) unsigned char lds_raw[];
    LAS unsigned char* L = (LAS unsigned char*)lds_raw;
#define QNAPC 3
#define BARAT 1
#define CODEPF 23168
    static_assert(L_W == 0, "first tile");
    u64 t_start;
    asm volatile(
        "s_load_dwordx2 s[64:65], %[ka], 0x18\n\t"
        "s_load_dwordx2 s[66:67], %[ka], 0x38\n\t"
        "s_load_dword s78, %[ka], 0x40\n\t"
        "s_getpc_b64 s[68:69]\n\t"
        "v_readfirstlane_b32 s72, %[tid]\n\t"
        "v_and_b32 v140, 63, %[tid]\n\t"
        "s_lshr_b32 s72, s72, 6\n\t"
        "s_cmp_eq_u32 s72, 0\n\t"
        "s_cbranch_scc1 4f\n\t"
        "s_cmp_eq_u32 s72, 1\n\t"
        "s_cbranch_scc0 2f\n\t"
        "s_and_b32 s68, s68, 0xffffff00\n\t"
        "s_and_b32 s69, s69, 0xffff\n\t"
        "s_mov_b32 s70, %[pf]\n\t"
        "s_mov_b32 s71, 0x20000\n\t"
        "v_lshlrev_b32 v141, 4, v140\n\t"
        "s_lshr_b32 s73, %[b], 3\n\t"
        "s_mov_b32 m0, %[junk]\n\t"
        "s_lshl_b32 s73, s73, 10\n\t"
        "buffer_load_dwordx4 v141, s[68:71], s73 offen lds\n\t"
        "2:\n\t"
        "s_cmp_eq_u32 s72, 2\n\t"
        "s_cbranch_scc0 3f\n\t"
        "v_lshlrev_b32 v141, 2, v140\n\t"
        "v_mov_b32 v142, 0\n\t"
        "v_add_u32 v141, %[cnt], v141\n\t"
        "ds_write_b32 v141, v142\n\t"
        "ds_write_b32 v141, v142 offset:256\n\t"
        "3:\n\t"
        "s_waitcnt lgkmcnt(0)\n\t"
        "s_barrier\n\t"
        "s_memrealtime %[t]\n\t"
        "s_branch 9f\n\t"
        "4:\n\t"
        "s_bfe_u32 s73, %[b], 0x30003\n\t"
        "s_lshr_b32 s74, %[b], 6\n\t"
        "s_and_b32 s75, %[b], 7\n\t"
        "v_lshrrev_b32 v141, 3, v140\n\t"
        "s_cmp_lt_u32 s73, 2\n\t"
        "s_cbranch_scc1 5f\n\t"
        "s_lshl2_add_u32 s75, s75, s74\n\t"
        "s_mul_i32 s75, s75, 3\n\t"
        "s_add_u32 s75, s75, s73\n\t"
        "s_cmp_gt_u32 s73, 4\n\t"
        "s_cselect_b32 s76, 30715, -2\n\t"
        "s_add_u32 s75, s75, s76\n\t"
        "s_lshl_b32 s75, s75, 7\n\t"
        "v_and_b32 v142, 7, v140\n\t"
        "v_bitop3_b32 v142, v141, v142, 2 bitop3:0x6c\n\t"
        "v_mul_u32_u24 v141, 0x3000, v141\n\t"
        "v_lshl_or_b32 v141, v142, 4, v141\n\t"
        "v_add_u32 v143, s75, v141\n\t"
        "v_xor_b32 v144, 64, v143\n\t"
        "v_add_u32 v145, 0x2f800, v143\n\t"
        "v_add_u32 v146, 0x47400, v144\n\t"
        "v_add_u32 v144, 0x17c00, v144\n\t"
        "s_mov_b32 s76, 0x60000\n\t"
        "s_waitcnt lgkmcnt(0)\n\t"
        "s_mov_b32 s66, 0x780000\n\t"
        "s_branch 6f\n\t"
        "5:\n\t"
        "s_cmp_gt_u32 s74, 2\n\t"
        "s_cbranch_scc1 3b\n\t"
        "s_mul_i32 s75, s75, 3\n\t"
        "s_add_u32 s75, s75, s74\n\t"
        "s_lshl1_add_u32 s75, s75, s73\n\t"
        "s_mul_i32 s75, s75, 0xa000\n\t"
        "v_bitop3_b32 v141, v141, v140, 6 bitop3:0x6c\n\t"
        "v_lshlrev_b32 v141, 4, v141\n\t"
        "v_add_u32 v143, s75, v141\n\t"
        "v_mov_b32 v144, v143\n\t"
        "v_xor_b32 v145, 0x80, v143\n\t"
        "v_mov_b32 v146, v145\n\t"
        "s_movk_i32 s76, 0x1000\n\t"
        "s_waitcnt lgkmcnt(0)\n\t"
        "s_mov_b64 s[64:65], s[66:67]\n\t"
        "s_mov_b32 s66, 0x1e0000\n\t"
        "6:\n\t"
        "s_and_b32 s65, s65, 0xffff\n\t"
        "s_mov_b32 s67, 0x20000\n\t"
        "s_mov_b32 m0, 0\n\t"
        "s_mov_b32 s77, 0\n\t"
        "7:\n\t"
        "buffer_load_dwordx4 v143, s[64:67], s77 offen nt lds\n\t"
        "buffer_load_dwordx4 v144, s[64:67], s77 offen offset:1024 nt lds\n\t"
        "buffer_load_dwordx4 v145, s[64:67], s77 offen offset:2048 nt lds\n\t"
        "buffer_load_dwordx4 v146, s[64:67], s77 offen offset:3072 nt lds\n\t"
        "s_add_u32 s77, s77, s76\n\t"
        "s_add_u32 m0, m0, 0x1000\n\t"
        "s_cmp_eq_u32 m0, %[barat]\n\t"
        "s_cbranch_scc0 8f\n\t"
        "s_barrier\n\t"
        "s_memrealtime %[t]\n\t"
        "8:\n\t"
        "s_mov_b32 s79, %[nap]\n\t"
        "10:\n\t"
        "s_sleep 1\n\t"
        "s_sub_u32 s79, s79, 1\n\t"
        "s_cmp_gt_i32 s79, 0\n\t"
        "s_cbranch_scc1 10b\n\t"
        "s_cmp_lg_u32 m0, 0xa000\n\t"
        "s_cbranch_scc1 7b\n\t"
        "v_mov_b32 v141, %[wqf]\n\t"
        "v_mov_b32 v142, 1\n\t"
        "s_waitcnt vmcnt(0)\n\t"
        "s_mov_b64 exec, 1\n\t"
        "ds_add_u32 v141, v142\n\t"
        "s_mov_b64 exec, -1\n\t"
        "9:\n\t"
        "s_waitcnt lgkmcnt(0)\n\t"
        : [t] "=&s"(t_start)
        : [ka] "s"(__builtin_amdgcn_kernarg_segment_ptr()), [tid] "v"(threadIdx.x), [b] "s"(blockIdx.x), [pf] "n"(CODEPF), [junk] "n"(L_JUNK + 256), [cnt] "n"(L_CNT),
          [wqf] "n"(L_CNT + 4 * C_WQ), [nap] "n"(QNAPC), [barat] "n"(BARAT * 4096)
        : "s64", "s65", "s66", "s67", "s68", "s69", "s70", "s71", "s72", "s73", "s74", "s75", "s76", "s77", "s78", "s79", "v140", "v141", "v142", "v143", "v144", "v145", "v146", "scc", "memory");
    const int pw = __builtin_amdgcn_readfirstlane(threadIdx.x >> 6);
    const int lane = threadIdx.x & 63;
    const int wave = pw == 0 ? 4 : pw == 4 ? 0 : pw;
    const int b = blockIdx.x, xcd = b & 7, idx = b >> 3;
    const int h = xcd * 4 + (idx >> 3), j = idx & 7;
    const int g = lane >> 4, tq = (lane & 15) >> 2, tp = lane & 3, i16 = lane & 15;
    const bool qcu = j >= 2;
    const int qr = (j + 3) >> 3, qc = j - 2 - 3 * qr;
    const int gi = (idx >> 3) * 2 + j;
    const int gcu = (!qcu && gi < 6) ? xcd * 6 + gi : -1;
    const bool pre = qcu || gcu >= 0;
    const rsrc_t rws = mkrs(ws, WS_END);
    unsigned tag, xp[5];
    asm volatile("" : "=v"(xp[0]), "=v"(xp[1]), "=v"(xp[2]), "=v"(xp[3]), "=v"(xp[4]), "=v"(tag));
    if (__builtin_expect(wave == 9, 1)) {
        const unsigned xo = lane * 4, zo = 0;
        __builtin_amdgcn_raw_buffer_store_b64(v2u{(unsigned)t_start, (unsigned)(t_start >> 32)}, rws, (lane == 0 && idx == 0) ? WS_T0 + xcd * 128 : OOB, 0, 16);
        asm volatile("global_load_dword %0, %6, %7\n\tglobal_load_dword %1, %6, %7 offset:256\n\tglobal_load_dword %2, %6, %7 offset:512\n\t"
                     "global_load_dword %3, %6, %7 offset:768\n\tglobal_load_dword %4, %6, %7 offset:1024\n\tglobal_load_dword %5, %8, %9 sc1"
                     : "+v"(xp[0]), "+v"(xp[1]), "+v"(xp[2]), "+v"(xp[3]), "+v"(xp[4]), "+v"(tag) : "v"(xo), "s"(x), "v"(zo), "s"(ws) : "memory");
    }
    const bool wocu = j < 5;
    const int oi = (idx >> 3) * 3 + (j - 2), oc = (idx >> 3) * 3 + (j - 5);
    const int ocu = (wocu && qcu && oi < 5) ? xcd * 5 + oi : -1;
    const int n1 = __builtin_amdgcn_readfirstlane(!pre ? N1G + 4 : wocu ? N1G : 0);
    const int pv0 = P_K + 6 * n1;
    const f32x4 z4 = {0.f, 0.f, 0.f, 0.f};
    auto sel4 = [&](float a0, float a1, float a2, float a3) {
        const float lo = (lane & 16) ? a1 : a0, hi = (lane & 16) ? a3 : a2;
        return (lane & 32) ? hi : lo;
    };
    auto t_end = [&]() { return t_start - __builtin_amdgcn_readfirstlane(*(volatile LAS unsigned*)(L + L_CNT + 4 * C_LATE)) + (TE + 128); };
    auto note_late = [&](int word, u64 deadline, unsigned no) {
        const unsigned to = lane < 8 ? WS_TP + lane * 256 + word * 4 : OOB;
        until_hot(deadline);
        const v2u f = {*(volatile LAS unsigned*)(L + L_CNT + 4 * C_PROG), *(volatile LAS unsigned*)(L + L_CNT + 4 * C_O)};
        if ((unsigned)__builtin_amdgcn_readfirstlane(f[0]) < (unsigned)P_WO || (unsigned)__builtin_amdgcn_readfirstlane(f[1]) < no) {
            putw(rws, to, (unsigned)deadline + PEND);
            lds_wait<false>(L, C_PROG, P_WO);
            lds_wait<false>(L, C_O, no);
            const unsigned tr = (unsigned)__builtin_amdgcn_s_memrealtime();
            waitvm<0>();
            putw(rws, to, tr);
        }
    };
    unsigned rc;
    const int col = 4 * tp + g;
    auto make_h = [&](int a) {
        float xr = lo16(x[80 * xcd + 16 * a + col]);
        unsigned ha = L_O + (16 * a + col) * 2, ho = (tq == 0 && !(g & 1) && oc < 8) ? WS_HB + oc * 4096 + (40 * xcd + 8 * a + (col >> 1)) * 8 : OOB;
        PIN(ha); PIN(ho); PIN(xr);
        lds_wait<true>(L, C_OWN, 4);
        delay(D2);
        until_hot(t_end() + G2);
        float s = sum_words2(rws, lane * 16, WS_AP + (5 * xcd + a) * 2048, tag, rc);
        s += dpp<XOR8>(s); s += dpp<ROR4>(s);
        const unsigned hb = bfbits(xr + rbf(s));
        *(LAS u16*)(L + ha) = (u16)hb;
        lds_signal(C_O);
        auto pr = __builtin_amdgcn_permlane16_swap(hb, hb, false, false);
        put1(rws, ho, tag, pr[0] | (pr[1] << 16));
    };
    auto look_ap = [&](bool all) {
        lds_wait<true>(L, C_O, 3);
        const unsigned s0 = (unsigned)t_start, t2 = (unsigned)(t_end() - t_start) + G2;
        until(t_start + t2 + (all ? DOBS_ALL : DOBS));
        unsigned tl = 0;
#pragma unroll 1
        for (int it = 0; it < 128; ++it) {
            MEMBAR();
            const unsigned t = __builtin_amdgcn_raw_buffer_load_b32(rws, WS_TP + xcd * 256 + (all ? lane : lane & 31) * 4, 0, 16) - s0;
            if (!__any(t - PEND < 4096u)) {
                const unsigned c = t >= 4096u ? 0u : (all && lane >= 32) ? t + G3 + LATE_F1_ADD : t + D3;
                tl = __builtin_amdgcn_readfirstlane((unsigned)wave_max((float)c));
                break;
            }
            __builtin_amdgcn_s_sleep(2);
        }
        return t_start + max(tl, t2 + G3);
    };

    if (__builtin_expect(wave == 0, 1)) {
        __builtin_amdgcn_s_setprio(3);
        unsigned ko[6], vo[6], po[6];
#pragma unroll
        for (int c = 0; c < 3; ++c) {
            const unsigned P = 64 * c + lane, r = P / 24, ch = P - r * 24;
            ko[c] = r * 384 + (ch ^ (r >> 1)) * 16 - c * 1024; ko[c + 3] = (ko[c] ^ 64) + 3072;
        }
        const size_t kvoff = ((size_t)h * SK + (SK - W) + j * 16) * DH;
        constexpr unsigned gs = 49152, kvn = 15 * gs + 6144;
        Ld s;
        s.issued = 0; s.va = L_CNT + 4 * C_PROG; s.w0 = WIN0; s.sh = WINSH;
        s.pace = ((unsigned)(TE - (pre ? TAQ : 40) - (7 * TAPER) / 4) * (!pre ? 262144 / (35 + N1G) : wocu ? 262144 / (31 + N1G) : 262144 / 31)) >> 16;
        s.tnext = ((unsigned)t_start + (pre ? TAQ : 40)) * 4;
        const unsigned rn = h & 15;
        const rsrc_t rk = mkrs(kc + kvoff, kvn);
        ld_stream<false>(s, rk, ko, rn * gs, gs, L_V, 13, 16 * gs);
        lds_set(C_LK, 1u + min((unsigned)__builtin_amdgcn_s_memrealtime() * 4 - (s.tnext - s.pace), 400u));
        ld_gate(s, C_LATE, 1);
        const unsigned late = __builtin_amdgcn_readfirstlane(*(volatile LAS unsigned*)(L + L_CNT + 4 * C_LATE)) - 128;
        s.pace -= (late * (!pre ? 262144 / (22 + N1G) : wocu ? 262144 / (18 + N1G) : 262144 / 18)) >> 16;
        ld_stream<false>(s, rk, ko, ((rn + 13) & 15) * gs, gs, L_V + 13 * 6144, 3, 16 * gs);
#pragma unroll
        for (int c = 0; c < 3; ++c) {
            const unsigned P = 64 * c + lane, r = P / 24, ch = P - r * 24;
            vo[c] = r * 384 + (ch ^ (r & 2u)) * 16 - c * 1024; vo[c + 3] = (vo[c] ^ 64) + 3072;
            const unsigned wb = (i16 ^ (g << 1)) * 16;
            const unsigned rst = wocu ? 1280 : 3072;
            po[c] = (4 * c + g) * rst + (wb ^ ((c >> 1) << 7)) - c * 1024;
            po[c + 3] = (4 * c + 12 + g) * rst + (wb ^ ((((c + 3) >> 1) & 1) << 7)) - c * 1024;
        }
        const rsrc_t rv = mkrs(vc + kvoff, kvn);
        const rsrc_t ro = mkrs(wo + (size_t)(h * DH) * D + j * 128, 191 * 1280 + 256);
        ld_gate(s, C_QP, __builtin_amdgcn_readfirstlane(qcu ? 4 : pre ? 8 : 0));
        if (n1) ld_stream<true>(s, ro, po, 0, 30720, L_W, n1);
        ld_gate(s, C_KA, 4);
        ld_stream<false>(s, rv, vo, rn * gs, gs, L_V, 5, 16 * gs);
        lds_set(C_LV, 1u + min((unsigned)__builtin_amdgcn_s_memrealtime() * 4 - (s.tnext - s.pace), 400u));
        ld_gate(s, C_EXT, 1);
        int ext;
        {
            ext = (int)__builtin_amdgcn_readfirstlane(*(volatile LAS unsigned*)(L + L_CNT + 4 * C_EXT)) - 64;
            s.pace += (ext * 26) >> 6;
            lds_set(C_LATE, late + 128 - ext);
        }
        ld_stream<false>(s, rv, vo, ((rn + 5) & 15) * gs, gs, L_V + 5 * 6144, 3, 16 * gs);
        ld_gate(s, C_KB, 4);
        s.pace += TAPER;
        ld_stream<false>(s, rv, vo, (rn ^ 8) * gs, gs, L_V + 49152, 5, 16 * gs);
        ld_gate(s, C_EX2, 1);
        {
            const int e2 = max((int)__builtin_amdgcn_readfirstlane(*(volatile LAS unsigned*)(L + L_CNT + 4 * C_EX2)) - 64 - ext, 0) + ENDPAD;
            s.tnext += (e2 * 4) / 3; s.pace += (e2 * 4) / 3;
            lds_set(C_LATE, late + 128 - ext - e2);
        }
        ld_stream<false>(s, rv, vo, ((rn + 13) & 15) * gs, gs, L_V + 13 * 6144, 3, 16 * gs);
        s.pace = 0; s.w0 = WMAX - 6;
        if (wocu) {
            if (n1 < 8) ld_stream<true>(s, ro, po, n1 * 30720, 30720, L_W + n1 * 6144, 8 - n1);
        } else {
            const rsrc_t r0 = mkrs(wg + (size_t)(80 * xcd) * F + 128 * oc, 79 * 3072 + 256), r1 = mkrs(wu + (size_t)(80 * xcd) * F + 128 * oc, 79 * 3072 + 256);
            s.tnext += FDELAY * 4;
            ld_stream<true>(s, r0, po, 0, 73728, L_W, 4);
            ld_stream<true>(s, r1, po, 0, 73728, L_W + 24576, 4);
        }
        ld_drain(s);
        if (!wocu && __builtin_amdgcn_s_memrealtime() > t_end() + G2 + LATE_F1) putw(rws, lane < 8 ? WS_TP + lane * 256 + (32 + h) * 4 : OOB, (unsigned)__builtin_amdgcn_s_memrealtime());
        lds_wait<true>(L, C_X, 1);
        tag = __builtin_amdgcn_readfirstlane(*(LAS unsigned*)(L + L_TAG));
        if (gcu >= 0) {
            unsigned ha = L_HH + (4 * i16 + g) * 2, ca = L_LN + lane * 4;
            PIN(ha); PIN(ca);
            const u64 t3 = look_ap(false);
            lds_set(C_GO, (unsigned)(t3 - t_start));
            until_hot(t3);
            float sv = sum_words2(rws, lane * 16, WS_GP + gcu * 2048, tag, rc);
            f32x4 st;
            float cs;
            unsigned cap = SPIN_LDS;
#pragma unroll 1
            do {
                st = *(volatile LAS f32x4*)(L + L_ST);
                cs = *(volatile LAS float*)(L + ca);
            } while (__builtin_amdgcn_readfirstlane(__float_as_uint(st[2]) & __float_as_uint(st[3])) == 0u && --cap != 0);
            const float v = rbf((sv - st[0] * cs) * st[1]);
            const float sg = rbf(__builtin_amdgcn_rcpf(1.0f + __expf(-v)));
            *(LAS u16*)(L + ha) = (u16)bfbits(sg * dpp<XOR8>(v));
            lds_signal(C_HH);
        } else if (ocu >= 0) {
            const rsrc_t rout = mkrs(out, 1280);
            unsigned oo = tq == 0 ? (16 * ocu + col) * 2 : OOB;
            PIN(oo);
            lds_wait<true>(L, C_O, 3);
            until(t_end() + G2 + G3);
            v2u hres[1] = {v2u{0u, ~tag}};
            gather<4>(rws, (col >> 1) * 8, WS_HB + xcd * 4096 + 64 * ocu, 0, tag, hres);
            float hr = (g & 1) ? hi16(hres[0][0]) : lo16(hres[0][0]);
            PIN(hr);
            if (ocu == 0 && lane == 0) __hip_atomic_store((gu32*)(ws + WS_EPOCH), tag, RLX_AGENT);
            until_hot(look_ap(true) + D4);
            float s3 = sum_words3(rws, lane * 16, WS_DP + ocu * 3072, tag, rc);
            s3 += dpp<XOR8>(s3); s3 += dpp<ROR4>(s3);
            __builtin_amdgcn_raw_buffer_store_b16((u16)bfbits(hr + rbf(s3)), rout, oo, 0, 16);
        }
    } else if (wave == 9) {
        asm volatile("s_waitcnt vmcnt(0)" : "+v"(xp[0]), "+v"(xp[1]), "+v"(xp[2]), "+v"(xp[3]), "+v"(xp[4]), "+v"(tag) :: "memory");
        float mb, rden;
        ln_stats(xp, mb, rden);
#pragma unroll
        for (int i = 0; i < 5; ++i) *(LAS unsigned*)(L + L_LN + (lane + 64 * i) * 4) = ln_pair(xp[i], mb, rden);
        tag = __builtin_amdgcn_readfirstlane(tag) + 0x01010101u;
        if (lane == 0) *(LAS unsigned*)(L + L_TAG) = tag;
        lds_signal(C_X);
        {
            until(t_start + TQ);
            v4u qp[3];
#pragma unroll
            for (int k = 0; k < 3; ++k) qp[k] = v4u{0u, ~tag, 0u, ~tag};
            gather<8>(rws, lane * 16, WS_QP + h * 3072, 1024, tag, qp);
#pragma unroll
            for (int k = 0; k < 3; ++k) *(LAS unsigned*)(L + L_Q + k * 128 + (lane & 31) * 4) = pk2(sw32_sum(fv(qp[k][0])), sw32_sum(fv(qp[k][2])));
            lds_signal(C_Q);
        }
        lds_wait<true>(L, C_P, 4);
        const f32x4 mx = *(LAS f32x4*)(L + L_MX), ls = *(LAS f32x4*)(L + L_MX + 16);
        const float m = fmaxf(fmaxf(mx[0], mx[1]), fmaxf(mx[2], mx[3]));
        const float l = (ls[0] + ls[1]) + (ls[2] + ls[3]);
        put2(rws, lane == 0 ? WS_ST + (h * 8 + j) * 128 : OOB, tag, m, l);
        if (wocu) {
            until(t_end() + TS);
            const unsigned tt = lane < 8 ? ~tag : tag;
            v4u st[1] = {v4u{0u, tt, 0u, tt}};
            gather<8>(rws, lane * 128, WS_ST + h * 1024, 0, tag, st);
            const float ms = fv(st[0][0]);
            float M = fmaxf(ms, dpp<XOR1>(ms)); M = fmaxf(M, dpp<XOR2>(M)); M = fmaxf(M, dpp<XOR7>(M));
            const float e = __expf(ms - M);
            float Ls = e * fv(st[0][2]);
            Ls += dpp<XOR1>(Ls); Ls += dpp<XOR2>(Ls); Ls += dpp<XOR7>(Ls);
            if (lane < 8) *(LAS float*)(L + L_WGT + lane * 4) = e * __builtin_amdgcn_rcpf(Ls);
            lds_signal(C_W);
            if (gcu >= 0) {
                lds_wait<true>(L, C_O, 3);
                const u64 th = t_end() + G2 + G3 - HB_LEAD;
                until(th - 12);
                until(t_start + __builtin_amdgcn_readfirstlane(*(volatile LAS unsigned*)(L + L_CNT + 4 * C_GO)) - HB_LEAD - 12);
                until_hot(th);
                v2u hv[5];
#pragma unroll
                for (int i = 0; i < 5; ++i) hv[i] = v2u{0u, ~tag};
                rc = gather<2>(rws, lane * 8, WS_HB + xcd * 4096, 512, tag, hv);
#pragma unroll
                for (int i = 0; i < 5; ++i) xp[i] = hv[i][0];
                ln_stats_fast(xp, mb, rden);
                if (lane == 0) { *(LAS v2u*)(L + L_ST) = v2u{__float_as_uint(mb), __float_as_uint(rden)}; MEMBAR(); *(LAS float*)(L + L_ST + 8) = 1.0f; }
            }
        } else {
            make_h(4);
        }
    } else {
        bf16x8 dw[5];
        if (gcu >= 0) {
            lds_wait<true>(L, C_WQ, 1);
#pragma unroll
            for (int t = 0; t < 5; ++t) {
                const int tt = 5 * (wave - 1) + t;
                unsigned ad[2];
#pragma unroll
                for (int u = 0; u < 2; ++u) {
                    const int r = 8 * g + 4 * u + tq, B = 5 * r + (tt >> 3);
                    ad[u] = L_W + r * 1280 + (tt ^ ((B & 3) | ((B >> 1) & 4))) * 32 + tp * 8;
                }
                dw[t] = cat8(trd(L, ad[0]), trd(L, ad[1]));
            }
            lds_signal(C_QP);
        }
        if (wave < 5) {
            const int a = wave - 1;
            lds_wait<true>(L, C_X, 1);
            tag = __builtin_amdgcn_readfirstlane(*(LAS unsigned*)(L + L_TAG));
            if (qcu) {
                bf16x8 xa[10];
#pragma unroll
                for (int s = 0; s < 10; ++s) xa[s] = *(LAS bf16x8*)(L + L_LN + (320 * qr + 32 * s + 8 * g) * 2);
                lds_wait<true>(L, C_WQ, 1);
                const unsigned ca = L_W + (8 * g + tq) * 128 + ((a ^ ((tq >> 1) | ((g & 1) << 1))) * 2 + (tp >> 1)) * 16 + (tp & 1) * 8;
                f32x4 q0 = z4, q1 = z4;
#pragma unroll
                for (int s = 0; s < 10; s += 2) {
                    q0 = mfma32(xa[s], cat8(trd(L, ca + s * 4096), trd(L, ca + s * 4096 + 512)), q0);
                    q1 = mfma32(xa[s + 1], cat8(trd(L, ca + s * 4096 + 4096), trd(L, ca + s * 4096 + 4608)), q1);
                }
                lds_signal(C_QP);
                put1(rws, lane < 16 ? WS_QP + h * 3072 + qc * 1024 + qr * 512 + (16 * a + lane) * 8 : OOB, tag, __float_as_uint(q0[0] + q1[0]));
            }
            if (a == 3 && j == 3) {
                lds_wait<true>(L, C_LK, 1);
                putw(rws, lane == 0 ? WS_LK + xcd * 128 + (idx >> 3) * 4 : OOB, (tag & 0xFFFFF000u) | *(volatile LAS unsigned*)(L + L_CNT + 4 * C_LK));
                lds_wait<true>(L, C_LV, 1);
                putw(rws, lane == 0 ? WS_LK + 1024 + xcd * 128 + (idx >> 3) * 4 : OOB, (tag & 0xFFFFF000u) | *(volatile LAS unsigned*)(L + L_CNT + 4 * C_LV));
            }
            if (a == 1) {
                lds_wait<true>(L, C_LATE, 1);
                const u64 t0 = t_end() - TE;
#pragma unroll 1
                for (int p = 0; p < 2; ++p) {
                    until(t0 + (p ? TLV : TLK));
                    MEMBAR();
                    const v4u t = __builtin_amdgcn_raw_buffer_load_b128(rws, WS_LK + p * 1024 + (lane & 7) * 128, 0, 16);
                    float m = 0.f;
#pragma unroll
                    for (int k = 0; k < 4; ++k) m += (float)min(t[k] ^ (tag & 0xFFFFF000u), 400u);
                    m += dpp<XOR1>(m); m += dpp<XOR2>(m); m += dpp<XOR7>(m);
                    m *= 1.0f / 128.0f;
                    const float e = fminf(fmaxf((m - (float)(p ? LV1 : LK1)) * (float)(p ? LVS : LKS), 0.f), 70.f);
                    lds_set(p ? C_EX2 : C_EXT, (unsigned)(64 + (int)e));
                }
            }
            if (wocu) {
                if (a < 3) {
                    unsigned oa = L_O + (64 * a + 4 * i16 + g) * 2, wa = L_OWN + (64 * a + 4 * i16) * 4;
                    PIN(oa); PIN(wa);
                    const u64 mo = ~(0xFFFFull << (16 * (j & 3)));
                    lds_wait<true>(L, C_W, 1);
                    float w0 = *(LAS float*)(L + L_WGT + 4 * g), w1 = *(LAS float*)(L + L_WGT + 16 + 4 * g);
                    PIN(w0); PIN(w1);
                    lds_wait<true>(L, C_PROG, pv0 + 84);
                    const u64 t1 = t_end() + G1;
                    bf16x8 own[1];
                    spin_rd(C_OWN, 4, wa, own);
                    nap(3);
                    until_hot(t1);
                    const float o = sum_words_w(rws, g * 768 + i16 * 16, WS_OP + h * 6144 + a * 256, tag, rc, __builtin_bit_cast(v4u, own[0]), j < 4 ? mo : ~0ull, j < 4 ? ~0ull : mo, w0, w1, 0);
                    *(LAS u16*)(L + oa) = (u16)bfbits(o);
                    lds_signal(C_O);
                } else {
                    lds_wait<true>(L, C_PROG, pv0 + 84);
                    note_late(h, t_end() + G2 - LATE_AP, 3);
                    if (gcu >= 0) {
                        until(t_end() + G2 + 45);
                        *(LAS float*)(L + L_LN + lane * 4) = sum_words2(rws, lane * 16, WS_CP + gcu * 2048, tag, rc);
                        MEMBAR();
                        if (lane == 0) *(LAS float*)(L + L_ST + 12) = 1.0f;
                    }
                }
            } else {
                make_h(a);
            }
        } else {
            const int kw = wave - 5;
            if (kw == 3) { until(t_start + 150); MEMBAR(); lds_set(C_LATE, lateness(rws, lane, t_start) + 128); }
            if (wave == 6) {
                const u16* p = j < 2 ? vc + ((size_t)h * SK + (SK - W) + j * (W - 1)) * DH : j < 4 ? wo + (size_t)(h * DH + (j - 2) * (DH - 1)) * D
                             : ((j & 1) ? wu : wg) + (size_t)(80 * xcd + (j >> 1 & 1) * 79) * F;
                if (lane == 0) asm volatile("s_mov_b32 m0, %0\n\ts_nop 0\n\tglobal_load_lds_ushort %1, off nt" :: "s"(L_JUNK + 256), "v"(p) : "memory");
            }
            bf16x8 kf[4][6];
            {
                const unsigned kb = L_V + (16 * kw + i16) * 384;
#pragma unroll
                for (int n = 0; n < 4; ++n) {
                    lds_wait<true>(L, C_PROG, 12 * ((kw >> 1) + 2 * n + 1));
#pragma unroll
                    for (int s = 0; s < 6; ++s) kf[n][s] = *(LAS bf16x8*)(L + kb + n * 24576 + ((4 * s + g) ^ (i16 >> 1)) * 16);
                    if (n & 1) lds_signal(n == 1 ? C_KA : C_KB);
                }
            }
            lds_wait<true>(L, C_Q, 1);
            tag = __builtin_amdgcn_readfirstlane(*(LAS unsigned*)(L + L_TAG));
            f32x4 sa[4] = {z4, z4, z4, z4};
#pragma unroll
            for (int s = 0; s < 6; ++s) {
                const bf16x8 qf = *(LAS bf16x8*)(L + L_Q + (32 * s + 8 * g) * 2);
#pragma unroll
                for (int n = 0; n < 4; ++n) sa[n] = mfma32(qf, kf[n][s], sa[n]);
            }
            const float raw = sel4(sa[0][0], sa[1][0], sa[2][0], sa[3][0]);
            const float s = rbf(rbf(raw) * 0.07216878364870322f);
            const float mw = wave_max(s);
            if (lane == 0) *(LAS float*)(L + L_MX + kw * 4) = mw;
            lds_signal(C_MAX);
            lds_wait<false>(L, C_MAX, 4);
            const f32x4 mx = *(LAS f32x4*)(L + L_MX);
            const float m = fmaxf(fmaxf(mx[0], mx[1]), fmaxf(mx[2], mx[3]));
            const float e = __expf(s - m);
            *(LAS u16*)(L + L_P + (16 * (kw + 4 * g) + i16) * 2) = (u16)bfbits(e);
            const float lw = wave_sum(e);
            if (lane == 0) *(LAS float*)(L + L_MX + 16 + kw * 4) = lw;
            lds_signal(C_P);
            lds_wait<false>(L, C_P, 4);
            bf16x8 pf[8];
#pragma unroll
            for (int s8 = 0; s8 < 8; ++s8) pf[s8] = *(LAS bf16x8*)(L + L_P + (32 * s8 + 8 * g) * 2);
            const int fvs = (tq >> 1) | ((g & 1) << 1);
            unsigned vb[3], vl[3];
#pragma unroll
            for (int t = 0; t < 3; ++t) { vb[t] = (8 * g + tq) * 384 + (((3 * kw + t) ^ fvs) * 2 + (tp >> 1)) * 16 + (tp & 1) * 8; vl[t] = L_V + 6 * 12288 + vb[t]; PIN(vl[t]); }
            unsigned oo = lane < 48 ? WS_OP + ((h * 8 + j) * 192 + 48 * kw + lane) * 4 : OOB, ow = L_OWN + (lane < 48 ? 48 * kw + lane : 192 + lane) * 4;
            unsigned tb = (tag >> (8 * tp)) & 255u;
            PIN(tb);
            PIN(oo); PIN(ow);
            f32x4 oa[3] = {z4, z4, z4};
            lds_wait<true>(L, C_PROG, pv0 + 72);
#pragma unroll
            for (int s8 = 0; s8 < 6; ++s8)
#pragma unroll
                for (int t = 0; t < 3; ++t) oa[t] = mfma32(pf[s8], cat8(trd(L, L_V + vb[t] + s8 * 12288), trd(L, L_V + vb[t] + s8 * 12288 + 1536)), oa[t]);
            {
                s16x4 vf[6];
                spin_tr<0>(C_PROG, pv0 + 84, vl, vf);
#pragma unroll
                for (int t = 0; t < 3; ++t) oa[t] = mfma32(pf[6], cat8(vf[2 * t], vf[2 * t + 1]), oa[t]);
                spin_tr<12288>(C_PROG, pv0 + 96, vl, vf);
#pragma unroll
                for (int t = 0; t < 3; ++t) oa[t] = mfma32(pf[7], cat8(vf[2 * t], vf[2 * t + 1]), oa[t]);
            }
            const unsigned op = enc(sel4(oa[0][0], oa[1][0], oa[2][0], oa[2][0]), tb);
            putw(rws, oo, op);
            *(LAS unsigned*)(L + ow) = op;
            lds_signal(C_OWN);
            if (wocu) {
                unsigned ao = lane < 32 ? WS_AP + (((8 * j + 2 * kw + g) * 32 + h) * 16 + i16) * 4 : OOB, al = L_O + 16 * g;
                PIN(ao); PIN(al);
                lds_wait<false>(L, C_PROG, P_WO);
                bf16x8 bw[2][6], af[6];
                const int f3 = tq | ((g & 1) << 2);
#pragma unroll
                for (int t = 0; t < 2; ++t) {
                    const unsigned cb = L_W + (8 * g + tq) * 256 + (((2 * kw + t) ^ f3) * 2 + (tp >> 1)) * 16 + (tp & 1) * 8;
#pragma unroll
                    for (int s6 = 0; s6 < 6; ++s6) bw[t][s6] = cat8(trd(L, cb + s6 * 8192), trd(L, cb + s6 * 8192 + 1024));
                }
                spin_rd(C_O, 3, al, af);
                f32x4 a0 = z4, a1 = z4;
#pragma unroll
                for (int s6 = 0; s6 < 6; ++s6) { a0 = mfma32(af[s6], bw[0][s6], a0); a1 = mfma32(af[s6], bw[1][s6], a1); }
                putw(rws, ao, enc((lane & 16) ? a1[0] : a0[0], tb));
            } else {
                *(LAS unsigned*)(L + L_O + 160 + (lane & 7) * 4) = 0u;
                lds_wait<true>(L, C_PROG, P_WO - 24);
                lds_wait<false>(L, C_PROG, P_WO);
                bf16x8 bw[2][2][3], af[3];
                const int f3 = tq | ((g & 1) << 2);
#pragma unroll
                for (int mt = 0; mt < 2; ++mt)
#pragma unroll
                    for (int t = 0; t < 2; ++t) {
                        const unsigned cb = L_W + mt * 24576 + (8 * g + tq) * 256 + ((2 * kw + t) ^ f3) * 32 + tp * 8;
#pragma unroll
                        for (int s3 = 0; s3 < 3; ++s3) bw[mt][t][s3] = cat8(trd(L, cb + s3 * 8192), trd(L, cb + s3 * 8192 + 1024));
                    }
                unsigned go = WS_CP + ((4 * oc + kw) * 8 + xcd) * 256 + lane * 4, g2 = go + (WS_GP - WS_CP), al = L_O + 16 * g;
                PIN(al); PIN(g2);
                auto tile4 = [&](unsigned off) {
                    f32x4 d[2][2] = {{z4, z4}, {z4, z4}};
#pragma unroll
                    for (int s3 = 0; s3 < 3; ++s3)
#pragma unroll
                        for (int mt = 0; mt < 2; ++mt)
#pragma unroll
                            for (int t = 0; t < 2; ++t) d[mt][t] = mfma32(af[s3], bw[mt][t][s3], d[mt][t]);
                    SB();
                    putw(rws, off, enc(sel4(d[0][0][0], d[0][1][0], d[1][0][0], d[1][1][0]), tb));
                };
                const v4u ones = {0x3F803F80u, 0x3F803F80u, 0x3F803F80u, 0x3F803F80u};
                af[0] = af[1] = af[2] = asb(ones);
                tile4(go);
                spin_rd(C_O, 5, al, af);
                tile4(g2);
            }
        }
        if (gcu >= 0) {
            const int w8 = wave - 1;
            unsigned d0 = WS_DP + (((5 * w8 + g) * 48 + gcu) * 16 + i16) * 4, d1 = lane < 16 ? WS_DP + (((5 * w8 + 4) * 48 + gcu) * 16 + i16) * 4 : OOB, al = L_HH + 16 * g;
            unsigned tb = (tag >> (8 * tp)) & 255u;
            PIN(tb);
            PIN(d0); PIN(d1); PIN(al);
            bf16x8 af[1];
            lds_wait<true>(L, C_GO, 1);
            spin_rd(C_HH, 1, al, af);
            f32x4 d[5];
#pragma unroll
            for (int t = 0; t < 5; ++t) d[t] = mfma32(af[0], dw[t], z4);
            SB();
            putw(rws, d0, enc(sel4(d[0][0], d[1][0], d[2][0], d[3][0]), tb));
            putw(rws, d1, enc(d[4][0], tb));
        }
    }
}

extern "C" void kernel_launch(void* const* d_in, const int* in_sizes, int n_in, void* d_out, int out_size, void* d_ws, size_t ws_size, hipStream_t stream) {
    (void)hipFuncSetAttribute((const void*)k_block, hipFuncAttributeMaxDynamicSharedMemorySize, L_END);
    k_block<<<256, 640, L_END, stream>>>((const u16*)d_in[0], (const u16*)d_in[1], (const u16*)d_in[2], (const u16*)d_in[3], (const u16*)d_in[4],
                                         (const u16*)d_in[6], (const u16*)d_in[7], (const u16*)d_in[8], (u16*)d_out, (unsigned char*)d_ws
        );
}
```
